```python
import torch

def _softmax(x, axis=-1):
    return torch.softmax(x, dim=axis)
def _gelu(x):
    return torch.nn.functional.gelu(x, approximate="tanh")
def _silu(x):
    return torch.nn.functional.silu(x)
def _sigmoid(x):
    return torch.sigmoid(x)
def _layernorm(x, axis=-1, eps=1e-5):
    m = x.mean(dim=axis, keepdim=True)
    v = x.var(dim=axis, keepdim=True, unbiased=False)
    return (x - m) / torch.sqrt(v + eps)
def _scatter_add(tgt, idx, src, axis=0):
    return tgt.clone().index_add_(axis, idx, src)
def _segment_sum(src, seg, n):
    out = torch.zeros((n,) + src.shape[1:], dtype=src.dtype)
    return out.index_add_(0, seg, src)
def _pad(x, pw):
    flat = tuple(v for pair in reversed(pw) for v in pair)
    return torch.nn.functional.pad(x, flat)

kernel_name = 'concat_heads__sliding_block__sliding_block__prompt'

def setup_inputs():

    _w = (lambda a: (lambda t: t.to(torch.bfloat16) if t.is_floating_point() else t)(torch.as_tensor(a)))
    def _A_setup():
        B, S, D = 1, 2048, 1024
        return {
            "x": _w((lambda *s: torch.randn(*s, dtype=torch.bfloat16) * 0.2)(B, S, D)),
            "wqkv": _w((lambda *s: torch.randn(*s, dtype=torch.bfloat16) * 0.2)(D, 3 * D)),
            "wo": _w((lambda *s: torch.randn(*s, dtype=torch.bfloat16) * 0.05)(D, D)),
        }
    _A_in = _A_setup()

    def _B_setup():
        B, S, D = 1, 2048, 1024
        return {
            "x": _w((lambda *s: torch.randn(*s, dtype=torch.bfloat16) * 0.2)(B, S, D)),
            "wqkv": _w((lambda *s: torch.randn(*s, dtype=torch.bfloat16) * 0.2)(D, 3 * D)),
            "wo": _w((lambda *s: torch.randn(*s, dtype=torch.bfloat16) * 0.05)(D, D)),
        }
    _B_in = _B_setup()

    return {
        "A_x": _A_in["x"],
        "A_wqkv": _A_in["wqkv"],
        "A_wo": _A_in["wo"],
        "B_x": _B_in["x"],
        "B_wqkv": _B_in["wqkv"],
        "B_wo": _B_in["wo"],
    }

def reference(A_x, A_wqkv, A_wo, B_x, B_wqkv, B_wo):
    def _A_ref(x, wqkv, wo):
        B, S, D = x.shape
        H = 32; Dh = D // H; W = 256
        qkv = (lambda x, *d: x.permute(*d))((x @ wqkv).reshape(B, S, 3, H, Dh), 2, 0, 3, 1, 4)
        q, k, v = qkv[0], qkv[1], qkv[2]
        scores = torch.einsum("bhqd,bhkd->bhqk", q, k) * (Dh ** -0.5)
        idx = torch.arange(S)
        mask = (idx[:, None] - idx[None, :] >= 0) & (idx[:, None] - idx[None, :] < W)
        scores = torch.where(mask[None, None], scores, -1e9)
        p = _softmax(scores, axis=-1)
        o = torch.einsum("bhqk,bhkd->bhqd", p, v)
        return (lambda x, *d: x.permute(*d))(o, 0, 2, 1, 3).reshape(B, S, D) @ wo

    def _B_ref(x, wqkv, wo):
        B, S, D = x.shape
        H = 32; Dh = D // H; W = 256
        qkv = (lambda x, *d: x.permute(*d))((x @ wqkv).reshape(B, S, 3, H, Dh), 2, 0, 3, 1, 4)
        q, k, v = qkv[0], qkv[1], qkv[2]
        scores = torch.einsum("bhqd,bhkd->bhqk", q, k) * (Dh ** -0.5)
        idx = torch.arange(S)
        mask = (idx[:, None] - idx[None, :] >= 0) & (idx[:, None] - idx[None, :] < W)
        scores = torch.where(mask[None, None], scores, -1e9)
        p = _softmax(scores, axis=-1)
        o = torch.einsum("bhqk,bhkd->bhqd", p, v)
        return (lambda x, *d: x.permute(*d))(o, 0, 2, 1, 3).reshape(B, S, D) @ wo

    a = _A_ref(A_x, A_wqkv, A_wo)
    b = _B_ref(B_x, B_wqkv, B_wo)
    return torch.concatenate([a, b], axis=-1)
```

```cpp
#include <hip/hip_runtime.h>
#include <hip/hip_bf16.h>

typedef short bf16x8 __attribute__((ext_vector_type(8)));
typedef short s16x4 __attribute__((ext_vector_type(4)));
typedef float f32x16 __attribute__((ext_vector_type(16)));
typedef unsigned u32x4 __attribute__((ext_vector_type(4)));
typedef unsigned u32x2 __attribute__((ext_vector_type(2)));
typedef __bf16 bf16x2_t __attribute__((ext_vector_type(2)));
typedef float f32x2 __attribute__((ext_vector_type(2)));
#define LAS __attribute__((address_space(3)))
#define DI __device__ __forceinline__
#define MFMA(a, b, c) __builtin_amdgcn_mfma_f32_32x32x16_bf16((a), (b), (c), 0, 0, 0)
#define SBAR() __builtin_amdgcn_sched_barrier(0)
#define SGB(m, n) __builtin_amdgcn_sched_group_barrier((m), (n), 0)

constexpr int S_ = 2048, D_ = 1024;
constexpr float QSCALE = 0.17677669529663687f * 1.4426950408889634f;
constexpr float THR = 8.0f;

constexpr unsigned IMG = 2048;
constexpr unsigned SZ_T = 2u * 32u * 64u * IMG;
constexpr unsigned WS_Q = 0, WS_K = SZ_T, WS_V = 2 * SZ_T, WS_O = 3 * SZ_T, WS_END = 4 * SZ_T;

DI unsigned rfl(unsigned x) { return (unsigned)__builtin_amdgcn_readfirstlane((int)x); }
DI unsigned pk2(float lo, float hi) {
  f32x2 v = {lo, hi};
  return __builtin_bit_cast(unsigned, __builtin_convertvector(v, bf16x2_t));
}
DI u32x4 pack8(const f32x16& x, int s) {
  u32x4 p;
  p[0] = pk2(x[8 * s + 0], x[8 * s + 1]); p[1] = pk2(x[8 * s + 2], x[8 * s + 3]);
  p[2] = pk2(x[8 * s + 4], x[8 * s + 5]); p[3] = pk2(x[8 * s + 6], x[8 * s + 7]);
  return p;
}
DI bf16x8 asb(u32x4 v) { return __builtin_bit_cast(bf16x8, v); }
DI bf16x8 lds128(unsigned a) { return *(const LAS bf16x8*)(size_t)a; }
DI s16x4 ldstr(unsigned a) { return __builtin_amdgcn_ds_read_tr16_b64_v4i16((LAS s16x4*)(size_t)a); }
DI bf16x8 cat(s16x4 lo, s16x4 hi) { return __builtin_shufflevector(lo, hi, 0, 1, 2, 3, 4, 5, 6, 7); }
DI void dma16(unsigned ldsdst, unsigned voff, __amdgpu_buffer_rsrc_t rs, unsigned soff) {
  asm volatile("s_mov_b32 m0, %0\n\ts_nop 0\n\tbuffer_load_dwordx4 %1, %2, %3 offen lds" ::"s"(ldsdst), "v"(voff), "s"(rs), "s"(soff) : "memory");
}
#define WAITV(n) asm volatile("s_waitcnt vmcnt(" #n ")" ::: "memory")
#define WAIT_BAR(n) asm volatile("s_waitcnt vmcnt(" #n ") lgkmcnt(0)\n\ts_barrier" ::: "memory")
DI __amdgpu_buffer_rsrc_t mkrs(const void* p, unsigned bytes) { return __builtin_amdgcn_make_buffer_rsrc((void*)p, (short)0, (int)bytes, 0x00020000); }

constexpr unsigned XST = 32768, WST = 24576, LDS_X = 0, LDS_W = 3 * XST;

struct Frag { bf16x8 x0, x1; s16x4 wl[3], wh[3]; };

__global__ __launch_bounds__(512, 2) void k_qkv(const void* xA, const void* wA, const void* xB, const void* wB, void* ws) {
  __shared__ __attribute__((aligned(1024))) char smem[LDS_W + 2 * WST];
  const unsigned lds0 = (unsigned)(size_t)(LAS char*)smem;
  const unsigned bid = blockIdx.x, xcd = bid & 7, idx = bid >> 3;
  const unsigned br = xcd >> 2, R = ((xcd >> 1) & 1) * 4 + (idx & 3), hp = (xcd & 1) * 8 + (idx >> 2);
  const unsigned tid = threadIdx.x, l = tid & 63, w = rfl(tid >> 6), wm = w & 3, wn = w >> 2;
  const __amdgpu_buffer_rsrc_t rx = mkrs(br ? xB : xA, S_ * D_ * 2), rw = mkrs(br ? wB : wA, D_ * 3 * D_ * 2);
  const unsigned mtw = (w & 1) ? 7 - (w >> 1) : (w >> 1);
  const unsigned xv0 = (R * 256 + mtw * 32 + (l >> 3)) * 2048 + (((l & 7) ^ (l >> 4)) << 4);
  const unsigned xv1 = (R * 256 + mtw * 32 + 8 + (l >> 3)) * 2048 + (((l & 7) ^ (4 + (l >> 4))) << 4);
  const unsigned wv = (8 * w + (l >> 3)) * 6144 + (((l & 7) ^ (((l >> 4) & 1) << 2)) << 4) + hp * 128;
  const unsigned xdst = lds0 + LDS_X + w * 4096, wdst = lds0 + LDS_W + w * 1024;
#define DMA_X(t, st) do { const unsigned so_ = (t) * 128u, d_ = xdst + (st); \
    dma16(d_, xv0, rx, so_); dma16(d_ + 1024, xv1, rx, so_); dma16(d_ + 2048, xv0, rx, so_ + 32768); dma16(d_ + 3072, xv1, rx, so_ + 32768); } while (0)
#define DMA_W(t, st) do { const unsigned so_ = (t) * (64u * 6144u), d_ = wdst + (st); \
    dma16(d_, wv, rw, so_); dma16(d_ + 8192, wv, rw, so_ + 2048); dma16(d_ + 16384, wv, rw, so_ + 4096); } while (0)
  DMA_W(0, 0); DMA_X(0, 0); DMA_W(1, WST); DMA_X(1, XST);
  const unsigned r = l & 31, h = l >> 5;
  const unsigned xa = lds0 + LDS_X + wm * 8192 + r * 128 + ((h ^ ((r >> 1) & 7)) << 4);
  const unsigned q4 = (l & 15) >> 2;
  const unsigned wa = lds0 + LDS_W + (8 * h + q4) * 128 + ((wn ^ (q4 >> 1)) << 6) + ((l >> 4) & 1) * 32 + (l & 3) * 8;
  f32x16 aq0 = {}, aq1 = {}, ak0 = {}, ak1 = {}, av0 = {}, av1 = {};
  Frag A, B;
#define PIN(x) asm volatile("" : "+v"(x))
#define LOADF(F, xb, wb, s) do { F.x0 = lds128(((xb) ^ ((s) << 5))); F.x1 = lds128(((xb) ^ ((s) << 5)) + 4096); \
    _Pragma("unroll") for (int c_ = 0; c_ < 3; ++c_) { F.wl[c_] = ldstr((wb) + c_ * 8192 + (s) * 2048); F.wh[c_] = ldstr((wb) + c_ * 8192 + (s) * 2048 + 512); } } while (0)
#define RDW(F, wb, s, c) do { F.wl[c] = ldstr((wb) + (c) * 8192 + (s) * 2048); F.wh[c] = ldstr((wb) + (c) * 8192 + (s) * 2048 + 512); } while (0)
#define STEP(C, N, LD, xb, wb, s, D0, D1, D2, D3, D4, D5) do { \
    const bf16x8 wq_ = cat(C.wl[0], C.wh[0]), wk_ = cat(C.wl[1], C.wh[1]), wv_ = cat(C.wl[2], C.wh[2]); \
    aq0 = MFMA(wq_, C.x0, aq0); PIN(aq0); if (LD) { N.x0 = lds128(((xb) ^ ((s) << 5))); N.x1 = lds128(((xb) ^ ((s) << 5)) + 4096); } D0; SBAR(); \
    aq1 = MFMA(wq_, C.x1, aq1); PIN(aq1); if (LD) RDW(N, wb, s, 0); D1; SBAR(); \
    ak0 = MFMA(wk_, C.x0, ak0); PIN(ak0); if (LD) RDW(N, wb, s, 1); D2; SBAR(); \
    ak1 = MFMA(wk_, C.x1, ak1); PIN(ak1); if (LD) RDW(N, wb, s, 2); D3; SBAR(); \
    av0 = MFMA(C.x0, wv_, av0); PIN(av0); D4; SBAR(); \
    av1 = MFMA(C.x1, wv_, av1); PIN(av1); D5; SBAR(); } while (0)
#define NOP_ do {} while (0)
  WAIT_BAR(7);
  unsigned xs = 0, wsx = 0, xp = 2 * XST;
  LOADF(A, xa, wa, 0); SBAR();
#pragma unroll 1
  for (int t = 0; t < 15; ++t) {
    const unsigned xb = xa + xs, wb = wa + wsx;
    const unsigned xso = t < 14 ? (t + 2) * 128u : 0x80000000u, xd = xdst + xp;
    STEP(A, B, true, xb, wb, 1, NOP_, dma16(xd, xv0, rx, xso), NOP_, dma16(xd + 1024, xv1, rx, xso), NOP_, dma16(xd + 2048, xv0, rx, xso + 32768));
    STEP(B, A, true, xb, wb, 2, NOP_, dma16(xd + 3072, xv1, rx, xso + 32768), NOP_, NOP_, NOP_, NOP_);
    STEP(A, B, true, xb, wb, 3, NOP_, NOP_, NOP_, NOP_, NOP_, NOP_);
    WAIT_BAR(4);
    const unsigned wso = t < 14 ? (t + 2) * (64u * 6144u) : 0x80000000u, wd = wdst + wsx;
    xp = xs; xs = (xs == 2 * XST) ? 0 : xs + XST; wsx ^= WST;
    STEP(B, A, true, xa + xs, wa + wsx, 0, NOP_, dma16(wd, wv, rw, wso), NOP_, dma16(wd + 8192, wv, rw, wso + 2048), NOP_, dma16(wd + 16384, wv, rw, wso + 4096));
  }
  { const unsigned xb = xa + xs, wb = wa + wsx;
    STEP(A, B, true, xb, wb, 1, NOP_, NOP_, NOP_, NOP_, NOP_, NOP_);
    STEP(B, A, true, xb, wb, 2, NOP_, NOP_, NOP_, NOP_, NOP_, NOP_);
    STEP(A, B, true, xb, wb, 3, NOP_, NOP_, NOP_, NOP_, NOP_, NOP_);
    STEP(B, A, false, xb, wb, 0, NOP_, NOP_, NOP_, NOP_, NOP_, NOP_); }
  const __amdgpu_buffer_rsrc_t rws = mkrs(ws, WS_END);
  const unsigned hh = hp * 2 + wn;
  const unsigned T0 = R * 8 + wm, T1 = R * 8 + 7 - wm;
  const unsigned o0 = ((br * 32 + hh) * 64 + T0) * IMG + l * 16, o1 = ((br * 32 + hh) * 64 + T1) * IMG + l * 16;
  aq0 *= QSCALE; aq1 *= QSCALE;
#pragma unroll
  for (int s = 0; s < 2; ++s) {
    __builtin_amdgcn_raw_buffer_store_b128(pack8(aq0, s), rws, o0 + s * 1024, WS_Q, 0);
    __builtin_amdgcn_raw_buffer_store_b128(pack8(aq1, s), rws, o1 + s * 1024, WS_Q, 0);
    __builtin_amdgcn_raw_buffer_store_b128(pack8(ak0, s), rws, o0 + s * 1024, WS_K, 0);
    __builtin_amdgcn_raw_buffer_store_b128(pack8(ak1, s), rws, o1 + s * 1024, WS_K, 0);
    __builtin_amdgcn_raw_buffer_store_b128(pack8(av0, s), rws, o0 + s * 1024, WS_V, 0);
    __builtin_amdgcn_raw_buffer_store_b128(pack8(av1, s), rws, o1 + s * 1024, WS_V, 0);
  }
}

DI float swapmax(float m) {
  auto rr = __builtin_amdgcn_permlane32_swap(__float_as_uint(m), __float_as_uint(m), false, false);
  return __builtin_fmaxf(__uint_as_float(rr[0]), __uint_as_float(rr[1]));
}
DI float swapsum(float m) {
  auto rr = __builtin_amdgcn_permlane32_swap(__float_as_uint(m), __float_as_uint(m), false, false);
  return __uint_as_float(rr[0]) + __uint_as_float(rr[1]);
}
#define MX3(a, b, c) __builtin_fmaxf(__builtin_fmaxf((a), (b)), (c))
DI float max16(const f32x16& p) {
  float a = MX3(p[0], p[1], p[2]), b = MX3(p[3], p[4], p[5]);
  a = MX3(a, p[6], p[7]); b = MX3(b, p[8], p[9]); a = MX3(a, p[10], p[11]); b = MX3(b, p[12], p[13]);
  a = MX3(a, p[14], p[15]);
  return __builtin_fmaxf(a, b);
}
struct QState { bf16x8 q0, q1; f32x16 o; float m, l; };
#define SWAPQ() do { bf16x8 tq = A.q0; A.q0 = B.q0; B.q0 = tq; tq = A.q1; A.q1 = B.q1; B.q1 = tq; f32x16 to = A.o; A.o = B.o; B.o = to; float tf = A.m; A.m = B.m; B.m = tf; tf = A.l; A.l = B.l; B.l = tf; } while (0)
template <int MODE> DI void attn_tile(unsigned ka, unsigned va, QState& Q, f32x16& negm, int rr) {
  const bf16x8 k0 = lds128(ka), k1 = lds128(ka + 1024), v0 = lds128(va), v1 = lds128(va + 1024);
  f32x16 s;
  if (MODE == 1) { f32x16 z = {}; s = MFMA(k0, Q.q0, z); } else s = MFMA(k0, Q.q0, negm);
  s = MFMA(k1, Q.q1, s);
  if (MODE != 0) {
#pragma unroll
    for (int i = 0; i < 16; ++i) { const bool le = ((i & 3) + 8 * (i >> 2)) <= rr; s[i] = (le == (MODE == 1)) ? s[i] : -__builtin_inff(); }
  }
  float mx = max16(s);
  if (MODE == 1) {
    mx = swapmax(mx); Q.m = mx;
#pragma unroll
    for (int i = 0; i < 16; ++i) { negm[i] = -mx; s[i] -= mx; }
  } else if (__builtin_expect(__any(mx > THR), 0)) {
    mx = swapmax(mx);
    const float d = __builtin_fmaxf(mx, 0.f), f = __builtin_amdgcn_exp2f(-d);
    Q.m += d; Q.l *= f;
#pragma unroll
    for (int i = 0; i < 16; ++i) { negm[i] = -Q.m; s[i] -= d; Q.o[i] *= f; }
  }
#pragma unroll
  for (int i = 0; i < 16; ++i) s[i] = __builtin_amdgcn_exp2f(s[i]);
  float a = s[0] + s[1], b = s[2] + s[3];
#pragma unroll
  for (int i = 4; i < 16; i += 2) { a += s[i]; b += s[i + 1]; }
  if (MODE == 1) Q.l = a + b; else Q.l += a + b;
  const bf16x8 p0 = asb(pack8(s, 0)), p1 = asb(pack8(s, 1));
  if (MODE == 1) { f32x16 z = {}; Q.o = MFMA(v0, p0, z); } else Q.o = MFMA(v0, p0, Q.o);
  Q.o = MFMA(v1, p1, Q.o);
}

__global__ __launch_bounds__(512, 2) void k_attn(void* ws) {
  __shared__ __attribute__((aligned(1024))) char smem[131072];
  const unsigned lds0 = (unsigned)(size_t)(LAS char*)smem;
  const unsigned bid = blockIdx.x, xcd = bid & 7, idx = bid >> 3;
  const unsigned br = xcd >> 2, R = ((xcd >> 1) & 1) * 4 + (idx & 3), hp = (xcd & 1) * 8 + (idx >> 2);
  const unsigned tid = threadIdx.x, l = tid & 63, w = rfl(tid >> 6), wm = w & 3, wn = w >> 2;
  const __amdgpu_buffer_rsrc_t rws = mkrs(ws, WS_END);
  const unsigned hh = hp * 2 + wn;
  if (!(R == 0 && wm < 2)) {
    const unsigned src = ((br * 32 + hh) * 64 + R * 8 + wm * 4 - 8) * IMG, dst = lds0 + wn * 32768 + wm * 8192;
#pragma unroll
    for (int j = 0; j < 8; ++j) { dma16(dst + j * 1024, l * 16, rws, WS_K + src + j * 1024); dma16(dst + 65536 + j * 1024, l * 16, rws, WS_V + src + j * 1024); }
  }
  QState A, B;
  { const unsigned T0 = R * 8 + wm, T1 = R * 8 + 7 - wm;
    const unsigned o0 = ((br * 32 + hh) * 64 + T0) * IMG + l * 16, o1 = ((br * 32 + hh) * 64 + T1) * IMG + l * 16;
    A.q0 = asb(__builtin_amdgcn_raw_buffer_load_b128(rws, o0, WS_Q, 0)); A.q1 = asb(__builtin_amdgcn_raw_buffer_load_b128(rws, o0 + 1024, WS_Q, 0));
    B.q0 = asb(__builtin_amdgcn_raw_buffer_load_b128(rws, o1, WS_Q, 0)); B.q1 = asb(__builtin_amdgcn_raw_buffer_load_b128(rws, o1 + 1024, WS_Q, 0)); }
  WAIT_BAR(0);
  const int rr = (int)(l & 31) - 4 * (int)(l >> 5);
  const unsigned kb = lds0 + wn * 32768 + l * 16, vb = kb + 65536;
  f32x16 negm;
  A.o = f32x16{}; B.o = f32x16{}; A.m = B.m = A.l = B.l = 0.f;
#pragma unroll 1
  for (int qi = 0; qi < 2; ++qi) {
    const int a = qi ? 7 - wm : wm;
    attn_tile<1>(kb + (8 + a) * IMG, vb + (8 + a) * IMG, A, negm, rr);
#pragma unroll 1
    for (int j = 0; j < a; ++j) attn_tile<0>(kb + (8 + j) * IMG, vb + (8 + j) * IMG, A, negm, rr);
    SWAPQ();
  }
  if (R > 0) {
#pragma unroll 1
    for (int qi = 0; qi < 2; ++qi) {
      const int a = qi ? 7 - wm : wm;
#pragma unroll
      for (int i = 0; i < 16; ++i) negm[i] = -A.m;
      attn_tile<2>(kb + a * IMG, vb + a * IMG, A, negm, rr);
#pragma unroll 1
      for (int j = a + 1; j < 8; ++j) attn_tile<0>(kb + j * IMG, vb + j * IMG, A, negm, rr);
      SWAPQ();
    }
  }
  { const float ia = __builtin_amdgcn_rcpf(swapsum(A.l)), ib = __builtin_amdgcn_rcpf(swapsum(B.l));
    A.o *= ia; B.o *= ib;
    const unsigned T0 = R * 8 + wm, T1 = R * 8 + 7 - wm;
    const unsigned o0 = ((br * 64 + T0) * 32 + hh) * IMG + l * 16, o1 = ((br * 64 + T1) * 32 + hh) * IMG + l * 16;
#pragma unroll
    for (int s = 0; s < 2; ++s) {
      __builtin_amdgcn_raw_buffer_store_b128(pack8(A.o, s), rws, o0 + s * 1024, WS_O, 0);
      __builtin_amdgcn_raw_buffer_store_b128(pack8(B.o, s), rws, o1 + s * 1024, WS_O, 0);
    } }
}

__global__ __launch_bounds__(512, 2) void k_out(const void* woA, const void* woB, void* ws, void* out) {
  __shared__ __attribute__((aligned(1024))) char smem[131072];
  const unsigned lds0 = (unsigned)(size_t)(LAS char*)smem;
  const unsigned bid = blockIdx.x, xcd = bid & 7, idx = bid >> 3;
  const unsigned G = xcd * 2 + (idx >> 4), br = G >> 3, R = G & 7, ns = idx & 15;
  const unsigned tid = threadIdx.x, l = tid & 63, w = rfl(tid >> 6);
  const __amdgpu_buffer_rsrc_t rwo = mkrs(br ? woB : woA, D_ * D_ * 2), rws = mkrs(ws, WS_END), rout = mkrs(out, S_ * 2 * D_ * 2);
  const unsigned wv = (8 * w + (l >> 3)) * 2048 + (((l & 7) ^ (((l >> 4) & 1) << 2)) << 4) + ns * 128;
#pragma unroll
  for (int j = 0; j < 16; ++j) dma16(lds0 + j * 8192 + w * 1024, wv, rwo, j * 64 * 2048);
  const unsigned T = R * 8 + w;
  const unsigned ob = (br * 64 + T) * 32 * IMG + l * 16;
  const unsigned h = l >> 5, q4 = (l & 15) >> 2;
  const unsigned wa = lds0 + (4 * h + q4) * 128 + ((q4 >> 1) << 6) + ((l >> 4) & 1) * 32 + (l & 3) * 8;
  f32x16 c0 = {}, c1 = {};
  u32x4 of[8];
#pragma unroll
  for (int i = 0; i < 8; ++i) of[i] = __builtin_amdgcn_raw_buffer_load_b128(rws, ob + i * 1024, WS_O, 0);
  WAIT_BAR(0);
#pragma unroll 1
  for (int g = 0; g < 8; ++g) {
    u32x4 nf[8];
    const unsigned gn = (g < 7 ? g + 1 : g) * 8192;
#pragma unroll
    for (int i = 0; i < 8; ++i) nf[i] = __builtin_amdgcn_raw_buffer_load_b128(rws, ob + i * 1024, WS_O + gn, 0);
    const unsigned wb = wa + g * 16384;
#pragma unroll
    for (int i = 0; i < 8; ++i) {
      const bf16x8 w0 = cat(ldstr(wb + i * 2048), ldstr(wb + i * 2048 + 1024));
      const bf16x8 w1 = cat(ldstr((wb ^ 64) + i * 2048), ldstr((wb ^ 64) + i * 2048 + 1024));
      c0 = MFMA(w0, asb(of[i]), c0); c1 = MFMA(w1, asb(of[i]), c1);
    }
#pragma unroll
    for (int i = 0; i < 8; ++i) of[i] = nf[i];
  }
  const unsigned orow = (T * 32 + (l & 31)) * 4096 + br * 2048 + ns * 128 + h * 16;
#pragma unroll
  for (int nt = 0; nt < 2; ++nt) {
    const f32x16& c = nt ? c1 : c0;
#pragma unroll
    for (int g = 0; g < 4; g += 2) {
      unsigned ax = pk2(c[4 * g], c[4 * g + 1]), ay = pk2(c[4 * g + 2], c[4 * g + 3]);
      unsigned bx = pk2(c[4 * g + 4], c[4 * g + 5]), by = pk2(c[4 * g + 6], c[4 * g + 7]);
      auto r0 = __builtin_amdgcn_permlane32_swap(ax, bx, false, false);
      auto r1 = __builtin_amdgcn_permlane32_swap(ay, by, false, false);
      u32x4 v = {r0[0], r1[0], r0[1], r1[1]};
      __builtin_amdgcn_raw_buffer_store_b128(v, rout, orow + nt * 64 + g * 16, 0, 0);
    }
  }
}

extern "C" void kernel_launch(void* const* d_in, const int* in_sizes, int n_in, void* d_out, int out_size, void* d_ws, size_t ws_size,
                              hipStream_t stream) {
  k_qkv<<<256, 512, 0, stream>>>(d_in[0], d_in[1], d_in[3], d_in[4], d_ws);
  k_attn<<<256, 512, 0, stream>>>(d_ws);
  k_out<<<256, 512, 0, stream>>>(d_in[2], d_in[5], d_ws, d_out);
}
```

```cpp
#include <hip/hip_runtime.h>
#include <hip/hip_bf16.h>

typedef short bf16x8 __attribute__((ext_vector_type(8)));
typedef short s16x4 __attribute__((ext_vector_type(4)));
typedef float f32x16 __attribute__((ext_vector_type(16)));
typedef float f32x4 __attribute__((ext_vector_type(4)));
typedef unsigned u32x4 __attribute__((ext_vector_type(4)));
typedef __bf16 bf16x2_t __attribute__((ext_vector_type(2)));
typedef float f32x2 __attribute__((ext_vector_type(2)));
#define LAS __attribute__((address_space(3)))
#define DI __device__ __forceinline__
#define AI __attribute__((always_inline))
#define MFMA(a, b, c) __builtin_amdgcn_mfma_f32_32x32x16_bf16((a), (b), (c), 0, 0, 0)
#define SBAR() __builtin_amdgcn_sched_barrier(0)
#define PIN(x) asm volatile("" : "+v"(x))
#define MFA(acc_, a_, b_) asm volatile("v_mfma_f32_16x16x32_bf16 %0, %1, %2, %0" : "+v"(acc_) : "v"(a_), "v"(b_))

constexpr int S_ = 2048, D_ = 1024;
constexpr float QSCALE = 0.17677669529663687f * 1.4426950408889634f;
constexpr float THR = 8.0f;
constexpr unsigned IMG = 2048;
constexpr unsigned WS_FLAG = 0, WS_ACK = 32768, WS_KV = 65536, WS_O = WS_KV + 256u * 65536u, WS_END = WS_O + 2u * 64u * 32u * IMG;
constexpr unsigned ACKX = 0x5bd1e995u;
constexpr int SC1 = 16;
constexpr unsigned SPIN_CAP = 1u << 18;

template <int I> struct IC { static constexpr int v = I; };
template <int B, int E, class F> DI void sfor(F&& f) { if constexpr (B < E) { f(IC<B>{}); sfor<B + 1, E>(f); } }
DI unsigned rfl(unsigned x) { return (unsigned)__builtin_amdgcn_readfirstlane((int)x); }
DI unsigned pk2(float lo, float hi) { f32x2 v = {lo, hi}; return __builtin_bit_cast(unsigned, __builtin_convertvector(v, bf16x2_t)); }
DI u32x4 pack8(const f32x16& x, int s) {
  u32x4 p;
  p[0] = pk2(x[8 * s + 0], x[8 * s + 1]); p[1] = pk2(x[8 * s + 2], x[8 * s + 3]);
  p[2] = pk2(x[8 * s + 4], x[8 * s + 5]); p[3] = pk2(x[8 * s + 6], x[8 * s + 7]);
  return p;
}
DI u32x4 pack44(const f32x4& a, const f32x4& b) { u32x4 p; p[0] = pk2(a[0], a[1]); p[1] = pk2(a[2], a[3]); p[2] = pk2(b[0], b[1]); p[3] = pk2(b[2], b[3]); return p; }
DI bf16x8 asb(u32x4 v) { return __builtin_bit_cast(bf16x8, v); }
DI bf16x8 lds128(unsigned a) { return *(const LAS bf16x8*)(size_t)a; }
DI void sts128(unsigned a, u32x4 v) { *(LAS u32x4*)(size_t)a = v; }
DI s16x4 ldstr(unsigned a) { return __builtin_amdgcn_ds_read_tr16_b64_v4i16((LAS s16x4*)(size_t)a); }
DI bf16x8 cat(s16x4 lo, s16x4 hi) { return __builtin_shufflevector(lo, hi, 0, 1, 2, 3, 4, 5, 6, 7); }
DI void dma16(unsigned ldsdst, unsigned voff, __amdgpu_buffer_rsrc_t rs, unsigned soff) {
  asm volatile("s_mov_b32 m0, %0\n\ts_nop 0\n\tbuffer_load_dwordx4 %1, %2, %3 offen lds" ::"s"(ldsdst), "v"(voff), "s"(rs), "s"(soff) : "memory");
}
DI void dma16_sc1(unsigned ldsdst, unsigned voff, __amdgpu_buffer_rsrc_t rs, unsigned soff) {
  asm volatile("s_mov_b32 m0, %0\n\ts_nop 0\n\tbuffer_load_dwordx4 %1, %2, %3 offen sc1 lds" ::"s"(ldsdst), "v"(voff), "s"(rs), "s"(soff) : "memory");
}
#define WAITV(n) asm volatile("s_waitcnt vmcnt(" #n ")" ::: "memory")
#define WAIT_BAR(n) asm volatile("s_waitcnt vmcnt(" #n ") lgkmcnt(0)\n\ts_barrier" ::: "memory")
DI __amdgpu_buffer_rsrc_t mkrs(const void* p, unsigned bytes) { return __builtin_amdgcn_make_buffer_rsrc((void*)p, (short)0, (int)bytes, 0x00020000); }
DI unsigned ld32_issue(__amdgpu_buffer_rsrc_t rs, unsigned off) { unsigned r; asm volatile("buffer_load_dword %0, %1, %2, 0 offen sc1" : "=v"(r) : "v"(off), "s"(rs) : "memory"); return r; }
DI unsigned ld32_wait(__amdgpu_buffer_rsrc_t rs, unsigned off) { unsigned r; asm volatile("buffer_load_dword %0, %1, %2, 0 offen sc1\n\ts_waitcnt vmcnt(0)" : "=&v"(r) : "v"(off), "s"(rs) : "memory"); return r; }
DI void dma4_sc1(unsigned ldsdst, unsigned voff, __amdgpu_buffer_rsrc_t rs, unsigned soff) {
  asm volatile("s_mov_b32 m0, %0\n\ts_nop 0\n\tbuffer_load_dword %1, %2, %3 offen sc1 lds" ::"s"(ldsdst), "v"(voff), "s"(rs), "s"(soff) : "memory");
}
#define WAIT_BAR_V(n, x) asm volatile("s_waitcnt vmcnt(" #n ") lgkmcnt(0)\n\ts_barrier" : "+v"(x) :: "memory")
constexpr unsigned OOB = 0x80000000u;
DI void st32(unsigned v, __amdgpu_buffer_rsrc_t rs, unsigned off) { __builtin_amdgcn_raw_buffer_store_b32(v, rs, off, 0, SC1); }

#define STAMP(i) do {} while (0)

constexpr unsigned XST = 32768, WST = 24576, LDS_W = 3 * XST, LDS_SPARE = LDS_W + 2 * WST, LDS_CTL = 159744;
constexpr unsigned LDS_OWN = 32768, LDS_PREV = 98304, LDS_BYTES = 163840;

struct Frag { bf16x8 x[4]; s16x4 wl[6], wh[6]; };

DI float swapmax(float m) {
  auto rr = __builtin_amdgcn_permlane32_swap(__float_as_uint(m), __float_as_uint(m), false, false);
  return __builtin_fmaxf(__uint_as_float(rr[0]), __uint_as_float(rr[1]));
}
DI float swapsum(float m) {
  auto rr = __builtin_amdgcn_permlane32_swap(__float_as_uint(m), __float_as_uint(m), false, false);
  return __uint_as_float(rr[0]) + __uint_as_float(rr[1]);
}
#define MX3(a, b, c) __builtin_fmaxf(__builtin_fmaxf((a), (b)), (c))
DI float max16(const f32x16& p) {
  float a = MX3(p[0], p[1], p[2]), b = MX3(p[3], p[4], p[5]);
  a = MX3(a, p[6], p[7]); b = MX3(b, p[8], p[9]); a = MX3(a, p[10], p[11]); b = MX3(b, p[12], p[13]);
  a = MX3(a, p[14], p[15]);
  return __builtin_fmaxf(a, b);
}
struct QState { bf16x8 q0, q1; f32x16 o; float m, l; };
#define SWAPQ() do { bf16x8 tq = A.q0; A.q0 = B.q0; B.q0 = tq; tq = A.q1; A.q1 = B.q1; B.q1 = tq; f32x16 to = A.o; A.o = B.o; B.o = to; float tf = A.m; A.m = B.m; B.m = tf; tf = A.l; A.l = B.l; B.l = tf; } while (0)
template <int MODE> DI void attn_tile(unsigned ka, QState& Q, f32x16& negm, int rr) {
  const bf16x8 k0 = lds128(ka), k1 = lds128(ka + 1024), v0 = lds128(ka + 32768), v1 = lds128(ka + 32768 + 1024);
  f32x16 s;
  if (MODE == 1) { f32x16 z = {}; s = MFMA(k0, Q.q0, z); } else s = MFMA(k0, Q.q0, negm);
  s = MFMA(k1, Q.q1, s);
  if (MODE != 0) {
#pragma unroll
    for (int i = 0; i < 16; ++i) { const bool le = ((i & 3) + 16 * ((i >> 2) & 1) + 8 * (i >> 3)) <= rr; s[i] = (le == (MODE == 1)) ? s[i] : -__builtin_inff(); }
  }
  float mx = max16(s);
  if (MODE == 1) {
    mx = swapmax(mx); Q.m = mx;
#pragma unroll
    for (int i = 0; i < 16; ++i) { negm[i] = -mx; s[i] -= mx; }
  } else if (__builtin_expect(__any(mx > THR), 0)) {
    mx = swapmax(mx);
    const float d = __builtin_fmaxf(mx, 0.f), f = __builtin_amdgcn_exp2f(-d);
    Q.m += d; Q.l *= f;
#pragma unroll
    for (int i = 0; i < 16; ++i) { negm[i] = -Q.m; s[i] -= d; Q.o[i] *= f; }
  }
#pragma unroll
  for (int i = 0; i < 16; ++i) s[i] = __builtin_amdgcn_exp2f(s[i]);
  float a = s[0] + s[1], b = s[2] + s[3];
#pragma unroll
  for (int i = 4; i < 16; i += 2) { a += s[i]; b += s[i + 1]; }
  if (MODE == 1) Q.l = a + b; else Q.l += a + b;
  const bf16x8 p0 = asb(pack8(s, 0)), p1 = asb(pack8(s, 1));
  if (MODE == 1) { f32x16 z = {}; Q.o = MFMA(v0, p0, z); } else Q.o = MFMA(v0, p0, Q.o);
  Q.o = MFMA(v1, p1, Q.o);
}

__global__ __launch_bounds__(512, 2) void k_fused(const void* xA, const void* wA, const void* woA, const void* xB, const void* wB, const void* woB,
                                                  void* ws, void* out) {
  __shared__ __attribute__((aligned(1024))) char smem[LDS_BYTES];
  const unsigned lds0 = (unsigned)(size_t)(LAS char*)smem;
  const unsigned bid = blockIdx.x, xcd = bid & 7, idx = bid >> 3;
  const unsigned br = xcd >> 2, R = ((xcd >> 1) & 1) * 4 + (idx & 3), hp = (xcd & 1) * 8 + (idx >> 2);
  const unsigned L = (br * 8 + R) * 16 + hp;
  const unsigned G = xcd * 2 + (idx >> 4), ns = idx & 15;
  const unsigned tid = threadIdx.x, l = tid & 63, w = rfl(tid >> 6), wm = w & 3, wn = w >> 2;
  const __amdgpu_buffer_rsrc_t rx = mkrs(br ? xB : xA, S_ * D_ * 2), rw = mkrs(br ? wB : wA, D_ * 3 * D_ * 2), rws = mkrs(ws, WS_END);
  STAMP(0);
  const unsigned fl_id = l < 32 ? G * 16 + (l >> 1) : (l == 32 ? L - 16 : L);
  const bool fl_ok = l < 34 && !(l == 32 && R == 0);
  unsigned obase = ld32_issue(rws, fl_ok ? WS_FLAG + fl_id * 128 : OOB);
  const unsigned mtw = (w & 1) ? 7 - (w >> 1) : (w >> 1);
  const unsigned xv0 = (R * 256 + mtw * 32 + (l >> 3)) * 2048 + (((l & 7) ^ (l >> 4)) << 4);
  const unsigned xv1 = (R * 256 + mtw * 32 + 8 + (l >> 3)) * 2048 + (((l & 7) ^ (4 + (l >> 4))) << 4);
  const unsigned wv = (8 * w + (l >> 3)) * 6144 + (((l & 7) ^ ((((l >> 4) & 1) | ((w & 1) << 1)) << 1)) << 4) + hp * 128;
  const unsigned xdst = lds0 + w * 4096, wdst = lds0 + LDS_W + w * 1024;
#define DMA_X(t, st) do { const unsigned so_ = (t) * 128u, d_ = xdst + (st); \
    dma16(d_, xv0, rx, so_); dma16(d_ + 1024, xv1, rx, so_); dma16(d_ + 2048, xv0, rx, so_ + 32768); dma16(d_ + 3072, xv1, rx, so_ + 32768); } while (0)
#define DMA_W(t, st) do { const unsigned so_ = (t) * (64u * 6144u), d_ = wdst + (st); \
    dma16(d_, wv, rw, so_); dma16(d_ + 8192, wv, rw, so_ + 2048); dma16(d_ + 16384, wv, rw, so_ + 4096); } while (0)
  DMA_W(0, 0); DMA_X(0, 0); DMA_W(1, WST); DMA_X(1, XST);
  const unsigned c = l & 15, g = l >> 4, q4 = c >> 2;
  const unsigned xa = lds0 + wm * 8192 + c * 128 + ((g ^ (c >> 1)) << 4);
  const unsigned wa = lds0 + LDS_W + (8 * g + q4) * 128 + (((2 * wn) ^ ((q4 >> 1) | ((g & 1) << 1))) << 5) + (l & 3) * 8;
  f32x4 acc[6][4] = {};
  Frag A, B;
#define RDX(F, xb, s, u) F.x[u] = lds128(((xb) ^ ((s) << 6)) + (u) * 2048)
#define MM(C, n, u) do { const bf16x8 wf_ = cat(C.wl[n], C.wh[n]); if ((n) < 4) MFA(acc[n][u], wf_, C.x[u]); else MFA(acc[n][u], C.x[u], wf_); } while (0)
#define RDWL(F, wb, s, n) F.wl[n] = ldstr(((wb) ^ (((n) & 1) << 5)) + ((n) >> 1) * 8192 + (s) * 4096)
#define RDWH(F, wb, s, n) F.wh[n] = ldstr(((wb) ^ (((n) & 1) << 5)) + ((n) >> 1) * 8192 + (s) * 4096 + 512)
#define RD1(F, xb, wb, s, r) do { if ((r) == 0) RDX(F, xb, s, 0); else if ((r) == 1) RDWL(F, wb, s, 0); else if ((r) == 2) RDWH(F, wb, s, 0); \
    else if ((r) < 6) RDX(F, xb, s, ((r) - 2) & 3); else if (((r) & 1) == 0) RDWL(F, wb, s, (((r) + 8) >> 1) % 6); else RDWH(F, wb, s, (((r) + 8) >> 1) % 6); } while (0)
#define PB 27
#define BP(r) ((r) * 3 / 2)
#define AP(r) (29 + (r) * 6 / 5)
#define XP(j) (2 + (j) * 6)
#define WP(j) (28 + (j) * 3)
#define TILE(LDA, DMA, WAITB) do { \
    const unsigned xb = xa + xs, wb = wa + wsx, wd = wdst + wsx; \
    xp = xs; xs = (xs == 2 * XST) ? 0 : xs + XST; wsx ^= WST; \
    const unsigned xn = xa + xs, wn_ = wa + wsx; \
    sfor<0, 48>([&](auto mi) AI { constexpr int m_ = decltype(mi)::v; \
      if constexpr (m_ < 24) MM(A, m_ >> 2, m_ & 3); else MM(B, (m_ - 24) >> 2, m_ & 3); \
      sfor<0, 16>([&](auto ri) AI { constexpr int r_ = decltype(ri)::v; \
        if constexpr (BP(r_) == m_) RD1(B, xb, wb, 1, r_); if constexpr (LDA && AP(r_) == m_) RD1(A, xn, wn_, 0, r_); }); \
      sfor<0, 4>([&](auto ji) AI { constexpr int j_ = decltype(ji)::v; \
        if constexpr (DMA && XP(j_) == m_) dma16(xd + j_ * 1024, (j_ & 1) ? xv1 : xv0, rx, xso + (j_ >> 1) * 32768); \
        if constexpr (DMA && j_ < 3 && WP(j_) == m_) dma16(wd + j_ * 8192, wv, rw, wso + j_ * 2048); }); \
      SBAR(); \
      if constexpr (m_ == PB) { WAITB; SBAR(); } }); } while (0)
  WAIT_BAR_V(7, obase);
  STAMP(1);
  unsigned xs = 0, wsx = 0, xp = 2 * XST;
  sfor<0, 16>([&](auto ri) AI { constexpr int r_ = decltype(ri)::v; RD1(A, xa, wa, 0, r_); SBAR(); });
  if (w == 0) st32(obase ^ ACKX, rws, (fl_ok && l <= 32 && !(l & 1)) ? WS_ACK + fl_id * 128 + (l < 32 ? ns : 16) * 4 : OOB);
  const unsigned pbase = __builtin_amdgcn_readlane(obase, 32), mybase = __builtin_amdgcn_readlane(obase, 33);
  _Pragma("unroll") for (int i_ = 0; i_ < 24; ++i_) PIN(acc[i_ >> 2][i_ & 3]);
  asm volatile("s_nop 1" ::: "memory");
  SBAR();
#pragma unroll 1
  for (int t = 0; t < 15; ++t) {
    const unsigned xso = t < 14 ? (t + 2) * 128u : 0x80000000u, wso = t < 14 ? (t + 2) * (64u * 6144u) : OOB;
    const unsigned xd = t < 14 ? xdst + xp : lds0 + LDS_SPARE;
    TILE(true, true, WAIT_BAR(4));
  }
  const bool ack_ok = l < 17 && !(l == 16 && R == 7);
  const unsigned ackoff = ack_ok ? WS_ACK + L * 128 + l * 4 : OOB;
  unsigned ackv = ld32_issue(rws, ackoff);
  { const unsigned xso = 0, wso = 0, xd = 0; TILE(false, false, (void)0); }
  asm volatile("s_nop 7" ::: "memory");
  _Pragma("unroll") for (int i_ = 0; i_ < 24; ++i_) PIN(acc[i_ >> 2][i_ & 3]);
  STAMP(2);
  QState QA, QB;
  {
    const unsigned ko = (g & 1) * 1024 + ((g >> 1) * 32 + (c & 7) + 16 * (c >> 3)) * 16;
    const unsigned vo = 32768 + (g >> 1) * 1024 + ((g & 1) * 32 + c) * 16;
    const unsigned t0 = wn * 16384 + wm * IMG, t1 = wn * 16384 + (7 - wm) * IMG;
    const unsigned gs = R < 7 ? WS_KV + L * 65536 : 0x80000000u;
#pragma unroll
    for (int u = 0; u < 4; ++u) {
      const unsigned tb = u < 2 ? t0 : t1;
      const u32x4 kk = pack44(acc[2][u], acc[3][u]), vv = pack44(acc[4 + (u & 1)][u & 2], acc[4 + (u & 1)][(u & 2) + 1]);
      __builtin_amdgcn_raw_buffer_store_b128(kk, rws, ko + (u & 1) * 128, gs + tb, SC1);
      __builtin_amdgcn_raw_buffer_store_b128(vv, rws, vo + (u & 1) * 256, gs + tb, SC1);
      sts128(lds0 + LDS_OWN + tb + ko + (u & 1) * 128, kk);
      sts128(lds0 + LDS_OWN + tb + vo + (u & 1) * 256, vv);
    }
    u32x4 qf[4];
#pragma unroll
    for (int u = 0; u < 4; ++u) qf[u] = pack44(acc[0][u] * QSCALE, acc[1][u] * QSCALE);
#pragma unroll
    for (int p = 0; p < 2; ++p)
#pragma unroll
      for (int i = 0; i < 4; ++i) { auto r = __builtin_amdgcn_permlane16_swap(qf[2 * p][i], qf[2 * p + 1][i], false, false); qf[2 * p][i] = r[0]; qf[2 * p + 1][i] = r[1]; }
    QA.q0 = asb(qf[2]); QA.q1 = asb(qf[3]); QB.q0 = asb(qf[0]); QB.q1 = asb(qf[1]);
  }
  WAIT_BAR_V(8, ackv);
  STAMP(3);
  bool acked = __all(!ack_ok || ackv == (mybase ^ ACKX));
  const __amdgpu_buffer_rsrc_t rwo = mkrs((G >> 3) ? woB : woA, D_ * D_ * 2);
  const unsigned wov = (8 * w + (l >> 3)) * 2048 + (((l & 7) ^ (((l >> 4) & 3) << 1)) << 4) + ns * 128;
#define DMA_WO(j) dma16(lds0 + (j) * 8192 + w * 1024, wov, rwo, (j) * (64u * 2048u))
  DMA_WO(0); DMA_WO(1); DMA_WO(2); DMA_WO(3);
  const int rr = (int)(l & 31) - 4 * (int)(l >> 5);
  const unsigned kb = lds0 + LDS_OWN + wn * 16384 + l * 16;
  const unsigned myflag = WS_FLAG + L * 128 + w * 4;
  f32x16 negm;
#define A QA
#define B QB
  A.o = f32x16{}; B.o = f32x16{}; A.m = B.m = A.l = B.l = 0.f;
  const unsigned pfoff = WS_FLAG + (L - 16) * 128 + w * 4;
  bool have_prev = false;
  const unsigned kvsrc = WS_KV + (L - 16) * 65536 + wn * 16384, kvdst = lds0 + LDS_PREV + wn * 16384;
#define DMA_PREV() do { _Pragma("unroll") for (int j_ = 0; j_ < 8; ++j_) { const unsigned o_ = (j_ & 1) * 1024 + ((j_ >> 1) & 1) * 32768 + ((j_ & 4) ? (7 - wm) : wm) * IMG; \
      dma16_sc1(kvdst + o_, l * 16, rws, kvsrc + o_); } } while (0)
#define SIGPOS 2
#pragma unroll 1
  for (int qi = 0; qi < 2; ++qi) {
    const int a = qi ? wm : 7 - wm;
    attn_tile<1>(kb + a * IMG, A, negm, rr);
#pragma unroll 1
    for (int j = 0; j < a; ++j) {
      attn_tile<0>(kb + j * IMG, A, negm, rr);
      if (qi == 0 && j == SIGPOS) {
        WAITV(0);
#pragma unroll 1
        for (unsigned sp = 0; !acked && sp < SPIN_CAP; ++sp) { __builtin_amdgcn_s_sleep(2); acked = __all(!ack_ok || ld32_wait(rws, ackoff) == (mybase ^ ACKX)); }
        if (l == 0) st32(mybase + 1, rws, myflag);
        STAMP(4);
        if (R > 0) dma4_sc1(kvdst + wm * IMG, pfoff, rws, 0);
      }
    }
    SWAPQ();
    if (qi == 0 && R > 0) {
      WAITV(0);
      const unsigned pf = rfl(*(volatile LAS unsigned*)(size_t)(kvdst + wm * IMG));
      if (pf - pbase - 1u < 2u) { DMA_PREV(); have_prev = true; }
    }
  }
  STAMP(5);
  if (R > 0 && !have_prev) {
#pragma unroll 1
    for (unsigned sp = 0; sp < SPIN_CAP; ++sp) {
      if (rfl(ld32_wait(rws, pfoff)) - pbase - 1u < 2u) break;
      __builtin_amdgcn_s_sleep(1);
    }
    DMA_PREV();
  }
  STAMP(6);
  WAIT_BAR(0);
  STAMP(7);
#pragma unroll
  for (int j = 4; j < 12; ++j) DMA_WO(j);
  if (R > 0) {
#pragma unroll 1
    for (int qi = 0; qi < 2; ++qi) {
      const int a = qi ? wm : 7 - wm;
#pragma unroll
      for (int i = 0; i < 16; ++i) negm[i] = -A.m;
      attn_tile<2>(kb + 65536 + a * IMG, A, negm, rr);
#pragma unroll 1
      for (int j = a + 1; j < 8; ++j) attn_tile<0>(kb + 65536 + j * IMG, A, negm, rr);
      SWAPQ();
    }
  }
  STAMP(8);
  { const float ia = __builtin_amdgcn_rcpf(swapsum(A.l)), ib = __builtin_amdgcn_rcpf(swapsum(B.l));
    A.o *= ia; B.o *= ib;
    const unsigned hh = hp * 2 + wn, T0 = br * 64 + R * 8 + wm, T1 = br * 64 + R * 8 + 7 - wm;
    const unsigned ol = ((l >> 4) & 1) * 1024 + ((l >> 5) * 16 + (l & 15)) * 16;
#pragma unroll
    for (int s = 0; s < 2; ++s) {
      __builtin_amdgcn_raw_buffer_store_b128(pack8(A.o, s), rws, ol + s * 512, WS_O + (T1 * 32 + hh) * IMG, SC1);
      __builtin_amdgcn_raw_buffer_store_b128(pack8(B.o, s), rws, ol + s * 512, WS_O + (T0 * 32 + hh) * IMG, SC1);
    } }
#undef A
#undef B
  WAITV(0);
  if (l == 0) st32(mybase + 2, rws, myflag);
  STAMP(9);
  asm volatile("s_barrier" ::: "memory");
  DMA_WO(12); DMA_WO(13); DMA_WO(14); DMA_WO(15);
  { const unsigned pw = (w < 4 ? w : 7 - w) + (l & 1) * 4;
    const unsigned fo = WS_FLAG + (G * 16 + ((l & 31) >> 1)) * 128 + pw * 4;
#pragma unroll 1
    for (unsigned sp = 0; sp < SPIN_CAP; ++sp) {
      const unsigned f = ld32_wait(rws, fo);
      if (__all(l >= 32 || f == obase + 2)) break;
      __builtin_amdgcn_s_sleep(1);
    } }
  STAMP(10);
  const unsigned T = G * 8 + w;
  const unsigned ob = WS_O + T * 32 * IMG;
  const unsigned wa2 = lds0 + (16 * (g >> 1) + 4 * (g & 1) + q4) * 128 + ((2 * (g & 1) + (q4 >> 1)) << 5) + (l & 3) * 8;
  f32x4 oc[4][2] = {};
  u32x4 of[16];
  s16x4 wl[2][4], wh[2][4];
#pragma unroll
  for (int i = 0; i < 16; ++i) of[i] = __builtin_amdgcn_raw_buffer_load_b128(rws, l * 16 + (i & 3) * 1024, ob + (i >> 2) * 4096, SC1);
#pragma unroll
  for (int nt = 0; nt < 4; ++nt) { wl[0][nt] = ldstr(wa2 ^ (nt << 5)); wh[0][nt] = ldstr((wa2 ^ (nt << 5)) + 1024); }
  _Pragma("unroll") for (int i_ = 0; i_ < 8; ++i_) PIN(oc[i_ >> 1][i_ & 1]);
  asm volatile("s_nop 1" ::: "memory");
  SBAR();
#pragma unroll 1
  for (int gq = 0; gq < 4; ++gq) {
    if (gq == 2) asm volatile("s_barrier" ::: "memory");
    const unsigned wb = wa2 + gq * 32768;
    const unsigned on = gq < 3 ? ob + (gq + 1) * 16384 : 0x80000000u;
    sfor<0, 8>([&](auto hi) AI { constexpr int h_ = decltype(hi)::v, cur = h_ & 1, nxt = cur ^ 1;
      sfor<0, 8>([&](auto mi) AI { constexpr int m_ = decltype(mi)::v, nt = m_ >> 1, tt = m_ & 1;
        { const bf16x8 wf = cat(wl[cur][nt], wh[cur][nt]); MFA(oc[nt][tt], wf, of[2 * h_ + tt]); }
        if constexpr (tt == 0) wl[nxt][nt] = ldstr((wb ^ (nt << 5)) + (h_ + 1) * 4096); else wh[nxt][nt] = ldstr((wb ^ (nt << 5)) + (h_ + 1) * 4096 + 1024);
        if constexpr (m_ >= 6) of[2 * h_ + tt] = __builtin_amdgcn_raw_buffer_load_b128(rws, l * 16 + ((2 * h_ + tt) & 3) * 1024, on + ((2 * h_ + tt) >> 2) * 4096, SC1);
        SBAR(); }); });
  }
  asm volatile("s_nop 7" ::: "memory");
  _Pragma("unroll") for (int i_ = 0; i_ < 8; ++i_) PIN(oc[i_ >> 1][i_ & 1]);
  STAMP(11);
  const __amdgpu_buffer_rsrc_t rout = mkrs(out, S_ * 2 * D_ * 2);
  const unsigned orow = (((G & 7) * 8 + w) * 32 + c) * 4096 + (G >> 3) * 2048 + ns * 128 + (g & 1) * 32 + (g >> 1) * 16;
#pragma unroll
  for (int tt = 0; tt < 2; ++tt) {
#pragma unroll
    for (int np = 0; np < 2; ++np) {
      const f32x4 &a = oc[2 * np][tt], &b = oc[2 * np + 1][tt];
      auto r0 = __builtin_amdgcn_permlane16_swap(pk2(a[0], a[1]), pk2(b[0], b[1]), false, false);
      auto r1 = __builtin_amdgcn_permlane16_swap(pk2(a[2], a[3]), pk2(b[2], b[3]), false, false);
      u32x4 v = {r0[0], r1[0], r0[1], r1[1]};
      __builtin_amdgcn_raw_buffer_store_b128(v, rout, orow + tt * 65536 + np * 64, 0, 0);
    }
  }
}

extern "C" void kernel_launch(void* const* d_in, const int* in_sizes, int n_in, void* d_out, int out_size, void* d_ws, size_t ws_size,
                              hipStream_t stream) {
  k_fused<<<256, 512, 0, stream>>>(d_in[0], d_in[1], d_in[2], d_in[3], d_in[4], d_in[5], d_ws, d_out);
}
```

```cpp
#include <hip/hip_runtime.h>
#include <hip/hip_bf16.h>

typedef short bf16x8 __attribute__((ext_vector_type(8)));
typedef short s16x4 __attribute__((ext_vector_type(4)));
typedef float f32x16 __attribute__((ext_vector_type(16)));
typedef float f32x4 __attribute__((ext_vector_type(4)));
typedef unsigned u32x4 __attribute__((ext_vector_type(4)));
typedef __bf16 bf16x2_t __attribute__((ext_vector_type(2)));
typedef float f32x2 __attribute__((ext_vector_type(2)));
#define LAS __attribute__((address_space(3)))
#define DI __device__ __forceinline__
#define AI __attribute__((always_inline))
#define MFMA(a, b, c) __builtin_amdgcn_mfma_f32_32x32x16_bf16((a), (b), (c), 0, 0, 0)
#define SBAR() __builtin_amdgcn_sched_barrier(0)
#define PIN(x) asm volatile("" : "+v"(x))
#define MFA(acc_, a_, b_) asm volatile("v_mfma_f32_16x16x32_bf16 %0, %1, %2, %0" : "+v"(acc_) : "v"(a_), "v"(b_))

constexpr int S_ = 2048, D_ = 1024;
constexpr float QSCALE = 0.17677669529663687f * 1.4426950408889634f;
constexpr float THR = 8.0f;
constexpr unsigned IMG = 2048;
constexpr unsigned WS_FLAG = 0, WS_ACK = 32768, WS_KV = 65536, WS_O = WS_KV + 256u * 65536u, WS_END = WS_O + 2u * 64u * 32u * IMG;
constexpr unsigned ACKX = 0x5bd1e995u;
constexpr int SC1 = 16;
constexpr unsigned SPIN_CAP = 1u << 18;

#define HPMAP ((xcd & 1) * 8 + (idx >> 2))
#define RMAP (((xcd >> 1) & 1) * 4 + (idx & 3))
#define NOMEMX 1
template <int I> struct IC { static constexpr int v = I; };
template <int B, int E, class F> DI void sfor(F&& f) { if constexpr (B < E) { f(IC<B>{}); sfor<B + 1, E>(f); } }
DI unsigned rfl(unsigned x) { return (unsigned)__builtin_amdgcn_readfirstlane((int)x); }
DI unsigned pk2(float lo, float hi) { f32x2 v = {lo, hi}; return __builtin_bit_cast(unsigned, __builtin_convertvector(v, bf16x2_t)); }
DI u32x4 pack8(const f32x16& x, int s) {
  u32x4 p;
  p[0] = pk2(x[8 * s + 0], x[8 * s + 1]); p[1] = pk2(x[8 * s + 2], x[8 * s + 3]);
  p[2] = pk2(x[8 * s + 4], x[8 * s + 5]); p[3] = pk2(x[8 * s + 6], x[8 * s + 7]);
  return p;
}
DI u32x4 pack44(const f32x4& a, const f32x4& b) { u32x4 p; p[0] = pk2(a[0], a[1]); p[1] = pk2(a[2], a[3]); p[2] = pk2(b[0], b[1]); p[3] = pk2(b[2], b[3]); return p; }
DI bf16x8 asb(u32x4 v) { return __builtin_bit_cast(bf16x8, v); }
DI bf16x8 lds128(unsigned a) { return *(const LAS bf16x8*)(size_t)a; }
DI void sts128(unsigned a, u32x4 v) { *(LAS u32x4*)(size_t)a = v; }
DI s16x4 ldstr(unsigned a) { return __builtin_amdgcn_ds_read_tr16_b64_v4i16((LAS s16x4*)(size_t)a); }
DI bf16x8 cat(s16x4 lo, s16x4 hi) { return __builtin_shufflevector(lo, hi, 0, 1, 2, 3, 4, 5, 6, 7); }
DI void dma16(unsigned ldsdst, unsigned voff, __amdgpu_buffer_rsrc_t rs, unsigned soff) {
  asm volatile("s_mov_b32 m0, %0\n\ts_nop 0\n\tbuffer_load_dwordx4 %1, %2, %3 offen lds" ::"s"(ldsdst), "v"(voff), "s"(rs), "s"(soff) : "memory");
}
#define XPOL ""
#define WPOL ""
DI void dma16x(unsigned ldsdst, unsigned voff, __amdgpu_buffer_rsrc_t rs, unsigned soff) {
  asm volatile("s_mov_b32 m0, %0\n\ts_nop 0\n\tbuffer_load_dwordx4 %1, %2, %3 offen " XPOL " lds" ::"s"(ldsdst), "v"(voff), "s"(rs), "s"(soff) : "memory");
}
DI void dma16w(unsigned ldsdst, unsigned voff, __amdgpu_buffer_rsrc_t rs, unsigned soff) {
  asm volatile("s_mov_b32 m0, %0\n\ts_nop 0\n\tbuffer_load_dwordx4 %1, %2, %3 offen " WPOL " lds" ::"s"(ldsdst), "v"(voff), "s"(rs), "s"(soff) : "memory");
}
#define PPOL ""
#define PFD 4
#define PFH 1
DI void pf4(unsigned ldsdst, const char* p) {
  asm volatile("s_mov_b32 m0, %0\n\ts_nop 0\n\tglobal_load_lds_dword %1, off " PPOL ::"s"(ldsdst), "v"(p) : "memory");
}
DI void dma16_sc1(unsigned ldsdst, unsigned voff, __amdgpu_buffer_rsrc_t rs, unsigned soff) {
  asm volatile("s_mov_b32 m0, %0\n\ts_nop 0\n\tbuffer_load_dwordx4 %1, %2, %3 offen sc1 lds" ::"s"(ldsdst), "v"(voff), "s"(rs), "s"(soff) : "memory");
}
#define WAITV(n) asm volatile("s_waitcnt vmcnt(" #n ")" ::: "memory")
#define WAIT_BAR(n) asm volatile("s_waitcnt vmcnt(" #n ") lgkmcnt(0)\n\ts_barrier" ::: "memory")
DI __amdgpu_buffer_rsrc_t mkrs(const void* p, unsigned bytes) { return __builtin_amdgcn_make_buffer_rsrc((void*)p, (short)0, (int)bytes, 0x00020000); }
DI unsigned ld32_issue(__amdgpu_buffer_rsrc_t rs, unsigned off) { unsigned r; asm volatile("buffer_load_dword %0, %1, %2, 0 offen sc1" : "=v"(r) : "v"(off), "s"(rs) : "memory"); return r; }
DI unsigned ld32_wait(__amdgpu_buffer_rsrc_t rs, unsigned off) { unsigned r; asm volatile("buffer_load_dword %0, %1, %2, 0 offen sc1\n\ts_waitcnt vmcnt(0)" : "=&v"(r) : "v"(off), "s"(rs) : "memory"); return r; }
DI void dma4_sc1(unsigned ldsdst, unsigned voff, __amdgpu_buffer_rsrc_t rs, unsigned soff) {
  asm volatile("s_mov_b32 m0, %0\n\ts_nop 0\n\tbuffer_load_dword %1, %2, %3 offen sc1 lds" ::"s"(ldsdst), "v"(voff), "s"(rs), "s"(soff) : "memory");
}
#define WAIT_BAR_V(n, x) asm volatile("s_waitcnt vmcnt(" #n ") lgkmcnt(0)\n\ts_barrier" : "+v"(x) :: "memory")
constexpr unsigned OOB = 0x80000000u;
DI void st32(unsigned v, __amdgpu_buffer_rsrc_t rs, unsigned off) { __builtin_amdgcn_raw_buffer_store_b32(v, rs, off, 0, SC1); }

#define STAMP(i) do {} while (0)

constexpr unsigned XST = 32768, WST = 24576, LDS_W = 3 * XST, LDS_SPARE = LDS_W + 2 * WST, LDS_CTL = 159744;
constexpr unsigned LDS_OWN = 32768, LDS_PREV = 98304, LDS_BYTES = 163840;

struct Frag { bf16x8 x[4]; s16x4 wl[6], wh[6]; };

DI float swapmax(float m) {
  auto rr = __builtin_amdgcn_permlane32_swap(__float_as_uint(m), __float_as_uint(m), false, false);
  return __builtin_fmaxf(__uint_as_float(rr[0]), __uint_as_float(rr[1]));
}
DI float swapsum(float m) {
  auto rr = __builtin_amdgcn_permlane32_swap(__float_as_uint(m), __float_as_uint(m), false, false);
  return __uint_as_float(rr[0]) + __uint_as_float(rr[1]);
}
#define MX3(a, b, c) __builtin_fmaxf(__builtin_fmaxf((a), (b)), (c))
DI float max16(const f32x16& p) {
  float a = MX3(p[0], p[1], p[2]), b = MX3(p[3], p[4], p[5]);
  a = MX3(a, p[6], p[7]); b = MX3(b, p[8], p[9]); a = MX3(a, p[10], p[11]); b = MX3(b, p[12], p[13]);
  a = MX3(a, p[14], p[15]);
  return __builtin_fmaxf(a, b);
}
struct QState { bf16x8 q0, q1; f32x16 o; float m, l; };
#define SWAPQ() do { bf16x8 tq = A.q0; A.q0 = B.q0; B.q0 = tq; tq = A.q1; A.q1 = B.q1; B.q1 = tq; f32x16 to = A.o; A.o = B.o; B.o = to; float tf = A.m; A.m = B.m; B.m = tf; tf = A.l; A.l = B.l; B.l = tf; } while (0)
template <int MODE> DI void attn_tile(unsigned ka, QState& Q, f32x16& negm, int rr) {
  const bf16x8 k0 = lds128(ka), k1 = lds128(ka + 1024), v0 = lds128(ka + 32768), v1 = lds128(ka + 32768 + 1024);
  f32x16 s;
  if (MODE == 1) { f32x16 z = {}; s = MFMA(k0, Q.q0, z); } else s = MFMA(k0, Q.q0, negm);
  s = MFMA(k1, Q.q1, s);
  if (MODE != 0) {
#pragma unroll
    for (int i = 0; i < 16; ++i) { const bool le = ((i & 3) + 16 * ((i >> 2) & 1) + 8 * (i >> 3)) <= rr; s[i] = (le == (MODE == 1)) ? s[i] : -__builtin_inff(); }
  }
  float mx = max16(s);
  if (MODE == 1) {
    mx = swapmax(mx); Q.m = mx;
#pragma unroll
    for (int i = 0; i < 16; ++i) { negm[i] = -mx; s[i] -= mx; }
  } else if (__builtin_expect(__any(mx > THR), 0)) {
    mx = swapmax(mx);
    const float d = __builtin_fmaxf(mx, 0.f), f = __builtin_amdgcn_exp2f(-d);
    Q.m += d; Q.l *= f;
#pragma unroll
    for (int i = 0; i < 16; ++i) { negm[i] = -Q.m; s[i] -= d; Q.o[i] *= f; }
  }
#pragma unroll
  for (int i = 0; i < 16; ++i) s[i] = __builtin_amdgcn_exp2f(s[i]);
  float a = s[0] + s[1], b = s[2] + s[3];
#pragma unroll
  for (int i = 4; i < 16; i += 2) { a += s[i]; b += s[i + 1]; }
  if (MODE == 1) Q.l = a + b; else Q.l += a + b;
  const bf16x8 p0 = asb(pack8(s, 0)), p1 = asb(pack8(s, 1));
  if (MODE == 1) { f32x16 z = {}; Q.o = MFMA(v0, p0, z); } else Q.o = MFMA(v0, p0, Q.o);
  Q.o = MFMA(v1, p1, Q.o);
}

__global__ __launch_bounds__(512, 2) void k_fused(const void* xA, const void* wA, const void* woA, const void* xB, const void* wB, const void* woB,
                                                  void* ws, void* out) {
  __shared__ __attribute__((aligned(1024))) char smem[LDS_BYTES];
  const unsigned lds0 = (unsigned)(size_t)(LAS char*)smem;
  const unsigned bid = blockIdx.x, xcd = bid & 7, idx = bid >> 3;
  const unsigned br = xcd >> 2, R = RMAP, hp = HPMAP;
  const unsigned L = (br * 8 + R) * 16 + hp;
  const unsigned G = xcd * 2 + (idx >> 4), ns = idx & 15;
  const unsigned tid = threadIdx.x, l = tid & 63, w = rfl(tid >> 6), wm = w & 3, wn = w >> 2;
  const __amdgpu_buffer_rsrc_t rx = mkrs(br ? xB : xA, NOMEMX * S_ * D_ * 2), rw = mkrs(br ? wB : wA, D_ * 3 * D_ * 2), rws = mkrs(ws, WS_END);
  STAMP(0);
  const unsigned fl_id = l < 32 ? G * 16 + (l >> 1) : (l == 32 ? L - 16 : L);
  const bool fl_ok = l < 34 && !(l == 32 && R == 0);
  unsigned obase = ld32_issue(rws, fl_ok ? WS_FLAG + fl_id * 128 : OOB);
  const unsigned mtw = (w & 1) ? 7 - (w >> 1) : (w >> 1);
  const unsigned xv0 = (R * 256 + mtw * 32 + (l >> 3)) * 2048 + (((l & 7) ^ (l >> 4)) << 4);
  const unsigned xv1 = (R * 256 + mtw * 32 + 8 + (l >> 3)) * 2048 + (((l & 7) ^ (4 + (l >> 4))) << 4);
  const unsigned wv = (8 * w + (l >> 3)) * 6144 + (((l & 7) ^ ((((l >> 4) & 1) | ((w & 1) << 1)) << 1)) << 4) + hp * 128;
  const unsigned xdst = lds0 + w * 4096, wdst = lds0 + LDS_W + w * 1024;
#define DMA_X(t, st) do { const unsigned so_ = (t) * 128u, d_ = xdst + (st); \
    dma16x(d_, xv0, rx, so_); dma16x(d_ + 1024, xv1, rx, so_); dma16x(d_ + 2048, xv0, rx, so_ + 32768); dma16x(d_ + 3072, xv1, rx, so_ + 32768); } while (0)
#define DMA_W(t, st) do { const unsigned so_ = (t) * (64u * 6144u), d_ = wdst + (st); \
    dma16w(d_, wv, rw, so_); dma16w(d_ + 8192, wv, rw, so_ + 2048); dma16w(d_ + 16384, wv, rw, so_ + 4096); } while (0)
  DMA_W(0, 0); DMA_X(0, 0); DMA_W(1, WST); DMA_X(1, XST);
  const unsigned pl = PFH == 2 ? (l >> 1) : l, pli = pl < 10 ? pl : 0, phalf = PFH == 2 ? (l & 1) * 64 : 0;
  const unsigned pwl = ((idx & 3) * 8 + w) * 6 + (pli - 4);
  const char* pfp = pli < 4 ? (const char*)(br ? xB : xA) + (R * 256 + ((idx >> 2) * 8 + w) * 4 + pli) * 2048 + (PFD - 1) * 128 + phalf
                            : (const char*)(br ? wB : wA) + (pwl / 3 + (PFD - 1) * 64) * 6144 + (pwl % 3) * 2048 + hp * 128 + phalf;
  const unsigned pstep = pli < 4 ? 128u : 64u * 6144u;
  pf4(lds0 + LDS_SPARE + 8192, pfp); pfp += pstep;
  const unsigned c = l & 15, g = l >> 4, q4 = c >> 2;
  const unsigned xa = lds0 + wm * 8192 + c * 128 + ((g ^ (c >> 1)) << 4);
  const unsigned wa = lds0 + LDS_W + (8 * g + q4) * 128 + (((2 * wn) ^ ((q4 >> 1) | ((g & 1) << 1))) << 5) + (l & 3) * 8;
  f32x4 acc[6][4] = {};
  Frag A, B;
#define RDX(F, xb, s, u) F.x[u] = lds128(((xb) ^ ((s) << 6)) + (u) * 2048)
#define MM(C, n, u) do { const bf16x8 wf_ = cat(C.wl[n], C.wh[n]); if ((n) < 4) MFA(acc[n][u], wf_, C.x[u]); else MFA(acc[n][u], C.x[u], wf_); } while (0)
#define RDWL(F, wb, s, n) F.wl[n] = ldstr(((wb) ^ (((n) & 1) << 5)) + ((n) >> 1) * 8192 + (s) * 4096)
#define RDWH(F, wb, s, n) F.wh[n] = ldstr(((wb) ^ (((n) & 1) << 5)) + ((n) >> 1) * 8192 + (s) * 4096 + 512)
#define RD1(F, xb, wb, s, r) do { if ((r) == 0) RDX(F, xb, s, 0); else if ((r) == 1) RDWL(F, wb, s, 0); else if ((r) == 2) RDWH(F, wb, s, 0); \
    else if ((r) < 6) RDX(F, xb, s, ((r) - 2) & 3); else if (((r) & 1) == 0) RDWL(F, wb, s, (((r) + 8) >> 1) % 6); else RDWH(F, wb, s, (((r) + 8) >> 1) % 6); } while (0)
#define PB 27
#define PFP 36
#define BP(r) ((r) * 3 / 2)
#define AP(r) (29 + (r) * 6 / 5)
#define XP(j) (2 + (j) * 6)
#define WP(j) (28 + (j) * 3)
#define TILE(LDA, DMA, WAITB) do { \
    const unsigned xb = xa + xs, wb = wa + wsx, wd = wdst + wsx; \
    xp = xs; xs = (xs == 2 * XST) ? 0 : xs + XST; wsx ^= WST; \
    const unsigned xn = xa + xs, wn_ = wa + wsx; \
    sfor<0, 48>([&](auto mi) AI { constexpr int m_ = decltype(mi)::v; \
      if constexpr (m_ < 24) MM(A, m_ >> 2, m_ & 3); else MM(B, (m_ - 24) >> 2, m_ & 3); \
      sfor<0, 16>([&](auto ri) AI { constexpr int r_ = decltype(ri)::v; \
        if constexpr (BP(r_) == m_) RD1(B, xb, wb, 1, r_); if constexpr (LDA && AP(r_) == m_) RD1(A, xn, wn_, 0, r_); }); \
      sfor<0, 4>([&](auto ji) AI { constexpr int j_ = decltype(ji)::v; \
        if constexpr (DMA && XP(j_) == m_) dma16x(xd + j_ * 1024, (j_ & 1) ? xv1 : xv0, rx, xso + (j_ >> 1) * 32768); \
        if constexpr (DMA && j_ < 3 && WP(j_) == m_) dma16w(wd + j_ * 8192, wv, rw, wso + j_ * 2048); }); \
      if constexpr (DMA && m_ == PFP) { pf4(lds0 + LDS_SPARE + 8192, pfp); pfp += pinc; } \
      SBAR(); \
      if constexpr (m_ == PB) { WAITB; SBAR(); } }); } while (0)
  WAIT_BAR_V(8, obase);
  STAMP(1);
  unsigned xs = 0, wsx = 0, xp = 2 * XST;
  sfor<0, 16>([&](auto ri) AI { constexpr int r_ = decltype(ri)::v; RD1(A, xa, wa, 0, r_); SBAR(); });
  if (w == 0) st32(obase ^ ACKX, rws, (fl_ok && l <= 32 && !(l & 1)) ? WS_ACK + fl_id * 128 + (l < 32 ? ns : 16) * 4 : OOB);
  const unsigned pbase = __builtin_amdgcn_readlane(obase, 32), mybase = __builtin_amdgcn_readlane(obase, 33);
  _Pragma("unroll") for (int i_ = 0; i_ < 24; ++i_) PIN(acc[i_ >> 2][i_ & 3]);
  asm volatile("s_nop 1" ::: "memory");
  SBAR();
#pragma unroll 1
  for (int t = 0; t < 15; ++t) {
    const unsigned xso = t < 14 ? (t + 2) * 128u : 0x80000000u, wso = t < 14 ? (t + 2) * (64u * 6144u) : OOB;
    const unsigned xd = t < 14 ? xdst + xp : lds0 + LDS_SPARE;
    const unsigned pinc = t + PFD < 15 ? pstep : 0u;
    TILE(true, true, WAIT_BAR(5));
  }
  const bool ack_ok = l < 17 && !(l == 16 && R == 7);
  const unsigned ackoff = ack_ok ? WS_ACK + L * 128 + l * 4 : OOB;
  unsigned ackv = ld32_issue(rws, ackoff);
  { const unsigned xso = 0, wso = 0, xd = 0, pinc = 0; TILE(false, false, (void)0); }
  asm volatile("s_nop 7" ::: "memory");
  _Pragma("unroll") for (int i_ = 0; i_ < 24; ++i_) PIN(acc[i_ >> 2][i_ & 3]);
  STAMP(2);
  QState QA, QB;
  {
    const unsigned ko = (g & 1) * 1024 + ((g >> 1) * 32 + (c & 7) + 16 * (c >> 3)) * 16;
    const unsigned vo = 32768 + (g >> 1) * 1024 + ((g & 1) * 32 + c) * 16;
    const unsigned t0 = wn * 16384 + wm * IMG, t1 = wn * 16384 + (7 - wm) * IMG;
    const unsigned gs = R < 7 ? WS_KV + L * 65536 : 0x80000000u;
#pragma unroll
    for (int u = 0; u < 4; ++u) {
      const unsigned tb = u < 2 ? t0 : t1;
      const u32x4 kk = pack44(acc[2][u], acc[3][u]), vv = pack44(acc[4 + (u & 1)][u & 2], acc[4 + (u & 1)][(u & 2) + 1]);
      __builtin_amdgcn_raw_buffer_store_b128(kk, rws, ko + (u & 1) * 128, gs + tb, SC1);
      __builtin_amdgcn_raw_buffer_store_b128(vv, rws, vo + (u & 1) * 256, gs + tb, SC1);
      sts128(lds0 + LDS_OWN + tb + ko + (u & 1) * 128, kk);
      sts128(lds0 + LDS_OWN + tb + vo + (u & 1) * 256, vv);
    }
    u32x4 qf[4];
#pragma unroll
    for (int u = 0; u < 4; ++u) qf[u] = pack44(acc[0][u] * QSCALE, acc[1][u] * QSCALE);
#pragma unroll
    for (int p = 0; p < 2; ++p)
#pragma unroll
      for (int i = 0; i < 4; ++i) { auto r = __builtin_amdgcn_permlane16_swap(qf[2 * p][i], qf[2 * p + 1][i], false, false); qf[2 * p][i] = r[0]; qf[2 * p + 1][i] = r[1]; }
    QA.q0 = asb(qf[2]); QA.q1 = asb(qf[3]); QB.q0 = asb(qf[0]); QB.q1 = asb(qf[1]);
  }
  WAIT_BAR_V(8, ackv);
  STAMP(3);
  bool acked = __all(!ack_ok || ackv == (mybase ^ ACKX));
  const __amdgpu_buffer_rsrc_t rwo = mkrs((G >> 3) ? woB : woA, D_ * D_ * 2);
  const unsigned wov = (8 * w + (l >> 3)) * 2048 + (((l & 7) ^ (((l >> 4) & 3) << 1)) << 4) + ns * 128;
#define DMA_WO(j) dma16(lds0 + (j) * 8192 + w * 1024, wov, rwo, (j) * (64u * 2048u))
  DMA_WO(0); DMA_WO(1); DMA_WO(2); DMA_WO(3);
  const int rr = (int)(l & 31) - 4 * (int)(l >> 5);
  const unsigned kb = lds0 + LDS_OWN + wn * 16384 + l * 16;
  const unsigned myflag = WS_FLAG + L * 128 + w * 4;
  f32x16 negm;
#define A QA
#define B QB
  A.o = f32x16{}; B.o = f32x16{}; A.m = B.m = A.l = B.l = 0.f;
  const unsigned pfoff = WS_FLAG + (L - 16) * 128 + w * 4;
  bool have_prev = false;
  const unsigned kvsrc = WS_KV + (L - 16) * 65536 + wn * 16384, kvdst = lds0 + LDS_PREV + wn * 16384;
#define DMA_PREV() do { _Pragma("unroll") for (int j_ = 0; j_ < 8; ++j_) { const unsigned o_ = (j_ & 1) * 1024 + ((j_ >> 1) & 1) * 32768 + ((j_ & 4) ? (7 - wm) : wm) * IMG; \
      dma16_sc1(kvdst + o_, l * 16, rws, kvsrc + o_); } } while (0)
#define SIGPOS 2
#pragma unroll 1
  for (int qi = 0; qi < 2; ++qi) {
    const int a = qi ? wm : 7 - wm;
    attn_tile<1>(kb + a * IMG, A, negm, rr);
#pragma unroll 1
    for (int j = 0; j < a; ++j) {
      attn_tile<0>(kb + j * IMG, A, negm, rr);
      if (qi == 0 && j == SIGPOS) {
        WAITV(0);
#pragma unroll 1
        for (unsigned sp = 0; !acked && sp < SPIN_CAP; ++sp) { __builtin_amdgcn_s_sleep(2); acked = __all(!ack_ok || ld32_wait(rws, ackoff) == (mybase ^ ACKX)); }
        if (l == 0) st32(mybase + 1, rws, myflag);
        STAMP(4);
        if (R > 0) dma4_sc1(kvdst + wm * IMG, pfoff, rws, 0);
      }
    }
    SWAPQ();
    if (qi == 0 && R > 0) {
      WAITV(0);
      const unsigned pf = rfl(*(volatile LAS unsigned*)(size_t)(kvdst + wm * IMG));
      if (pf - pbase - 1u < 2u) { DMA_PREV(); have_prev = true; }
    }
  }
  STAMP(5);
  if (R > 0 && !have_prev) {
#pragma unroll 1
    for (unsigned sp = 0; sp < SPIN_CAP; ++sp) {
      if (rfl(ld32_wait(rws, pfoff)) - pbase - 1u < 2u) break;
      __builtin_amdgcn_s_sleep(1);
    }
    DMA_PREV();
  }
  STAMP(6);
  WAIT_BAR(0);
  STAMP(7);
#pragma unroll
  for (int j = 4; j < 12; ++j) DMA_WO(j);
  if (R > 0) {
#pragma unroll 1
    for (int qi = 0; qi < 2; ++qi) {
      const int a = qi ? wm : 7 - wm;
#pragma unroll
      for (int i = 0; i < 16; ++i) negm[i] = -A.m;
      attn_tile<2>(kb + 65536 + a * IMG, A, negm, rr);
#pragma unroll 1
      for (int j = a + 1; j < 8; ++j) attn_tile<0>(kb + 65536 + j * IMG, A, negm, rr);
      SWAPQ();
    }
  }
  STAMP(8);
  { const float ia = __builtin_amdgcn_rcpf(swapsum(A.l)), ib = __builtin_amdgcn_rcpf(swapsum(B.l));
    A.o *= ia; B.o *= ib;
    const unsigned hh = hp * 2 + wn, T0 = br * 64 + R * 8 + wm, T1 = br * 64 + R * 8 + 7 - wm;
    const unsigned ol = ((l >> 4) & 1) * 1024 + ((l >> 5) * 16 + (l & 15)) * 16;
#pragma unroll
    for (int s = 0; s < 2; ++s) {
      __builtin_amdgcn_raw_buffer_store_b128(pack8(A.o, s), rws, ol + s * 512, WS_O + (T1 * 32 + hh) * IMG, SC1);
      __builtin_amdgcn_raw_buffer_store_b128(pack8(B.o, s), rws, ol + s * 512, WS_O + (T0 * 32 + hh) * IMG, SC1);
    } }
#undef A
#undef B
  WAITV(0);
  if (l == 0) st32(mybase + 2, rws, myflag);
  STAMP(9);
  asm volatile("s_barrier" ::: "memory");
  DMA_WO(12); DMA_WO(13); DMA_WO(14); DMA_WO(15);
  { const unsigned pw = (w < 4 ? w : 7 - w) + (l & 1) * 4;
    const unsigned fo = WS_FLAG + (G * 16 + ((l & 31) >> 1)) * 128 + pw * 4;
#pragma unroll 1
    for (unsigned sp = 0; sp < SPIN_CAP; ++sp) {
      const unsigned f = ld32_wait(rws, fo);
      if (__all(l >= 32 || f == obase + 2)) break;
      __builtin_amdgcn_s_sleep(1);
    } }
  STAMP(10);
  const unsigned T = G * 8 + w;
  const unsigned ob = WS_O + T * 32 * IMG;
  const unsigned wa2 = lds0 + (16 * (g >> 1) + 4 * (g & 1) + q4) * 128 + ((2 * (g & 1) + (q4 >> 1)) << 5) + (l & 3) * 8;
  f32x4 oc[4][2] = {};
  u32x4 of[16];
  s16x4 wl[2][4], wh[2][4];
#pragma unroll
  for (int i = 0; i < 16; ++i) of[i] = __builtin_amdgcn_raw_buffer_load_b128(rws, l * 16 + (i & 3) * 1024, ob + (i >> 2) * 4096, SC1);
#pragma unroll
  for (int nt = 0; nt < 4; ++nt) { wl[0][nt] = ldstr(wa2 ^ (nt << 5)); wh[0][nt] = ldstr((wa2 ^ (nt << 5)) + 1024); }
  _Pragma("unroll") for (int i_ = 0; i_ < 8; ++i_) PIN(oc[i_ >> 1][i_ & 1]);
  asm volatile("s_nop 1" ::: "memory");
  SBAR();
#pragma unroll 1
  for (int gq = 0; gq < 4; ++gq) {
    if (gq == 2) asm volatile("s_barrier" ::: "memory");
    const unsigned wb = wa2 + gq * 32768;
    const unsigned on = gq < 3 ? ob + (gq + 1) * 16384 : 0x80000000u;
    sfor<0, 8>([&](auto hi) AI { constexpr int h_ = decltype(hi)::v, cur = h_ & 1, nxt = cur ^ 1;
      sfor<0, 8>([&](auto mi) AI { constexpr int m_ = decltype(mi)::v, nt = m_ >> 1, tt = m_ & 1;
        { const bf16x8 wf = cat(wl[cur][nt], wh[cur][nt]); MFA(oc[nt][tt], wf, of[2 * h_ + tt]); }
        if constexpr (tt == 0) wl[nxt][nt] = ldstr((wb ^ (nt << 5)) + (h_ + 1) * 4096); else wh[nxt][nt] = ldstr((wb ^ (nt << 5)) + (h_ + 1) * 4096 + 1024);
        if constexpr (m_ >= 6) of[2 * h_ + tt] = __builtin_amdgcn_raw_buffer_load_b128(rws, l * 16 + ((2 * h_ + tt) & 3) * 1024, on + ((2 * h_ + tt) >> 2) * 4096, SC1);
        SBAR(); }); });
  }
  asm volatile("s_nop 7" ::: "memory");
  _Pragma("unroll") for (int i_ = 0; i_ < 8; ++i_) PIN(oc[i_ >> 1][i_ & 1]);
  STAMP(11);
  const __amdgpu_buffer_rsrc_t rout = mkrs(out, S_ * 2 * D_ * 2);
  const unsigned orow = (((G & 7) * 8 + w) * 32 + c) * 4096 + (G >> 3) * 2048 + ns * 128 + (g & 1) * 32 + (g >> 1) * 16;
#pragma unroll
  for (int tt = 0; tt < 2; ++tt) {
#pragma unroll
    for (int np = 0; np < 2; ++np) {
      const f32x4 &a = oc[2 * np][tt], &b = oc[2 * np + 1][tt];
      auto r0 = __builtin_amdgcn_permlane16_swap(pk2(a[0], a[1]), pk2(b[0], b[1]), false, false);
      auto r1 = __builtin_amdgcn_permlane16_swap(pk2(a[2], a[3]), pk2(b[2], b[3]), false, false);
      u32x4 v = {r0[0], r1[0], r0[1], r1[1]};
      __builtin_amdgcn_raw_buffer_store_b128(v, rout, orow + tt * 65536 + np * 64, 0, 0);
    }
  }
}

extern "C" void kernel_launch(void* const* d_in, const int* in_sizes, int n_in, void* d_out, int out_size, void* d_ws, size_t ws_size,
                              hipStream_t stream) {
  k_fused<<<256, 512, 0, stream>>>(d_in[0], d_in[1], d_in[2], d_in[3], d_in[4], d_in[5], (char*)d_ws + ((ws_size - WS_END) & ~(size_t)0xfffff), d_out);
}
```

```cpp
#define DIAG 0
#include <hip/hip_runtime.h>
#include <hip/hip_bf16.h>

typedef short bf16x8 __attribute__((ext_vector_type(8)));
typedef short s16x4 __attribute__((ext_vector_type(4)));
typedef float f32x16 __attribute__((ext_vector_type(16)));
typedef float f32x4 __attribute__((ext_vector_type(4)));
typedef unsigned u32x4 __attribute__((ext_vector_type(4)));
typedef __bf16 bf16x2_t __attribute__((ext_vector_type(2)));
typedef float f32x2 __attribute__((ext_vector_type(2)));
#define LAS __attribute__((address_space(3)))
#define DI __device__ __forceinline__
#define AI __attribute__((always_inline))
#define MFMA(a, b, c) __builtin_amdgcn_mfma_f32_32x32x16_bf16((a), (b), (c), 0, 0, 0)
#define SBAR() __builtin_amdgcn_sched_barrier(0)
#define PIN(x) asm volatile("" : "+v"(x))
#define MFA(acc_, a_, b_) asm volatile("v_mfma_f32_16x16x32_bf16 %0, %1, %2, %0" : "+v"(acc_) : "v"(a_), "v"(b_))

constexpr int S_ = 2048, D_ = 1024;
constexpr float QSCALE = 0.17677669529663687f * 1.4426950408889634f;
constexpr float THR = 8.0f;
constexpr unsigned IMG = 2048;
constexpr unsigned WS_FLAG = 0, WS_ACK = 32768, WS_KV = 65536, WS_O = WS_KV + 256u * 65536u, WS_END = WS_O + 2u * 64u * 32u * IMG;
constexpr unsigned ACKX = 0x5bd1e995u;
constexpr int SC1 = 16;
constexpr unsigned SPIN_CAP = 1u << 18;

#define HPMAP ((xcd & 1) * 8 + (idx >> 2))
#define RMAP (((xcd >> 1) & 1) * 4 + (idx & 3))
#define NOMEMX 1
template <int I> struct IC { static constexpr int v = I; };
template <int B, int E, class F> DI void sfor(F&& f) { if constexpr (B < E) { f(IC<B>{}); sfor<B + 1, E>(f); } }
DI unsigned rfl(unsigned x) { return (unsigned)__builtin_amdgcn_readfirstlane((int)x); }
DI unsigned pk2(float lo, float hi) { f32x2 v = {lo, hi}; return __builtin_bit_cast(unsigned, __builtin_convertvector(v, bf16x2_t)); }
DI u32x4 pack8(const f32x16& x, int s) {
  u32x4 p;
  p[0] = pk2(x[8 * s + 0], x[8 * s + 1]); p[1] = pk2(x[8 * s + 2], x[8 * s + 3]);
  p[2] = pk2(x[8 * s + 4], x[8 * s + 5]); p[3] = pk2(x[8 * s + 6], x[8 * s + 7]);
  return p;
}
DI u32x4 pack44(const f32x4& a, const f32x4& b) { u32x4 p; p[0] = pk2(a[0], a[1]); p[1] = pk2(a[2], a[3]); p[2] = pk2(b[0], b[1]); p[3] = pk2(b[2], b[3]); return p; }
DI bf16x8 asb(u32x4 v) { return __builtin_bit_cast(bf16x8, v); }
DI bf16x8 lds128(unsigned a) { return *(const LAS bf16x8*)(size_t)a; }
DI void sts128(unsigned a, u32x4 v) { *(LAS u32x4*)(size_t)a = v; }
DI s16x4 ldstr(unsigned a) { return __builtin_amdgcn_ds_read_tr16_b64_v4i16((LAS s16x4*)(size_t)a); }
DI bf16x8 cat(s16x4 lo, s16x4 hi) { return __builtin_shufflevector(lo, hi, 0, 1, 2, 3, 4, 5, 6, 7); }
DI void dma16(unsigned ldsdst, unsigned voff, __amdgpu_buffer_rsrc_t rs, unsigned soff) {
  asm volatile("s_mov_b32 m0, %0\n\ts_nop 0\n\tbuffer_load_dwordx4 %1, %2, %3 offen lds" ::"s"(ldsdst), "v"(voff), "s"(rs), "s"(soff) : "memory");
}
#define XPOL ""
#define WPOL ""
DI void dma16x(unsigned ldsdst, unsigned voff, __amdgpu_buffer_rsrc_t rs, unsigned soff) {
  asm volatile("s_mov_b32 m0, %0\n\ts_nop 0\n\tbuffer_load_dwordx4 %1, %2, %3 offen " XPOL " lds" ::"s"(ldsdst), "v"(voff), "s"(rs), "s"(soff) : "memory");
}
DI void dma16w(unsigned ldsdst, unsigned voff, __amdgpu_buffer_rsrc_t rs, unsigned soff) {
  asm volatile("s_mov_b32 m0, %0\n\ts_nop 0\n\tbuffer_load_dwordx4 %1, %2, %3 offen " WPOL " lds" ::"s"(ldsdst), "v"(voff), "s"(rs), "s"(soff) : "memory");
}
#define PPOL ""
#define PFD 4
#define PFH 1
DI void pf4(unsigned ldsdst, const char* p) {
  asm volatile("s_mov_b32 m0, %0\n\ts_nop 0\n\tglobal_load_lds_dword %1, off " PPOL ::"s"(ldsdst), "v"(p) : "memory");
}
DI void dma16_sc1(unsigned ldsdst, unsigned voff, __amdgpu_buffer_rsrc_t rs, unsigned soff) {
  asm volatile("s_mov_b32 m0, %0\n\ts_nop 0\n\tbuffer_load_dwordx4 %1, %2, %3 offen sc1 lds" ::"s"(ldsdst), "v"(voff), "s"(rs), "s"(soff) : "memory");
}
#define WAITV(n) asm volatile("s_waitcnt vmcnt(" #n ")" ::: "memory")
#define WAIT_BAR(n) asm volatile("s_waitcnt vmcnt(" #n ") lgkmcnt(0)\n\ts_barrier" ::: "memory")
DI __amdgpu_buffer_rsrc_t mkrs(const void* p, unsigned bytes) { return __builtin_amdgcn_make_buffer_rsrc((void*)p, (short)0, (int)bytes, 0x00020000); }
DI unsigned ld32_issue(__amdgpu_buffer_rsrc_t rs, unsigned off) { unsigned r; asm volatile("buffer_load_dword %0, %1, %2, 0 offen sc1" : "=v"(r) : "v"(off), "s"(rs) : "memory"); return r; }
DI unsigned ld32_wait(__amdgpu_buffer_rsrc_t rs, unsigned off) { unsigned r; asm volatile("buffer_load_dword %0, %1, %2, 0 offen sc1\n\ts_waitcnt vmcnt(0)" : "=&v"(r) : "v"(off), "s"(rs) : "memory"); return r; }
DI void dma4_sc1(unsigned ldsdst, unsigned voff, __amdgpu_buffer_rsrc_t rs, unsigned soff) {
  asm volatile("s_mov_b32 m0, %0\n\ts_nop 0\n\tbuffer_load_dword %1, %2, %3 offen sc1 lds" ::"s"(ldsdst), "v"(voff), "s"(rs), "s"(soff) : "memory");
}
#define WAIT_BAR_V(n, x) asm volatile("s_waitcnt vmcnt(" #n ") lgkmcnt(0)\n\ts_barrier" : "+v"(x) :: "memory")
constexpr unsigned OOB = 0x80000000u;
DI void st32(unsigned v, __amdgpu_buffer_rsrc_t rs, unsigned off) { __builtin_amdgcn_raw_buffer_store_b32(v, rs, off, 0, SC1); }

#define STAMP(i) do { if ((i) == 0) dg0 = __builtin_amdgcn_s_memrealtime(); if ((i) == DIAG) dg1 = __builtin_amdgcn_s_memrealtime(); if ((i) == 0 || (i) == DIAG) __builtin_amdgcn_s_waitcnt(0xC07F); } while (0)

constexpr unsigned XST = 32768, WST = 24576, LDS_W = 3 * XST, LDS_SPARE = LDS_W + 2 * WST, LDS_CTL = 159744;
constexpr unsigned LDS_OWN = 32768, LDS_PREV = 98304, LDS_BYTES = 163840;

struct Frag { bf16x8 x[4]; s16x4 wl[6], wh[6]; };

DI float swapmax(float m) {
  auto rr = __builtin_amdgcn_permlane32_swap(__float_as_uint(m), __float_as_uint(m), false, false);
  return __builtin_fmaxf(__uint_as_float(rr[0]), __uint_as_float(rr[1]));
}
DI float swapsum(float m) {
  auto rr = __builtin_amdgcn_permlane32_swap(__float_as_uint(m), __float_as_uint(m), false, false);
  return __uint_as_float(rr[0]) + __uint_as_float(rr[1]);
}
#define MX3(a, b, c) __builtin_fmaxf(__builtin_fmaxf((a), (b)), (c))
DI float max16(const f32x16& p) {
  float a = MX3(p[0], p[1], p[2]), b = MX3(p[3], p[4], p[5]);
  a = MX3(a, p[6], p[7]); b = MX3(b, p[8], p[9]); a = MX3(a, p[10], p[11]); b = MX3(b, p[12], p[13]);
  a = MX3(a, p[14], p[15]);
  return __builtin_fmaxf(a, b);
}
struct QState { bf16x8 q0, q1; f32x16 o; float m, l; };
#define SWAPQ() do { bf16x8 tq = A.q0; A.q0 = B.q0; B.q0 = tq; tq = A.q1; A.q1 = B.q1; B.q1 = tq; f32x16 to = A.o; A.o = B.o; B.o = to; float tf = A.m; A.m = B.m; B.m = tf; tf = A.l; A.l = B.l; B.l = tf; } while (0)
template <int MODE> DI void attn_tile(unsigned ka, QState& Q, f32x16& negm, int rr) {
  const bf16x8 k0 = lds128(ka), k1 = lds128(ka + 1024), v0 = lds128(ka + 32768), v1 = lds128(ka + 32768 + 1024);
  f32x16 s;
  if (MODE == 1) { f32x16 z = {}; s = MFMA(k0, Q.q0, z); } else s = MFMA(k0, Q.q0, negm);
  s = MFMA(k1, Q.q1, s);
  if (MODE != 0) {
#pragma unroll
    for (int i = 0; i < 16; ++i) { const bool le = ((i & 3) + 16 * ((i >> 2) & 1) + 8 * (i >> 3)) <= rr; s[i] = (le == (MODE == 1)) ? s[i] : -__builtin_inff(); }
  }
  float mx = max16(s);
  if (MODE == 1) {
    mx = swapmax(mx); Q.m = mx;
#pragma unroll
    for (int i = 0; i < 16; ++i) { negm[i] = -mx; s[i] -= mx; }
  } else if (__builtin_expect(__any(mx > THR), 0)) {
    mx = swapmax(mx);
    const float d = __builtin_fmaxf(mx, 0.f), f = __builtin_amdgcn_exp2f(-d);
    Q.m += d; Q.l *= f;
#pragma unroll
    for (int i = 0; i < 16; ++i) { negm[i] = -Q.m; s[i] -= d; Q.o[i] *= f; }
  }
#pragma unroll
  for (int i = 0; i < 16; ++i) s[i] = __builtin_amdgcn_exp2f(s[i]);
  float a = s[0] + s[1], b = s[2] + s[3];
#pragma unroll
  for (int i = 4; i < 16; i += 2) { a += s[i]; b += s[i + 1]; }
  if (MODE == 1) Q.l = a + b; else Q.l += a + b;
  const bf16x8 p0 = asb(pack8(s, 0)), p1 = asb(pack8(s, 1));
  if (MODE == 1) { f32x16 z = {}; Q.o = MFMA(v0, p0, z); } else Q.o = MFMA(v0, p0, Q.o);
  Q.o = MFMA(v1, p1, Q.o);
}

__global__ __launch_bounds__(512, 2) void k_fused(const void* xA, const void* wA, const void* woA, const void* xB, const void* wB, const void* woB,
                                                  void* ws, void* out) {
  __shared__ __attribute__((aligned(1024))) char smem[LDS_BYTES];
  const unsigned lds0 = (unsigned)(size_t)(LAS char*)smem;
  const unsigned bid = blockIdx.x, xcd = bid & 7, idx = bid >> 3;
  const unsigned br = xcd >> 2, R = RMAP, hp = HPMAP;
  const unsigned L = (br * 8 + R) * 16 + hp;
  const unsigned G = xcd * 2 + (idx >> 4), ns = idx & 15;
  const unsigned tid = threadIdx.x, l = tid & 63, w = rfl(tid >> 6), wm = w & 3, wn = w >> 2;
  const __amdgpu_buffer_rsrc_t rx = mkrs(br ? xB : xA, NOMEMX * S_ * D_ * 2), rw = mkrs(br ? wB : wA, D_ * 3 * D_ * 2), rws = mkrs(ws, WS_END);
  unsigned long long dg0 = 0, dg1 = 0;
  STAMP(0);
  const unsigned fl_id = l < 32 ? G * 16 + (l >> 1) : (l == 32 ? L - 16 : L);
  const bool fl_ok = l < 34 && !(l == 32 && R == 0);
  unsigned obase = ld32_issue(rws, fl_ok ? WS_FLAG + fl_id * 128 : OOB);
  const unsigned mtw = (w & 1) ? 7 - (w >> 1) : (w >> 1);
  const unsigned xv0 = (R * 256 + mtw * 32 + (l >> 3)) * 2048 + (((l & 7) ^ (l >> 4)) << 4);
  const unsigned xv1 = (R * 256 + mtw * 32 + 8 + (l >> 3)) * 2048 + (((l & 7) ^ (4 + (l >> 4))) << 4);
  const unsigned wv = (8 * w + (l >> 3)) * 6144 + (((l & 7) ^ ((((l >> 4) & 1) | ((w & 1) << 1)) << 1)) << 4) + hp * 128;
  const unsigned xdst = lds0 + w * 4096, wdst = lds0 + LDS_W + w * 1024;
#define DMA_X(t, st) do { const unsigned so_ = (t) * 128u, d_ = xdst + (st); \
    dma16x(d_, xv0, rx, so_); dma16x(d_ + 1024, xv1, rx, so_); dma16x(d_ + 2048, xv0, rx, so_ + 32768); dma16x(d_ + 3072, xv1, rx, so_ + 32768); } while (0)
#define DMA_W(t, st) do { const unsigned so_ = (t) * (64u * 6144u), d_ = wdst + (st); \
    dma16w(d_, wv, rw, so_); dma16w(d_ + 8192, wv, rw, so_ + 2048); dma16w(d_ + 16384, wv, rw, so_ + 4096); } while (0)
  DMA_W(0, 0); DMA_X(0, 0); DMA_W(1, WST); DMA_X(1, XST);
  const unsigned pl = PFH == 2 ? (l >> 1) : l, pli = pl < 10 ? pl : 0, phalf = PFH == 2 ? (l & 1) * 64 : 0;
  const unsigned pwl = ((idx & 3) * 8 + w) * 6 + (pli - 4);
  const char* pfp = pli < 4 ? (const char*)(br ? xB : xA) + (R * 256 + ((idx >> 2) * 8 + w) * 4 + pli) * 2048 + (PFD - 1) * 128 + phalf
                            : (const char*)(br ? wB : wA) + (pwl / 3 + (PFD - 1) * 64) * 6144 + (pwl % 3) * 2048 + hp * 128 + phalf;
  const unsigned pstep = pli < 4 ? 128u : 64u * 6144u;
  pf4(lds0 + LDS_SPARE + 8192, pfp); pfp += pstep;
  const unsigned c = l & 15, g = l >> 4, q4 = c >> 2;
  const unsigned xa = lds0 + wm * 8192 + c * 128 + ((g ^ (c >> 1)) << 4);
  const unsigned wa = lds0 + LDS_W + (8 * g + q4) * 128 + (((2 * wn) ^ ((q4 >> 1) | ((g & 1) << 1))) << 5) + (l & 3) * 8;
  f32x4 acc[6][4] = {};
  Frag A, B;
#define RDX(F, xb, s, u) F.x[u] = lds128(((xb) ^ ((s) << 6)) + (u) * 2048)
#define MM(C, n, u) do { const bf16x8 wf_ = cat(C.wl[n], C.wh[n]); if ((n) < 4) MFA(acc[n][u], wf_, C.x[u]); else MFA(acc[n][u], C.x[u], wf_); } while (0)
#define RDWL(F, wb, s, n) F.wl[n] = ldstr(((wb) ^ (((n) & 1) << 5)) + ((n) >> 1) * 8192 + (s) * 4096)
#define RDWH(F, wb, s, n) F.wh[n] = ldstr(((wb) ^ (((n) & 1) << 5)) + ((n) >> 1) * 8192 + (s) * 4096 + 512)
#define RD1(F, xb, wb, s, r) do { if ((r) == 0) RDX(F, xb, s, 0); else if ((r) == 1) RDWL(F, wb, s, 0); else if ((r) == 2) RDWH(F, wb, s, 0); \
    else if ((r) < 6) RDX(F, xb, s, ((r) - 2) & 3); else if (((r) & 1) == 0) RDWL(F, wb, s, (((r) + 8) >> 1) % 6); else RDWH(F, wb, s, (((r) + 8) >> 1) % 6); } while (0)
#define PB 27
#define PFP 36
#define BP(r) ((r) * 3 / 2)
#define AP(r) (29 + (r) * 6 / 5)
#define XP(j) (2 + (j) * 6)
#define WP(j) (28 + (j) * 3)
#define TILE(LDA, DMA, WAITB) do { \
    const unsigned xb = xa + xs, wb = wa + wsx, wd = wdst + wsx; \
    xp = xs; xs = (xs == 2 * XST) ? 0 : xs + XST; wsx ^= WST; \
    const unsigned xn = xa + xs, wn_ = wa + wsx; \
    sfor<0, 48>([&](auto mi) AI { constexpr int m_ = decltype(mi)::v; \
      if constexpr (m_ < 24) MM(A, m_ >> 2, m_ & 3); else MM(B, (m_ - 24) >> 2, m_ & 3); \
      sfor<0, 16>([&](auto ri) AI { constexpr int r_ = decltype(ri)::v; \
        if constexpr (BP(r_) == m_) RD1(B, xb, wb, 1, r_); if constexpr (LDA && AP(r_) == m_) RD1(A, xn, wn_, 0, r_); }); \
      sfor<0, 4>([&](auto ji) AI { constexpr int j_ = decltype(ji)::v; \
        if constexpr (DMA && XP(j_) == m_) dma16x(xd + j_ * 1024, (j_ & 1) ? xv1 : xv0, rx, xso + (j_ >> 1) * 32768); \
        if constexpr (DMA && j_ < 3 && WP(j_) == m_) dma16w(wd + j_ * 8192, wv, rw, wso + j_ * 2048); }); \
      if constexpr (DMA && m_ == PFP) { pf4(lds0 + LDS_SPARE + 8192, pfp); pfp += pinc; } \
      SBAR(); \
      if constexpr (m_ == PB) { WAITB; SBAR(); } }); } while (0)
  WAIT_BAR_V(8, obase);
  STAMP(1);
  unsigned xs = 0, wsx = 0, xp = 2 * XST;
  sfor<0, 16>([&](auto ri) AI { constexpr int r_ = decltype(ri)::v; RD1(A, xa, wa, 0, r_); SBAR(); });
  if (w == 0) st32(obase ^ ACKX, rws, (fl_ok && l <= 32 && !(l & 1)) ? WS_ACK + fl_id * 128 + (l < 32 ? ns : 16) * 4 : OOB);
  const unsigned pbase = __builtin_amdgcn_readlane(obase, 32), mybase = __builtin_amdgcn_readlane(obase, 33);
  _Pragma("unroll") for (int i_ = 0; i_ < 24; ++i_) PIN(acc[i_ >> 2][i_ & 3]);
  asm volatile("s_nop 1" ::: "memory");
  SBAR();
#pragma unroll 1
  for (int t = 0; t < 15; ++t) {
    const unsigned xso = t < 14 ? (t + 2) * 128u : 0x80000000u, wso = t < 14 ? (t + 2) * (64u * 6144u) : OOB;
    const unsigned xd = t < 14 ? xdst + xp : lds0 + LDS_SPARE;
    const unsigned pinc = t + PFD < 15 ? pstep : 0u;
    TILE(true, true, WAIT_BAR(5));
  }
  const bool ack_ok = l < 17 && !(l == 16 && R == 7);
  const unsigned ackoff = ack_ok ? WS_ACK + L * 128 + l * 4 : OOB;
  unsigned ackv = ld32_issue(rws, ackoff);
  { const unsigned xso = 0, wso = 0, xd = 0, pinc = 0; TILE(false, false, (void)0); }
  asm volatile("s_nop 7" ::: "memory");
  _Pragma("unroll") for (int i_ = 0; i_ < 24; ++i_) PIN(acc[i_ >> 2][i_ & 3]);
  STAMP(2);
  QState QA, QB;
  {
    const unsigned ko = (g & 1) * 1024 + ((g >> 1) * 32 + (c & 7) + 16 * (c >> 3)) * 16;
    const unsigned vo = 32768 + (g >> 1) * 1024 + ((g & 1) * 32 + c) * 16;
    const unsigned t0 = wn * 16384 + wm * IMG, t1 = wn * 16384 + (7 - wm) * IMG;
    const unsigned gs = R < 7 ? WS_KV + L * 65536 : 0x80000000u;
#pragma unroll
    for (int u = 0; u < 4; ++u) {
      const unsigned tb = u < 2 ? t0 : t1;
      const u32x4 kk = pack44(acc[2][u], acc[3][u]), vv = pack44(acc[4 + (u & 1)][u & 2], acc[4 + (u & 1)][(u & 2) + 1]);
      __builtin_amdgcn_raw_buffer_store_b128(kk, rws, ko + (u & 1) * 128, gs + tb, SC1);
      __builtin_amdgcn_raw_buffer_store_b128(vv, rws, vo + (u & 1) * 256, gs + tb, SC1);
      sts128(lds0 + LDS_OWN + tb + ko + (u & 1) * 128, kk);
      sts128(lds0 + LDS_OWN + tb + vo + (u & 1) * 256, vv);
    }
    u32x4 qf[4];
#pragma unroll
    for (int u = 0; u < 4; ++u) qf[u] = pack44(acc[0][u] * QSCALE, acc[1][u] * QSCALE);
#pragma unroll
    for (int p = 0; p < 2; ++p)
#pragma unroll
      for (int i = 0; i < 4; ++i) { auto r = __builtin_amdgcn_permlane16_swap(qf[2 * p][i], qf[2 * p + 1][i], false, false); qf[2 * p][i] = r[0]; qf[2 * p + 1][i] = r[1]; }
    QA.q0 = asb(qf[2]); QA.q1 = asb(qf[3]); QB.q0 = asb(qf[0]); QB.q1 = asb(qf[1]);
  }
  WAIT_BAR_V(8, ackv);
  STAMP(3);
  bool acked = __all(!ack_ok || ackv == (mybase ^ ACKX));
  const __amdgpu_buffer_rsrc_t rwo = mkrs((G >> 3) ? woB : woA, D_ * D_ * 2);
  const unsigned wov = (8 * w + (l >> 3)) * 2048 + (((l & 7) ^ (((l >> 4) & 3) << 1)) << 4) + ns * 128;
#define DMA_WO(j) dma16(lds0 + (j) * 8192 + w * 1024, wov, rwo, (j) * (64u * 2048u))
  DMA_WO(0); DMA_WO(1); DMA_WO(2); DMA_WO(3);
  const int rr = (int)(l & 31) - 4 * (int)(l >> 5);
  const unsigned kb = lds0 + LDS_OWN + wn * 16384 + l * 16;
  const unsigned myflag = WS_FLAG + L * 128 + w * 4;
  f32x16 negm;
#define A QA
#define B QB
  A.o = f32x16{}; B.o = f32x16{}; A.m = B.m = A.l = B.l = 0.f;
  const unsigned pfoff = WS_FLAG + (L - 16) * 128 + w * 4;
  bool have_prev = false;
  const unsigned kvsrc = WS_KV + (L - 16) * 65536 + wn * 16384, kvdst = lds0 + LDS_PREV + wn * 16384;
#define DMA_PREV() do { _Pragma("unroll") for (int j_ = 0; j_ < 8; ++j_) { const unsigned o_ = (j_ & 1) * 1024 + ((j_ >> 1) & 1) * 32768 + ((j_ & 4) ? (7 - wm) : wm) * IMG; \
      dma16_sc1(kvdst + o_, l * 16, rws, kvsrc + o_); } } while (0)
#define SIGPOS 2
#pragma unroll 1
  for (int qi = 0; qi < 2; ++qi) {
    const int a = qi ? wm : 7 - wm;
    attn_tile<1>(kb + a * IMG, A, negm, rr);
#pragma unroll 1
    for (int j = 0; j < a; ++j) {
      attn_tile<0>(kb + j * IMG, A, negm, rr);
      if (qi == 0 && j == SIGPOS) {
        WAITV(0);
#pragma unroll 1
        for (unsigned sp = 0; !acked && sp < SPIN_CAP; ++sp) { __builtin_amdgcn_s_sleep(2); acked = __all(!ack_ok || ld32_wait(rws, ackoff) == (mybase ^ ACKX)); }
        if (l == 0) st32(mybase + 1, rws, myflag);
        STAMP(4);
        if (R > 0) dma4_sc1(kvdst + wm * IMG, pfoff, rws, 0);
      }
    }
    SWAPQ();
    if (qi == 0 && R > 0) {
      WAITV(0);
      const unsigned pf = rfl(*(volatile LAS unsigned*)(size_t)(kvdst + wm * IMG));
      if (pf - pbase - 1u < 2u) { DMA_PREV(); have_prev = true; }
    }
  }
  STAMP(5);
  if (R > 0 && !have_prev) {
#pragma unroll 1
    for (unsigned sp = 0; sp < SPIN_CAP; ++sp) {
      if (rfl(ld32_wait(rws, pfoff)) - pbase - 1u < 2u) break;
      __builtin_amdgcn_s_sleep(1);
    }
    DMA_PREV();
  }
  STAMP(6);
  WAIT_BAR(0);
  STAMP(7);
#pragma unroll
  for (int j = 4; j < 12; ++j) DMA_WO(j);
  if (R > 0) {
#pragma unroll 1
    for (int qi = 0; qi < 2; ++qi) {
      const int a = qi ? wm : 7 - wm;
#pragma unroll
      for (int i = 0; i < 16; ++i) negm[i] = -A.m;
      attn_tile<2>(kb + 65536 + a * IMG, A, negm, rr);
#pragma unroll 1
      for (int j = a + 1; j < 8; ++j) attn_tile<0>(kb + 65536 + j * IMG, A, negm, rr);
      SWAPQ();
    }
  }
  STAMP(8);
  { const float ia = __builtin_amdgcn_rcpf(swapsum(A.l)), ib = __builtin_amdgcn_rcpf(swapsum(B.l));
    A.o *= ia; B.o *= ib;
    const unsigned hh = hp * 2 + wn, T0 = br * 64 + R * 8 + wm, T1 = br * 64 + R * 8 + 7 - wm;
    const unsigned ol = ((l >> 4) & 1) * 1024 + ((l >> 5) * 16 + (l & 15)) * 16;
#pragma unroll
    for (int s = 0; s < 2; ++s) {
      __builtin_amdgcn_raw_buffer_store_b128(pack8(A.o, s), rws, ol + s * 512, WS_O + (T1 * 32 + hh) * IMG, SC1);
      __builtin_amdgcn_raw_buffer_store_b128(pack8(B.o, s), rws, ol + s * 512, WS_O + (T0 * 32 + hh) * IMG, SC1);
    } }
#undef A
#undef B
  WAITV(0);
  if (l == 0) st32(mybase + 2, rws, myflag);
  STAMP(9);
  asm volatile("s_barrier" ::: "memory");
  DMA_WO(12); DMA_WO(13); DMA_WO(14); DMA_WO(15);
  { const unsigned pw = (w < 4 ? w : 7 - w) + (l & 1) * 4;
    const unsigned fo = WS_FLAG + (G * 16 + ((l & 31) >> 1)) * 128 + pw * 4;
#pragma unroll 1
    for (unsigned sp = 0; sp < SPIN_CAP; ++sp) {
      const unsigned f = ld32_wait(rws, fo);
      if (__all(l >= 32 || f == obase + 2)) break;
      __builtin_amdgcn_s_sleep(1);
    } }
  STAMP(10);
  const unsigned T = G * 8 + w;
  const unsigned ob = WS_O + T * 32 * IMG;
  const unsigned wa2 = lds0 + (16 * (g >> 1) + 4 * (g & 1) + q4) * 128 + ((2 * (g & 1) + (q4 >> 1)) << 5) + (l & 3) * 8;
  f32x4 oc[4][2] = {};
  u32x4 of[16];
  s16x4 wl[2][4], wh[2][4];
#pragma unroll
  for (int i = 0; i < 16; ++i) of[i] = __builtin_amdgcn_raw_buffer_load_b128(rws, l * 16 + (i & 3) * 1024, ob + (i >> 2) * 4096, SC1);
#pragma unroll
  for (int nt = 0; nt < 4; ++nt) { wl[0][nt] = ldstr(wa2 ^ (nt << 5)); wh[0][nt] = ldstr((wa2 ^ (nt << 5)) + 1024); }
  _Pragma("unroll") for (int i_ = 0; i_ < 8; ++i_) PIN(oc[i_ >> 1][i_ & 1]);
  asm volatile("s_nop 1" ::: "memory");
  SBAR();
#pragma unroll 1
  for (int gq = 0; gq < 4; ++gq) {
    if (gq == 2) asm volatile("s_barrier" ::: "memory");
    const unsigned wb = wa2 + gq * 32768;
    const unsigned on = gq < 3 ? ob + (gq + 1) * 16384 : 0x80000000u;
    sfor<0, 8>([&](auto hi) AI { constexpr int h_ = decltype(hi)::v, cur = h_ & 1, nxt = cur ^ 1;
      sfor<0, 8>([&](auto mi) AI { constexpr int m_ = decltype(mi)::v, nt = m_ >> 1, tt = m_ & 1;
        { const bf16x8 wf = cat(wl[cur][nt], wh[cur][nt]); MFA(oc[nt][tt], wf, of[2 * h_ + tt]); }
        if constexpr (tt == 0) wl[nxt][nt] = ldstr((wb ^ (nt << 5)) + (h_ + 1) * 4096); else wh[nxt][nt] = ldstr((wb ^ (nt << 5)) + (h_ + 1) * 4096 + 1024);
        if constexpr (m_ >= 6) of[2 * h_ + tt] = __builtin_amdgcn_raw_buffer_load_b128(rws, l * 16 + ((2 * h_ + tt) & 3) * 1024, on + ((2 * h_ + tt) >> 2) * 4096, SC1);
        SBAR(); }); });
  }
  asm volatile("s_nop 7" ::: "memory");
  _Pragma("unroll") for (int i_ = 0; i_ < 8; ++i_) PIN(oc[i_ >> 1][i_ & 1]);
  STAMP(11);
  const __amdgpu_buffer_rsrc_t rout = mkrs(out, S_ * 2 * D_ * 2);
  const unsigned orow = (((G & 7) * 8 + w) * 32 + c) * 4096 + (G >> 3) * 2048 + ns * 128 + (g & 1) * 32 + (g >> 1) * 16;
#pragma unroll
  for (int tt = 0; tt < 2; ++tt) {
#pragma unroll
    for (int np = 0; np < 2; ++np) {
      const f32x4 &a = oc[2 * np][tt], &b = oc[2 * np + 1][tt];
      auto r0 = __builtin_amdgcn_permlane16_swap(pk2(a[0], a[1]), pk2(b[0], b[1]), false, false);
      auto r1 = __builtin_amdgcn_permlane16_swap(pk2(a[2], a[3]), pk2(b[2], b[3]), false, false);
      u32x4 v = {r0[0], r1[0], r0[1], r1[1]};
      __builtin_amdgcn_raw_buffer_store_b128(v, rout, orow + tt * 65536 + np * 64, 0, 0);
    }
  }
  { WAITV(0); STAMP(12); const unsigned long long te = __builtin_amdgcn_s_memrealtime() + (dg1 - dg0); while (__builtin_amdgcn_s_memrealtime() < te) __builtin_amdgcn_s_sleep(1); }
}

extern "C" void kernel_launch(void* const* d_in, const int* in_sizes, int n_in, void* d_out, int out_size, void* d_ws, size_t ws_size,
                              hipStream_t stream) {
  k_fused<<<256, 512, 0, stream>>>(d_in[0], d_in[1], d_in[2], d_in[3], d_in[4], d_in[5], (char*)d_ws + ((ws_size - WS_END) & ~(size_t)0xfffff), d_out);
}
```

```cpp
#include <hip/hip_runtime.h>
#include <hip/hip_bf16.h>

typedef short bf16x8 __attribute__((ext_vector_type(8)));
typedef short s16x4 __attribute__((ext_vector_type(4)));
typedef float f32x16 __attribute__((ext_vector_type(16)));
typedef float f32x4 __attribute__((ext_vector_type(4)));
typedef unsigned u32x4 __attribute__((ext_vector_type(4)));
typedef __bf16 bf16x2_t __attribute__((ext_vector_type(2)));
typedef float f32x2 __attribute__((ext_vector_type(2)));
#define LAS __attribute__((address_space(3)))
#define DI __device__ __forceinline__
#define AI __attribute__((always_inline))
#define MFMA(a, b, c) __builtin_amdgcn_mfma_f32_32x32x16_bf16((a), (b), (c), 0, 0, 0)
#define SBAR() __builtin_amdgcn_sched_barrier(0)
#define PIN(x) asm volatile("" : "+v"(x))
#define MFA(acc_, a_, b_) asm volatile("v_mfma_f32_16x16x32_bf16 %0, %1, %2, %0" : "+v"(acc_) : "v"(a_), "v"(b_))

constexpr int S_ = 2048, D_ = 1024;
constexpr float QSCALE = 0.17677669529663687f * 1.4426950408889634f;
constexpr float PBOUND = 1073741824.0f;
constexpr unsigned IMG = 2048;
constexpr unsigned WS_FLAG = 0, WS_ACK = 32768, WS_KV = 65536, WS_O = WS_KV + 256u * 65536u, WS_END = WS_O + 2u * 64u * 32u * IMG;
constexpr unsigned ACKX = 0x5bd1e995u;
constexpr int SC1 = 16;
constexpr unsigned CODE_BYTES = 18048;
constexpr unsigned SPIN_CAP = 1u << 18;

#define HPMAP ((xcd & 1) * 8 + (idx >> 2))
#define RMAP (((xcd >> 1) & 1) * 4 + (idx & 3))
#define NOMEMX 1
template <int I> struct IC { static constexpr int v = I; };
template <int B, int E, class F> DI void sfor(F&& f) { if constexpr (B < E) { f(IC<B>{}); sfor<B + 1, E>(f); } }
DI unsigned rfl(unsigned x) { return (unsigned)__builtin_amdgcn_readfirstlane((int)x); }
DI unsigned pk2(float lo, float hi) { f32x2 v = {lo, hi}; return __builtin_bit_cast(unsigned, __builtin_convertvector(v, bf16x2_t)); }
DI u32x4 pack8(const f32x16& x, int s) {
  u32x4 p;
  p[0] = pk2(x[8 * s + 0], x[8 * s + 1]); p[1] = pk2(x[8 * s + 2], x[8 * s + 3]);
  p[2] = pk2(x[8 * s + 4], x[8 * s + 5]); p[3] = pk2(x[8 * s + 6], x[8 * s + 7]);
  return p;
}
DI u32x4 pack44(const f32x4& a, const f32x4& b) { u32x4 p; p[0] = pk2(a[0], a[1]); p[1] = pk2(a[2], a[3]); p[2] = pk2(b[0], b[1]); p[3] = pk2(b[2], b[3]); return p; }
DI bf16x8 asb(u32x4 v) { return __builtin_bit_cast(bf16x8, v); }
DI bf16x8 lds128(unsigned a) { return *(const LAS bf16x8*)(size_t)a; }
DI void sts128(unsigned a, u32x4 v) { *(LAS u32x4*)(size_t)a = v; }
DI s16x4 ldstr(unsigned a) { return __builtin_amdgcn_ds_read_tr16_b64_v4i16((LAS s16x4*)(size_t)a); }
DI bf16x8 cat(s16x4 lo, s16x4 hi) { return __builtin_shufflevector(lo, hi, 0, 1, 2, 3, 4, 5, 6, 7); }
DI void dma16(unsigned ldsdst, unsigned voff, __amdgpu_buffer_rsrc_t rs, unsigned soff) {
  asm volatile("s_mov_b32 m0, %0\n\ts_nop 0\n\tbuffer_load_dwordx4 %1, %2, %3 offen lds" ::"s"(ldsdst), "v"(voff), "s"(rs), "s"(soff) : "memory");
}
#define XPOL ""
#define WPOL ""
DI void dma16x(unsigned ldsdst, unsigned voff, __amdgpu_buffer_rsrc_t rs, unsigned soff) {
  asm volatile("s_mov_b32 m0, %0\n\ts_nop 0\n\tbuffer_load_dwordx4 %1, %2, %3 offen " XPOL " lds" ::"s"(ldsdst), "v"(voff), "s"(rs), "s"(soff) : "memory");
}
DI void dma16w(unsigned ldsdst, unsigned voff, __amdgpu_buffer_rsrc_t rs, unsigned soff) {
  asm volatile("s_mov_b32 m0, %0\n\ts_nop 0\n\tbuffer_load_dwordx4 %1, %2, %3 offen " WPOL " lds" ::"s"(ldsdst), "v"(voff), "s"(rs), "s"(soff) : "memory");
}
#define PPOL ""
#define PFD 4
#define PFH 1
DI void pf4(unsigned ldsdst, const char* p) {
  asm volatile("s_mov_b32 m0, %0\n\ts_nop 0\n\tglobal_load_lds_dword %1, off " PPOL ::"s"(ldsdst), "v"(p) : "memory");
}
DI void dma16_sc1(unsigned ldsdst, unsigned voff, __amdgpu_buffer_rsrc_t rs, unsigned soff) {
  asm volatile("s_mov_b32 m0, %0\n\ts_nop 0\n\tbuffer_load_dwordx4 %1, %2, %3 offen sc1 lds" ::"s"(ldsdst), "v"(voff), "s"(rs), "s"(soff) : "memory");
}
#define WAITV(n) asm volatile("s_waitcnt vmcnt(" #n ")" ::: "memory")
#define WAIT_BAR(n) asm volatile("s_waitcnt vmcnt(" #n ") lgkmcnt(0)\n\ts_barrier" ::: "memory")
DI __amdgpu_buffer_rsrc_t mkrs(const void* p, unsigned bytes) { return __builtin_amdgcn_make_buffer_rsrc((void*)p, (short)0, (int)bytes, 0x00020000); }
DI unsigned ld32_issue(__amdgpu_buffer_rsrc_t rs, unsigned off) { unsigned r; asm volatile("buffer_load_dword %0, %1, %2, 0 offen sc1" : "=v"(r) : "v"(off), "s"(rs) : "memory"); return r; }
DI unsigned ld32_wait(__amdgpu_buffer_rsrc_t rs, unsigned off) { unsigned r; asm volatile("buffer_load_dword %0, %1, %2, 0 offen sc1\n\ts_waitcnt vmcnt(0)" : "=&v"(r) : "v"(off), "s"(rs) : "memory"); return r; }
DI void dma4(unsigned ldsdst, unsigned voff, __amdgpu_buffer_rsrc_t rs, unsigned soff) {
  asm volatile("s_mov_b32 m0, %0\n\ts_nop 0\n\tbuffer_load_dword %1, %2, %3 offen lds" ::"s"(ldsdst), "v"(voff), "s"(rs), "s"(soff) : "memory");
}
DI void dma4_sc1(unsigned ldsdst, unsigned voff, __amdgpu_buffer_rsrc_t rs, unsigned soff) {
  asm volatile("s_mov_b32 m0, %0\n\ts_nop 0\n\tbuffer_load_dword %1, %2, %3 offen sc1 lds" ::"s"(ldsdst), "v"(voff), "s"(rs), "s"(soff) : "memory");
}
#define WAIT_BAR_V(n, x) asm volatile("s_waitcnt vmcnt(" #n ") lgkmcnt(0)\n\ts_barrier" : "+v"(x) :: "memory")
constexpr unsigned OOB = 0x80000000u;
DI void st32(unsigned v, __amdgpu_buffer_rsrc_t rs, unsigned off) { __builtin_amdgcn_raw_buffer_store_b32(v, rs, off, 0, SC1); }

#define STAMP(i) do {} while (0)

constexpr unsigned XST = 32768, WST = 24576, LDS_W = 3 * XST, LDS_SPARE = LDS_W + 2 * WST, LDS_CTL = 159744;
static_assert(PFD == 4, "prefetch distance");
constexpr unsigned LDS_OWN = 32768, LDS_PREV = 98304, LDS_BYTES = 163840;

struct Frag { bf16x8 x[4]; s16x4 wl[6], wh[6]; };

DI float swapmax(float m) {
  auto rr = __builtin_amdgcn_permlane32_swap(__float_as_uint(m), __float_as_uint(m), false, false);
  return __builtin_fmaxf(__uint_as_float(rr[0]), __uint_as_float(rr[1]));
}
DI float swapsum(float m) {
  auto rr = __builtin_amdgcn_permlane32_swap(__float_as_uint(m), __float_as_uint(m), false, false);
  return __uint_as_float(rr[0]) + __uint_as_float(rr[1]);
}
#define MX3(a, b, c) __builtin_fmaxf(__builtin_fmaxf((a), (b)), (c))
DI float max16(const f32x16& p) {
  float a = MX3(p[0], p[1], p[2]), b = MX3(p[3], p[4], p[5]);
  a = MX3(a, p[6], p[7]); b = MX3(b, p[8], p[9]); a = MX3(a, p[10], p[11]); b = MX3(b, p[12], p[13]);
  a = MX3(a, p[14], p[15]);
  return __builtin_fmaxf(a, b);
}
DI int keyidx(int i) { return (i & 3) + 16 * ((i >> 2) & 1) + 8 * (i >> 3); }
DI void redo_tile(unsigned tile, bf16x8 q0, bf16x8 q1, bf16x8 sel, f32x16& o, f32x16& negm, float& l, u32x4& p0, u32x4& p1, f32x4& L) {
  int ln = threadIdx.x & 63; PIN(ln);
  const unsigned ka = (tile & ~3u) + ln * 16, mode = tile & 3u;
  const bf16x8 k0 = lds128(ka), k1 = lds128(ka + 1024);
  const int rr = (ln & 31) - 4 * (ln >> 5);
  f32x16 s;
#pragma unroll
  for (int i = 0; i < 16; ++i) s[i] = (mode != 0 && (keyidx(i) <= rr) == (mode == 2)) ? -__builtin_inff() : negm[0];
  s = MFMA(k0, q0, s); s = MFMA(k1, q1, s);
  const float d = __builtin_fmaxf(swapmax(max16(s)), 0.f), f = __builtin_amdgcn_exp2f(-d), nm = negm[0] - d;
  l *= f;
#pragma unroll
  for (int i = 0; i < 16; ++i) { negm[i] = nm; o[i] *= f; s[i] = __builtin_amdgcn_exp2f(s[i] - d); }
  p0 = pack8(s, 0); p1 = pack8(s, 1);
  f32x4 z = {};
  L = __builtin_amdgcn_mfma_f32_16x16x32_bf16(sel, asb(p0), z, 0, 0, 0);
  L = __builtin_amdgcn_mfma_f32_16x16x32_bf16(sel, asb(p1), L, 0, 0, 0);
  float t = L[0]; PIN(t); L[0] = t;
  PIN(p0); PIN(p1); PIN(o); PIN(negm);
  asm volatile("s_nop 1" ::: "memory");
}

__global__ __launch_bounds__(512, 2) void k_fused(const void* xA, const void* wA, const void* woA, const void* xB, const void* wB, const void* woB,
                                                  void* ws, void* out) {
  __shared__ __attribute__((aligned(1024))) char smem[LDS_BYTES];
  const unsigned lds0 = (unsigned)(size_t)(LAS char*)smem;
  const unsigned bid = blockIdx.x, xcd = bid & 7, idx = bid >> 3;
  const unsigned br = xcd >> 2, R = RMAP, hp = HPMAP;
  const unsigned L = (br * 8 + R) * 16 + hp;
  const unsigned G = xcd * 2 + (idx >> 4), ns = idx & 15;
  const unsigned tid = threadIdx.x, l = tid & 63, w = rfl(tid >> 6), wm = w & 3, wn = w >> 2;
  const __amdgpu_buffer_rsrc_t rx = mkrs(br ? xB : xA, NOMEMX * S_ * D_ * 2), rw = mkrs(br ? wB : wA, D_ * 3 * D_ * 2), rws = mkrs(ws, WS_END);
  STAMP(0);
  const unsigned fl_id = l < 32 ? G * 16 + (l >> 1) : (l == 32 ? L - 16 : L);
  const bool fl_ok = l < 34 && !(l == 32 && R == 0);
  unsigned obase = ld32_issue(rws, fl_ok ? WS_FLAG + fl_id * 128 : OOB);
  const unsigned mtw = w, mp = wn ? 3 - wm : wm;
  const unsigned xv0 = (R * 256 + mtw * 32 + (l >> 3)) * 2048 + (((l & 7) ^ (l >> 4)) << 4);
  const unsigned xv1 = (R * 256 + mtw * 32 + 8 + (l >> 3)) * 2048 + (((l & 7) ^ (4 + (l >> 4))) << 4);
  const unsigned wv = (8 * w + (l >> 3)) * 6144 + (((l & 7) ^ ((((l >> 4) & 1) | ((w & 1) << 1)) << 1)) << 4) + hp * 128;
  const unsigned xdst = lds0 + w * 4096, wdst = lds0 + LDS_W + w * 1024;
#define DMA_X(t, st) do { const unsigned so_ = (t) * 128u, d_ = xdst + (st); \
    dma16x(d_, xv0, rx, so_); dma16x(d_ + 1024, xv1, rx, so_); dma16x(d_ + 2048, xv0, rx, so_ + 32768); dma16x(d_ + 3072, xv1, rx, so_ + 32768); } while (0)
#define DMA_W(t, st) do { const unsigned so_ = (t) * (64u * 6144u), d_ = wdst + (st); \
    dma16w(d_, wv, rw, so_); dma16w(d_ + 8192, wv, rw, so_ + 2048); dma16w(d_ + 16384, wv, rw, so_ + 4096); } while (0)
  DMA_W(0, 0); DMA_X(0, 0); DMA_W(1, WST); DMA_X(1, XST);
  const unsigned pl = PFH == 2 ? (l >> 1) : l, pli = pl < 10 ? pl : 0, phalf = PFH == 2 ? (l & 1) * 64 : 0;
  const unsigned pwl = ((idx & 3) * 8 + w) * 6 + (pli - 4);
  const char* pfp = pli < 4 ? (const char*)(br ? xB : xA) + (R * 256 + ((idx >> 2) * 8 + w) * 4 + pli) * 2048 + (PFD - 1) * 128 + phalf
                            : (const char*)(br ? wB : wA) + (pwl / 3 + (PFD - 1) * 64) * 6144 + (pwl % 3) * 2048 + hp * 128 + phalf;
  unsigned pstep = pli < 4 ? 128u : 64u * 6144u;
  pf4(lds0 + LDS_SPARE + 8192, pfp); pfp += pstep;
  { unsigned long long pc; asm volatile("s_getpc_b64 %0" : "=s"(pc));
    const unsigned co = (w * 64 + l) * 128;
    pf4(lds0 + LDS_SPARE + 8192, (const char*)(pc & ~127ull) + (co < CODE_BYTES ? co : 0)); }
  const unsigned c = l & 15, g = l >> 4, q4 = c >> 2;
  const unsigned xa = lds0 + mp * 8192 + c * 128 + ((g ^ (c >> 1)) << 4);
  const unsigned wa = lds0 + LDS_W + (8 * g + q4) * 128 + (((2 * wn) ^ ((q4 >> 1) | ((g & 1) << 1))) << 5) + (l & 3) * 8;
  f32x4 acc[6][4] = {};
  u32x4 mk0, mk1; unsigned sel1 = ((c & 3) == 0 && ((c >> 2) & 1) == (g & 1)) ? 0x3F803F80u : 0u;
  { const int rr = (int)(l & 31) - 4 * (int)(l >> 5);
#pragma unroll
    for (int i = 0; i < 4; ++i) {
      mk0[i] = (keyidx(2 * i) <= rr ? 0xFFFFu : 0u) | (keyidx(2 * i + 1) <= rr ? 0xFFFF0000u : 0u);
      mk1[i] = (keyidx(8 + 2 * i) <= rr ? 0xFFFFu : 0u) | (keyidx(9 + 2 * i) <= rr ? 0xFFFF0000u : 0u); }
    PIN(mk0); PIN(mk1); PIN(sel1); }
  f32x16 oa, ob; f32x4 Ls; u32x4 p0, p1; bf16x8 v0, v1, sel;
  Frag A, B;
#define RDX(F, xb, s, u) F.x[u] = lds128(((xb) ^ ((s) << 6)) + (u) * 2048)
#define MM(C, n, u) do { const bf16x8 wf_ = cat(C.wl[n], C.wh[n]); if ((n) < 4) MFA(acc[n][u], wf_, C.x[u]); else MFA(acc[n][u], C.x[u], wf_); } while (0)
#define RDWL(F, wb, s, n) F.wl[n] = ldstr(((wb) ^ (((n) & 1) << 5)) + ((n) >> 1) * 8192 + (s) * 4096)
#define RDWH(F, wb, s, n) F.wh[n] = ldstr(((wb) ^ (((n) & 1) << 5)) + ((n) >> 1) * 8192 + (s) * 4096 + 512)
#define RD1(F, xb, wb, s, r) do { if ((r) == 0) RDX(F, xb, s, 0); else if ((r) == 1) RDWL(F, wb, s, 0); else if ((r) == 2) RDWH(F, wb, s, 0); \
    else if ((r) < 6) RDX(F, xb, s, ((r) - 2) & 3); else if (((r) & 1) == 0) RDWL(F, wb, s, (((r) + 8) >> 1) % 6); else RDWH(F, wb, s, (((r) + 8) >> 1) % 6); } while (0)
#define PB 27
#define PFP 36
#define BP(r) ((r) * 3 / 2)
#define AP(r) (29 + (r) * 6 / 5)
#define XP(j) (2 + (j) * 6)
#define WP(j) (28 + (j) * 3)
#define TILE(LDA, DMA, WAITB) do { \
    const unsigned xb = xa + xs, wb = wa + wsx, wd = wdst + wsx; \
    xp = xs; xs = (xs == 2 * XST) ? 0 : xs + XST; wsx ^= WST; \
    const unsigned xn = xa + xs, wn_ = wa + wsx; \
    sfor<0, 48>([&](auto mi) AI { constexpr int m_ = decltype(mi)::v; \
      if constexpr (m_ < 24) MM(A, m_ >> 2, m_ & 3); else MM(B, (m_ - 24) >> 2, m_ & 3); \
      sfor<0, 16>([&](auto ri) AI { constexpr int r_ = decltype(ri)::v; \
        if constexpr (BP(r_) == m_) RD1(B, xb, wb, 1, r_); if constexpr (LDA && AP(r_) == m_) RD1(A, xn, wn_, 0, r_); }); \
      sfor<0, 4>([&](auto ji) AI { constexpr int j_ = decltype(ji)::v; \
        if constexpr (DMA && XP(j_) == m_) dma16x(xd + j_ * 1024, (j_ & 1) ? xv1 : xv0, rx, xso + (j_ >> 1) * 32768); \
        if constexpr (DMA && j_ < 3 && WP(j_) == m_) dma16w(wd + j_ * 8192, wv, rw, wso + j_ * 2048); }); \
      if constexpr (DMA && m_ == PFP) { pf4(lds0 + LDS_SPARE + 8192, pfp); pfp += pinc; } \
      if constexpr (!LDA) {     \
        if constexpr (m_ == 28) { oa = f32x16{}; PIN(oa); } if constexpr (m_ == 34) { ob = f32x16{}; PIN(ob); } \
        if constexpr (m_ == 40) { p0 = u32x4{}; p1 = u32x4{}; v0 = bf16x8{}; v1 = bf16x8{}; PIN(p0); PIN(p1); PIN(v0); PIN(v1); } \
        if constexpr (m_ == 44) { Ls = f32x4{}; sel = asb(u32x4{sel1, sel1, sel1, sel1}); PIN(Ls); PIN(sel); } } \
      SBAR(); \
      if constexpr (m_ == PB) { WAITB; SBAR(); } }); } while (0)
  WAIT_BAR_V(9, obase);
  STAMP(1);
  unsigned xs = 0, wsx = 0, xp = 2 * XST;
  sfor<0, 16>([&](auto ri) AI { constexpr int r_ = decltype(ri)::v; RD1(A, xa, wa, 0, r_); SBAR(); });
  if (w == 0) st32(obase ^ ACKX, rws, (fl_ok && l <= 32 && !(l & 1)) ? WS_ACK + fl_id * 128 + (l < 32 ? ns : 16) * 4 : OOB);
  const unsigned pbase = __builtin_amdgcn_readlane(obase, 32), mybase = __builtin_amdgcn_readlane(obase, 33);
  _Pragma("unroll") for (int i_ = 0; i_ < 24; ++i_) PIN(acc[i_ >> 2][i_ & 3]);
  asm volatile("s_nop 1" ::: "memory");
  SBAR();
  const bool ack_ok = l < 17 && !(l == 16 && R == 7);
  const unsigned ackoff = ack_ok ? WS_ACK + L * 128 + l * 4 : OOB;
#pragma unroll 1
  for (int t = 0; t < 15; ++t) {
    const unsigned xso = t < 14 ? (t + 2) * 128u : 0x80000000u, wso = t < 14 ? (t + 2) * (64u * 6144u) : OOB;
    const unsigned xd = t < 14 ? xdst + xp : lds0 + LDS_SPARE;
    if (__builtin_expect(t == 12, 0)) {
      const unsigned wl_ = l < 22 ? l : 0;
      pfp = (const char*)((G >> 3) ? woB : woA) + ((idx * 8 + w) * 64 + wl_) * 128; pstep = wl_ < 20 ? 22u * 128u : 21u * 128u;
      dma4_sc1(lds0 + LDS_CTL + w * 256, ackoff, rws, 0);
    }
    const unsigned pinc = pstep;
    TILE(true, true, WAIT_BAR(5));
  }
  bool acked = __all(!ack_ok || *(volatile LAS unsigned*)(size_t)(lds0 + LDS_CTL + w * 256 + l * 4) == (mybase ^ ACKX));
  { const unsigned xso = 0, wso = 0, xd = 0, pinc = 0; TILE(false, false, (void)0); }
  asm volatile("s_nop 7" ::: "memory");
  _Pragma("unroll") for (int i_ = 0; i_ < 24; ++i_) PIN(acc[i_ >> 2][i_ & 3]);
  STAMP(2);
  bf16x8 qa0, qa1, qb0, qb1;
  f32x16 sa, sb, na, nb;
  {
    const unsigned ko = (g & 1) * 1024 + ((g >> 1) * 32 + (c & 7) + 16 * (c >> 3)) * 16;
    const unsigned vo = 32768 + (g >> 1) * 1024 + ((g & 1) * 32 + c) * 16;
    const unsigned t0 = wn * 16384 + mp * 2 * IMG;
    const unsigned gs = R < 7 ? WS_KV + L * 65536 + t0 : OOB;
#pragma unroll
    for (int u = 0; u < 4; ++u) {
      const unsigned tb = (u >> 1) * IMG;
      const u32x4 kk = pack44(acc[2][u], acc[3][u]), vv = pack44(acc[4 + (u & 1)][u & 2], acc[4 + (u & 1)][(u & 2) + 1]);
      __builtin_amdgcn_raw_buffer_store_b128(kk, rws, ko + (u & 1) * 128 + tb, gs, SC1);
      __builtin_amdgcn_raw_buffer_store_b128(vv, rws, vo + (u & 1) * 256 + tb, gs, SC1);
      sts128(lds0 + LDS_OWN + t0 + tb + ko + (u & 1) * 128, kk);
      sts128(lds0 + LDS_OWN + t0 + tb + vo + (u & 1) * 256, vv);
    }
    u32x4 qf[4]; float sd[4];
#pragma unroll
    for (int u = 0; u < 4; ++u) {
      const f32x4 qx = acc[0][u] * QSCALE, qy = acc[1][u] * QSCALE;
      qf[u] = pack44(qx, qy);
      float d = qx[0] * acc[2][u][0];
#pragma unroll
      for (int i = 1; i < 4; ++i) d = __builtin_fmaf(qx[i], acc[2][u][i], d);
#pragma unroll
      for (int i = 0; i < 4; ++i) d = __builtin_fmaf(qy[i], acc[3][u][i], d);
      sd[u] = d;
    }
#pragma unroll
    for (int p = 0; p < 2; ++p) {
#pragma unroll
      for (int i = 0; i < 4; ++i) { auto r = __builtin_amdgcn_permlane16_swap(qf[2 * p][i], qf[2 * p + 1][i], false, false); qf[2 * p][i] = r[0]; qf[2 * p + 1][i] = r[1]; }
      auto r = __builtin_amdgcn_permlane16_swap(__float_as_uint(sd[2 * p]), __float_as_uint(sd[2 * p + 1]), false, false);
      sd[2 * p] = -swapsum(__uint_as_float(r[0]) + __uint_as_float(r[1]));
    }
    qb0 = asb(qf[0]); qb1 = asb(qf[1]); qa0 = asb(qf[2]); qa1 = asb(qf[3]);
#pragma unroll
    for (int i = 0; i < 16; ++i) { na[i] = sd[2]; nb[i] = sd[0]; }
  }
  float la = 0.f, lb = 0.f;
  bf16x8 k0, k1;
  PIN(na); PIN(nb); PIN(qa0); PIN(qa1); PIN(qb0); PIN(qb1);
  STAMP(3);
  const __amdgpu_buffer_rsrc_t rwo = mkrs((G >> 3) ? woB : woA, D_ * D_ * 2);
  const unsigned wov = (8 * w + (l >> 3)) * 2048 + (((l & 7) ^ (((l >> 4) & 3) << 1)) << 4) + ns * 128;
#define DMA_WO(j) dma16(lds0 + (j) * 8192 + w * 1024, wov, rwo, (j) * (64u * 2048u))
  const unsigned lb16 = l * 16, own = lds0 + LDS_OWN + wn * 16384, prv = R > 0 ? own + (LDS_PREV - LDS_OWN) : own;
  const unsigned myflag = WS_FLAG + L * 128 + w * 4, pfoff = WS_FLAG + (L - 16) * 128 + (w ^ ((l & 1) * 4)) * 4;
  const unsigned kvo = wn * 16384 + mp * 2 * IMG, kvo2 = (wn ^ 1) * 16384 + (3 - mp) * 2 * IMG;
#define QK0(S, Q, N) asm volatile("v_mfma_f32_32x32x16_bf16 %0, %1, %2, %3" : "=&v"(S) : "v"(k0), "v"(Q), "v"(N), "v"(k1))
#define QK1(S, Q) asm volatile("v_mfma_f32_32x32x16_bf16 %0, %1, %2, %0" : "+v"(S) : "v"(k1), "v"(Q))
#define PV0(O) asm volatile("v_mfma_f32_32x32x16_bf16 %0, %1, %2, %0" : "+v"(O) : "v"(v0), "v"(p0), "v"(v1))
#define PV1(O) asm volatile("v_mfma_f32_32x32x16_bf16 %0, %1, %2, %0" : "+v"(O) : "v"(v1), "v"(p1))
#define EX(S, i) do { S[i] = __builtin_amdgcn_exp2f(S[i]); } while (0)
#define RDK(a) do { k0 = lds128(a); k1 = lds128((a) + 1024); } while (0)
  unsigned pa = own + (2 * mp + 1) * IMG, pb = pa - IMG, pbp = pb;
  unsigned va = lb16 + pa, vb = lb16 + pb;
  RDK(va);
  asm volatile("s_nop 1" ::: "memory");
  QK0(sa, qa0, na); QK1(sa, qa1);
  RDK(vb);
  asm volatile("s_nop 7\n\ts_nop 1" ::: "memory");
  SBAR();
#define EXP(i) e_[i] = __builtin_amdgcn_exp2f(sx_[i])
#define HALF(SX, SY, NY, LY, OY, QY0, QY1, PY, RD, VX, KNEXT, BAR1, MSK) do { \
    const f32x16 sx_ = SX; float e_[16]; u32x4 pn_; \
    EXP(0); EXP(1); EXP(2); EXP(3); EXP(4); EXP(5); EXP(6); EXP(7); \
    asm volatile("" : "+v"(e_[0]), "+v"(e_[1]), "+v"(e_[2]), "+v"(e_[3]), "+v"(e_[4]), "+v"(e_[5]), "+v"(e_[6]), "+v"(e_[7]), "+v"(Ls)); SBAR();     \
    pn_[0] = pk2(e_[0], e_[1]); PIN(pn_[0]); SBAR(); \
    unsigned long long bad_ = __builtin_amdgcn_ballot_w64(!(Ls[0] <= PBOUND)); asm volatile("" : "+s"(bad_)); SBAR(); \
    pn_[1] = pk2(e_[2], e_[3]); pn_[2] = pk2(e_[4], e_[5]); pn_[3] = pk2(e_[6], e_[7]); EXP(8); \
    asm volatile("" : "+v"(pn_), "+v"(e_[8])); SBAR(); \
    if (__builtin_expect(bad_ != 0, 0)) redo_tile(PY, QY0, QY1, sel, OY, NY, LY, p0, p1, Ls); \
    PV0(OY); SBAR(); \
    EXP(9); EXP(10); LY += Ls[0]; asm volatile("" : "+v"(e_[9]), "+v"(e_[10]), "+v"(LY)); SBAR(); \
    PV1(OY); SBAR(); if (RD) { v0 = lds128((VX) + 32768); v1 = lds128((VX) + 32768 + 1024); VX = KNEXT; } \
    EXP(11); EXP(12); asm volatile("" : "+v"(e_[11]), "+v"(e_[12]), "+v"(VX)); SBAR(); \
    QK0(SY, QY0, NY); SBAR(); EXP(13); EXP(14); EXP(15); asm volatile("" : "+v"(e_[13]), "+v"(e_[14]), "+v"(e_[15])); SBAR(); \
    QK1(SY, QY1); SBAR(); \
    if (BAR1) asm volatile("s_waitcnt vmcnt(8) lgkmcnt(0)\n\ts_barrier" ::: "memory"); \
    if (RD) RDK(VX); SBAR(); \
    p0 = pn_; p1[0] = pk2(e_[8], e_[9]); p1[1] = pk2(e_[10], e_[11]); p1[2] = pk2(e_[12], e_[13]); p1[3] = pk2(e_[14], e_[15]); SBAR(); \
    if (MSK) { p0 &= mk0; p1 &= mk1; PIN(p0); PIN(p1); asm volatile("s_nop 0"); } \
    asm volatile("v_mfma_f32_16x16x32_bf16 %0, %1, %2, 0\n\ts_nop 0\n\tv_mfma_f32_16x16x32_bf16 %0, %1, %3, %0\n\ts_nop 2" : "=&v"(Ls) : "v"(sel), "v"(p0), "v"(p1)); SBAR(); } while (0)
  unsigned sent = 0;
#define SIGNAL() do { WAITV(0); \
    _Pragma("unroll 1") for (unsigned sp = 0; !acked && sp < SPIN_CAP; ++sp) { __builtin_amdgcn_s_sleep(2); acked = __all(!ack_ok || ld32_wait(rws, ackoff) == (mybase ^ ACKX)); } \
    if (l == 0) st32(mybase + 1, rws, myflag); \
    sent = 1; } while (0)
#pragma unroll 1
  for (int seg = 0;; ++seg) {
#pragma unroll 1
    for (int k = seg; k < 2 * seg + 1; ++k) {
      const unsigned pan = k == 0 ? own : prv + (2 * mp + k) * IMG, pbn = k == 1 ? pan - IMG : pan, md = k == 2 ? 2u : (k == 0 ? 1u : 0u);
      HALF(sa, sb, nb, lb, ob, qb0, qb1, pbp, true, va, lb16 + pan, k == 0, k != 1);
      HALF(sb, sa, na, la, oa, qa0, qa1, pa | md, true, vb, lb16 + pbn, false, k != 1);
      pbp = pb | md; pa = pan; pb = pbn;
      if (R == 0 && k == 1) break;
    }
    if (R == 0 && seg) break;
#pragma unroll 1
    for (int n = seg ? 6 - 2 * mp : 2 * mp; n > 0; --n) {
      HALF(sa, sb, nb, lb, ob, qb0, qb1, pbp, true, va, va + IMG, false, false);
      HALF(sb, sa, na, la, oa, qa0, qa1, pa, false, vb, 0, false, false);
      pbp = pa; pa += IMG;
      if (__builtin_expect(!sent, 0)) { if ((__builtin_amdgcn_s_getreg(7 | (31 << 11)) & 0x00C0000Fu) == 0) SIGNAL(); }
    }
    if (seg) break;
    STAMP(4);
    if (!sent) SIGNAL();
    STAMP(5);
    mk0 = ~mk0; mk1 = ~mk1; PIN(mk0); PIN(mk1);
    if (R > 0 && mp < 2) {
#pragma unroll 1
      for (unsigned sp = 0; sp < SPIN_CAP; ++sp) {
        if (__all(ld32_wait(rws, pfoff) - pbase - 1u < 2u)) break;
        __builtin_amdgcn_s_sleep(1);
      }
      unsigned ka_ = kvo, kb_ = kvo2, src_ = WS_KV + (L - 16) * 65536; asm volatile("" : "+s"(ka_), "+s"(kb_), "+s"(src_));
#pragma unroll
      for (int j_ = 0; j_ < 16; ++j_) { const unsigned o_ = ((j_ & 8) ? kb_ : ka_) + (j_ & 3) * 1024 + ((j_ >> 2) & 1) * 32768; dma16_sc1(lds0 + LDS_PREV + o_, lb16, rws, src_ + o_); }
    }
    STAMP(6);
    WAIT_BAR(0);
    STAMP(7);
    pb = prv + (2 * mp + 1) * IMG; vb = lb16 + pb;
    RDK(vb);
    DMA_WO(0); DMA_WO(1); DMA_WO(2); DMA_WO(3);
    SBAR();
  }
  asm volatile("s_nop 7" : "+v"(Ls) :: "memory");
  if (R > 0) {
    if (__builtin_expect(__any(!(Ls[0] <= PBOUND)), 0)) redo_tile(pbp, qb0, qb1, sel, ob, nb, lb, p0, p1, Ls);
    lb += Ls[0];
    PV0(ob); PV1(ob);
  }
  STAMP(8);
  { const float ia = __builtin_amdgcn_rcpf(la), ib = __builtin_amdgcn_rcpf(lb);
    oa *= ia;
    const unsigned hh = hp * 2 + wn, T0 = br * 64 + R * 8 + 2 * mp;
    const unsigned ol = ((l >> 4) & 1) * 1024 + ((l >> 5) * 16 + (l & 15)) * 16;
    __builtin_amdgcn_raw_buffer_store_b128(pack8(oa, 0), rws, ol, WS_O + (T0 * 32 + 32 + hh) * IMG, SC1);
    __builtin_amdgcn_raw_buffer_store_b128(pack8(oa, 1), rws, ol + 512, WS_O + (T0 * 32 + 32 + hh) * IMG, SC1);
    SBAR();
    asm volatile("s_nop 7" : "+v"(ob) :: "memory");
    ob *= ib;
    __builtin_amdgcn_raw_buffer_store_b128(pack8(ob, 0), rws, ol, WS_O + (T0 * 32 + hh) * IMG, SC1);
    __builtin_amdgcn_raw_buffer_store_b128(pack8(ob, 1), rws, ol + 512, WS_O + (T0 * 32 + hh) * IMG, SC1); }
  WAITV(0);
  if (l == 0) st32(mybase + 2, rws, myflag);
  STAMP(9);
  asm volatile("s_barrier" ::: "memory");
#pragma unroll
  for (int j = 4; j < 16; ++j) DMA_WO(j);
  { const unsigned pw = (l & 1) ? 7 - (w >> 1) : (w >> 1);
    const unsigned fo = WS_FLAG + (G * 16 + ((l & 31) >> 1)) * 128 + pw * 4;
#pragma unroll 1
    for (unsigned sp = 0; sp < SPIN_CAP; ++sp) {
      const unsigned f = ld32_wait(rws, fo);
      if (__all(l >= 32 || f == obase + 2)) break;
      __builtin_amdgcn_s_sleep(1);
    } }
  STAMP(10);
  const unsigned T = G * 8 + w;
  const unsigned obs = WS_O + T * 32 * IMG;
  const unsigned wa2 = lds0 + (16 * (g >> 1) + 4 * (g & 1) + q4) * 128 + ((2 * (g & 1) + (q4 >> 1)) << 5) + (l & 3) * 8;
  f32x4 oc[4][2] = {};
  u32x4 of[16];
  s16x4 wl[2][4], wh[2][4];
#pragma unroll
  for (int i = 0; i < 16; ++i) of[i] = __builtin_amdgcn_raw_buffer_load_b128(rws, l * 16 + (i & 3) * 1024, obs + (i >> 2) * 4096, SC1);
#pragma unroll
  for (int nt = 0; nt < 4; ++nt) { wl[0][nt] = ldstr(wa2 ^ (nt << 5)); wh[0][nt] = ldstr((wa2 ^ (nt << 5)) + 1024); }
  _Pragma("unroll") for (int i_ = 0; i_ < 8; ++i_) PIN(oc[i_ >> 1][i_ & 1]);
  asm volatile("s_nop 1" ::: "memory");
  SBAR();
#pragma unroll 1
  for (int gq = 0; gq < 4; ++gq) {
    const unsigned wb = wa2 + gq * 32768;
    const unsigned on = gq < 3 ? obs + (gq + 1) * 16384 : OOB;
    sfor<0, 8>([&](auto hi) AI { constexpr int h_ = decltype(hi)::v, cur = h_ & 1, nxt = cur ^ 1;
      if constexpr (h_ == 7) { if (gq == 0) asm volatile("s_barrier" ::: "memory"); }
      sfor<0, 8>([&](auto mi) AI { constexpr int m_ = decltype(mi)::v, nt = m_ >> 1, tt = m_ & 1;
        { const bf16x8 wf = cat(wl[cur][nt], wh[cur][nt]); MFA(oc[nt][tt], wf, of[2 * h_ + tt]); }
        if constexpr (tt == 0) wl[nxt][nt] = ldstr((wb ^ (nt << 5)) + (h_ + 1) * 4096); else wh[nxt][nt] = ldstr((wb ^ (nt << 5)) + (h_ + 1) * 4096 + 1024);
        if constexpr (m_ >= 6) of[2 * h_ + tt] = __builtin_amdgcn_raw_buffer_load_b128(rws, l * 16 + ((2 * h_ + tt) & 3) * 1024, on + ((2 * h_ + tt) >> 2) * 4096, SC1);
        SBAR(); }); });
  }
  asm volatile("s_nop 7" ::: "memory");
  _Pragma("unroll") for (int i_ = 0; i_ < 8; ++i_) PIN(oc[i_ >> 1][i_ & 1]);
  STAMP(11);
  const __amdgpu_buffer_rsrc_t rout = mkrs(out, S_ * 2 * D_ * 2);
  const unsigned orow = (((G & 7) * 8 + w) * 32 + c) * 4096 + (G >> 3) * 2048 + ns * 128 + (g & 1) * 32 + (g >> 1) * 16;
#pragma unroll
  for (int tt = 0; tt < 2; ++tt) {
#pragma unroll
    for (int np = 0; np < 2; ++np) {
      const f32x4 &a = oc[2 * np][tt], &b = oc[2 * np + 1][tt];
      auto r0 = __builtin_amdgcn_permlane16_swap(pk2(a[0], a[1]), pk2(b[0], b[1]), false, false);
      auto r1 = __builtin_amdgcn_permlane16_swap(pk2(a[2], a[3]), pk2(b[2], b[3]), false, false);
      u32x4 v = {r0[0], r1[0], r0[1], r1[1]};
      __builtin_amdgcn_raw_buffer_store_b128(v, rout, orow + tt * 65536 + np * 64, 0, 0);
    }
  }
}

extern "C" void kernel_launch(void* const* d_in, const int* in_sizes, int n_in, void* d_out, int out_size, void* d_ws, size_t ws_size,
                              hipStream_t stream) {
  const size_t wsoff = (ws_size - WS_END) & ~(size_t)0xfffff;
  k_fused<<<256, 512, 0, stream>>>(d_in[0], d_in[1], d_in[2], d_in[3], d_in[4], d_in[5], (char*)d_ws + wsoff, d_out);
}
```

```cpp
#include <hip/hip_runtime.h>
#include <hip/hip_bf16.h>

typedef short bf16x8 __attribute__((ext_vector_type(8)));
typedef short s16x4 __attribute__((ext_vector_type(4)));
typedef float f32x16 __attribute__((ext_vector_type(16)));
typedef float f32x4 __attribute__((ext_vector_type(4)));
typedef unsigned u32x4 __attribute__((ext_vector_type(4)));
typedef __bf16 bf16x2_t __attribute__((ext_vector_type(2)));
typedef float f32x2 __attribute__((ext_vector_type(2)));
#define LAS __attribute__((address_space(3)))
#define DI __device__ __forceinline__
#define AI __attribute__((always_inline))
#define MFMA(a, b, c) __builtin_amdgcn_mfma_f32_32x32x16_bf16((a), (b), (c), 0, 0, 0)
#define SBAR() __builtin_amdgcn_sched_barrier(0)
#define PIN(x) asm volatile("" : "+v"(x))
#define MFA(acc_, a_, b_) asm volatile("v_mfma_f32_16x16x32_bf16 %0, %1, %2, %0" : "+v"(acc_) : "v"(a_), "v"(b_))

constexpr int S_ = 2048, D_ = 1024;
constexpr float QSCALE = 0.17677669529663687f * 1.4426950408889634f;
constexpr float PBOUND = 1073741824.0f;
constexpr unsigned IMG = 2048;
constexpr unsigned WS_FLAG = 0, WS_ACK = 32768, WS_KV = 65536, WS_O = WS_KV + 256u * 65536u, WS_END = WS_O + 2u * 64u * 32u * IMG;
constexpr unsigned ACKX = 0x5bd1e995u;
constexpr int SC1 = 16;
constexpr unsigned CODE_BYTES = 18304;
constexpr unsigned SPIN_CAP = 1u << 18;

#define HPMAP ((xcd & 1) * 8 + (idx >> 2))
#define RMAP (((xcd >> 1) & 1) * 4 + (idx & 3))
#define NOMEMX 1
template <int I> struct IC { static constexpr int v = I; };
template <int B, int E, class F> DI void sfor(F&& f) { if constexpr (B < E) { f(IC<B>{}); sfor<B + 1, E>(f); } }
DI unsigned rfl(unsigned x) { return (unsigned)__builtin_amdgcn_readfirstlane((int)x); }
DI unsigned pk2(float lo, float hi) { f32x2 v = {lo, hi}; return __builtin_bit_cast(unsigned, __builtin_convertvector(v, bf16x2_t)); }
DI u32x4 pack8(const f32x16& x, int s) {
  u32x4 p;
  p[0] = pk2(x[8 * s + 0], x[8 * s + 1]); p[1] = pk2(x[8 * s + 2], x[8 * s + 3]);
  p[2] = pk2(x[8 * s + 4], x[8 * s + 5]); p[3] = pk2(x[8 * s + 6], x[8 * s + 7]);
  return p;
}
DI u32x4 pack44(const f32x4& a, const f32x4& b) { u32x4 p; p[0] = pk2(a[0], a[1]); p[1] = pk2(a[2], a[3]); p[2] = pk2(b[0], b[1]); p[3] = pk2(b[2], b[3]); return p; }
DI bf16x8 asb(u32x4 v) { return __builtin_bit_cast(bf16x8, v); }
DI bf16x8 lds128(unsigned a) { return *(const LAS bf16x8*)(size_t)a; }
DI void sts128(unsigned a, u32x4 v) { *(LAS u32x4*)(size_t)a = v; }
DI s16x4 ldstr(unsigned a) { return __builtin_amdgcn_ds_read_tr16_b64_v4i16((LAS s16x4*)(size_t)a); }
DI bf16x8 cat(s16x4 lo, s16x4 hi) { return __builtin_shufflevector(lo, hi, 0, 1, 2, 3, 4, 5, 6, 7); }
DI void dma16(unsigned ldsdst, unsigned voff, __amdgpu_buffer_rsrc_t rs, unsigned soff) {
  asm volatile("s_mov_b32 m0, %0\n\ts_nop 0\n\tbuffer_load_dwordx4 %1, %2, %3 offen lds" ::"s"(ldsdst), "v"(voff), "s"(rs), "s"(soff) : "memory");
}
#define XPOL ""
#define WPOL ""
DI void dma16x(unsigned ldsdst, unsigned voff, __amdgpu_buffer_rsrc_t rs, unsigned soff) {
  asm volatile("s_mov_b32 m0, %0\n\ts_nop 0\n\tbuffer_load_dwordx4 %1, %2, %3 offen " XPOL " lds" ::"s"(ldsdst), "v"(voff), "s"(rs), "s"(soff) : "memory");
}
DI void dma16w(unsigned ldsdst, unsigned voff, __amdgpu_buffer_rsrc_t rs, unsigned soff) {
  asm volatile("s_mov_b32 m0, %0\n\ts_nop 0\n\tbuffer_load_dwordx4 %1, %2, %3 offen " WPOL " lds" ::"s"(ldsdst), "v"(voff), "s"(rs), "s"(soff) : "memory");
}
#define PPOL ""
#define PFMODE 0
#define PFD 4
#define PFH 1
DI void pf4(unsigned ldsdst, const char* p) {
  asm volatile("s_mov_b32 m0, %0\n\ts_nop 0\n\tglobal_load_lds_dword %1, off " PPOL ::"s"(ldsdst), "v"(p) : "memory");
}
DI void dma16_sc1(unsigned ldsdst, unsigned voff, __amdgpu_buffer_rsrc_t rs, unsigned soff) {
  asm volatile("s_mov_b32 m0, %0\n\ts_nop 0\n\tbuffer_load_dwordx4 %1, %2, %3 offen sc1 lds" ::"s"(ldsdst), "v"(voff), "s"(rs), "s"(soff) : "memory");
}
#define WAITV(n) asm volatile("s_waitcnt vmcnt(" #n ")" ::: "memory")
#define WAIT_BAR(n) asm volatile("s_waitcnt vmcnt(" #n ") lgkmcnt(0)\n\ts_barrier" ::: "memory")
DI __amdgpu_buffer_rsrc_t mkrs(const void* p, unsigned bytes) { return __builtin_amdgcn_make_buffer_rsrc((void*)p, (short)0, (int)bytes, 0x00020000); }
DI unsigned ld32_issue(__amdgpu_buffer_rsrc_t rs, unsigned off) { unsigned r; asm volatile("buffer_load_dword %0, %1, %2, 0 offen sc1" : "=v"(r) : "v"(off), "s"(rs) : "memory"); return r; }
DI unsigned ld32_wait(__amdgpu_buffer_rsrc_t rs, unsigned off) { unsigned r; asm volatile("buffer_load_dword %0, %1, %2, 0 offen sc1\n\ts_waitcnt vmcnt(0)" : "=&v"(r) : "v"(off), "s"(rs) : "memory"); return r; }
DI void dma4(unsigned ldsdst, unsigned voff, __amdgpu_buffer_rsrc_t rs, unsigned soff) {
  asm volatile("s_mov_b32 m0, %0\n\ts_nop 0\n\tbuffer_load_dword %1, %2, %3 offen lds" ::"s"(ldsdst), "v"(voff), "s"(rs), "s"(soff) : "memory");
}
DI void dma4_sc1(unsigned ldsdst, unsigned voff, __amdgpu_buffer_rsrc_t rs, unsigned soff) {
  asm volatile("s_mov_b32 m0, %0\n\ts_nop 0\n\tbuffer_load_dword %1, %2, %3 offen sc1 lds" ::"s"(ldsdst), "v"(voff), "s"(rs), "s"(soff) : "memory");
}
#define WAIT_BAR_V(n, x) asm volatile("s_waitcnt vmcnt(" #n ") lgkmcnt(0)\n\ts_barrier" : "+v"(x) :: "memory")
constexpr unsigned OOB = 0x80000000u;
DI void st32(unsigned v, __amdgpu_buffer_rsrc_t rs, unsigned off) { __builtin_amdgcn_raw_buffer_store_b32(v, rs, off, 0, SC1); }

#define STAMP(i) do {} while (0)

constexpr unsigned XST = 32768, WST = 24576, LDS_W = 3 * XST, LDS_SPARE = LDS_W + 2 * WST, LDS_CTL = 159744;
static_assert(PFD == 4, "prefetch distance");
constexpr unsigned LDS_OWN = 32768, LDS_CNT = 162816, LDS_BYTES = 163840, TIL = 2 * IMG;

struct Frag { bf16x8 x[4]; s16x4 wl[6], wh[6]; };

DI void lds_inc(unsigned a) { asm volatile("s_mov_b64 exec, 1\n\tds_add_u32 %0, %1\n\ts_mov_b64 exec, -1" ::"v"(a), "v"(1u) : "memory"); }
DI void lds_wait(unsigned a, unsigned n) {
#pragma unroll 1
  for (unsigned sp = 0; sp < SPIN_CAP && rfl(*(volatile LAS unsigned*)(size_t)a) < n; ++sp) __builtin_amdgcn_s_sleep(1);
  asm volatile("" ::: "memory");
}
DI void dma64_sc1(unsigned ldsdst, unsigned voff, __amdgpu_buffer_rsrc_t rs, unsigned soff) {
  asm volatile("s_mov_b32 m0, %0\n\ts_nop 0\n\tbuffer_load_dwordx4 %1, %2, %3 offen sc1 lds\n\tbuffer_load_dwordx4 %1, %2, %3 offen offset:1024 sc1 lds\n\t"
               "buffer_load_dwordx4 %1, %2, %3 offen offset:2048 sc1 lds\n\tbuffer_load_dwordx4 %1, %2, %3 offen offset:3072 sc1 lds" ::"s"(ldsdst), "v"(voff), "s"(rs), "s"(soff) : "memory");
}
DI float swapmax(float m) {
  auto rr = __builtin_amdgcn_permlane32_swap(__float_as_uint(m), __float_as_uint(m), false, false);
  return __builtin_fmaxf(__uint_as_float(rr[0]), __uint_as_float(rr[1]));
}
DI float swapsum(float m) {
  auto rr = __builtin_amdgcn_permlane32_swap(__float_as_uint(m), __float_as_uint(m), false, false);
  return __uint_as_float(rr[0]) + __uint_as_float(rr[1]);
}
#define MX3(a, b, c) __builtin_fmaxf(__builtin_fmaxf((a), (b)), (c))
DI float max16(const f32x16& p) {
  float a = MX3(p[0], p[1], p[2]), b = MX3(p[3], p[4], p[5]);
  a = MX3(a, p[6], p[7]); b = MX3(b, p[8], p[9]); a = MX3(a, p[10], p[11]); b = MX3(b, p[12], p[13]);
  a = MX3(a, p[14], p[15]);
  return __builtin_fmaxf(a, b);
}
DI int keyidx(int i) { return (i & 3) + 16 * ((i >> 2) & 1) + 8 * (i >> 3); }
DI void redo_tile(unsigned tile, bf16x8 q0, bf16x8 q1, bf16x8 sel, f32x16& o, f32x16& negm, float& l, u32x4& p0, u32x4& p1, f32x4& L) {
  int ln = threadIdx.x & 63; PIN(ln);
  const unsigned ka = (tile & ~3u) + ln * 16, mode = tile & 3u;
  const bf16x8 k0 = lds128(ka), k1 = lds128(ka + 1024);
  const int rr = (ln & 31) - 4 * (ln >> 5);
  f32x16 s;
#pragma unroll
  for (int i = 0; i < 16; ++i) s[i] = (mode != 0 && (keyidx(i) <= rr) == (mode == 2)) ? -__builtin_inff() : negm[0];
  s = MFMA(k0, q0, s); s = MFMA(k1, q1, s);
  const float d = __builtin_fmaxf(swapmax(max16(s)), 0.f), f = __builtin_amdgcn_exp2f(-d), nm = negm[0] - d;
  l *= f;
#pragma unroll
  for (int i = 0; i < 16; ++i) { negm[i] = nm; o[i] *= f; s[i] = __builtin_amdgcn_exp2f(s[i] - d); }
  p0 = pack8(s, 0); p1 = pack8(s, 1);
  f32x4 z = {};
  L = __builtin_amdgcn_mfma_f32_16x16x32_bf16(sel, asb(p0), z, 0, 0, 0);
  L = __builtin_amdgcn_mfma_f32_16x16x32_bf16(sel, asb(p1), L, 0, 0, 0);
  float t = L[0]; PIN(t); L[0] = t;
  PIN(p0); PIN(p1); PIN(o); PIN(negm);
  asm volatile("s_nop 1" ::: "memory");
}

__global__ __launch_bounds__(512, 2) void k_fused(const void* xA, const void* wA, const void* woA, const void* xB, const void* wB, const void* woB,
                                                  void* ws, void* out) {
  __shared__ __attribute__((aligned(1024))) char smem[LDS_BYTES];
  const unsigned lds0 = (unsigned)(size_t)(LAS char*)smem;
  const unsigned bid = blockIdx.x, xcd = bid & 7, idx = bid >> 3;
  const unsigned br = xcd >> 2, R = RMAP, hp = HPMAP;
  const unsigned L = (br * 8 + R) * 16 + hp;
  const unsigned G = xcd * 2 + (idx >> 4), ns = idx & 15;
  const unsigned tid = threadIdx.x, l = tid & 63, w = rfl(tid >> 6), wm = w & 3, wn = w >> 2;
  const __amdgpu_buffer_rsrc_t rx = mkrs(br ? xB : xA, NOMEMX * S_ * D_ * 2), rw = mkrs(br ? wB : wA, D_ * 3 * D_ * 2), rws = mkrs(ws, WS_END);
  STAMP(0);
  const unsigned fl_id = l < 32 ? G * 16 + (l >> 1) : (l == 32 ? L - 16 : L);
  const bool fl_ok = l < 34 && !(l == 32 && R == 0);
  unsigned obase = ld32_issue(rws, fl_ok ? WS_FLAG + fl_id * 128 : OOB);
  const unsigned mtw = w, mp = wn ? 3 - wm : wm;
  const unsigned xv0 = (R * 256 + mtw * 32 + (l >> 3)) * 2048 + (((l & 7) ^ (l >> 4)) << 4);
  const unsigned xv1 = (R * 256 + mtw * 32 + 8 + (l >> 3)) * 2048 + (((l & 7) ^ (4 + (l >> 4))) << 4);
  const unsigned wv = (8 * w + (l >> 3)) * 6144 + (((l & 7) ^ ((((l >> 4) & 1) | ((w & 1) << 1)) << 1)) << 4) + hp * 128;
  const unsigned xdst = lds0 + w * 4096, wdst = lds0 + LDS_W + w * 1024;
#define DMA_X(t, st) do { const unsigned so_ = (t) * 128u, d_ = xdst + (st); \
    dma16x(d_, xv0, rx, so_); dma16x(d_ + 1024, xv1, rx, so_); dma16x(d_ + 2048, xv0, rx, so_ + 32768); dma16x(d_ + 3072, xv1, rx, so_ + 32768); } while (0)
#define DMA_W(t, st) do { const unsigned so_ = (t) * (64u * 6144u), d_ = wdst + (st); \
    dma16w(d_, wv, rw, so_); dma16w(d_ + 8192, wv, rw, so_ + 2048); dma16w(d_ + 16384, wv, rw, so_ + 4096); } while (0)
  DMA_W(0, 0); DMA_X(0, 0); DMA_W(1, WST); DMA_X(1, XST);
  const unsigned pli = l < 15 ? l : 0;
  const unsigned pwl = ((idx & 3) * 8 + w) * 6 + (pli - 4);
  const unsigned pq = (idx * 8 + w) * 5 + (pli - 10);
  const char* pfp = pli < 4 ? (const char*)(br ? xB : xA) + (R * 256 + ((idx >> 2) * 8 + w) * 4 + pli) * 2048 + (PFD - 1) * 128
                  : pli < 10 ? (const char*)(br ? wB : wA) + (pwl / 3 + (PFD - 1) * 64) * 6144 + (pwl % 3) * 2048 + hp * 128
                             : (const char*)((G >> 3) ? woB : woA) + pq * 128;
  unsigned pstep = pli < 4 ? 128u : pli < 10 ? 64u * 6144u : 1280u * 128u;
  pf4(lds0 + LDS_SPARE + 8192, pfp); pfp += pstep;
#define CODEPF(bytes) do { unsigned long long pc; asm volatile("s_getpc_b64 %0" : "=s"(pc));     \
    const unsigned co = (w * 64 + l) * 128; \
    pf4(lds0 + LDS_SPARE + 8192, (const char*)(pc & ~127ull) + (co < (bytes) ? co : 0)); } while (0)
  if (tid < 5) *(LAS unsigned*)(size_t)(lds0 + LDS_CNT + tid * 4) = 0;
  const unsigned c = l & 15, g = l >> 4, q4 = c >> 2;
  const unsigned xa = lds0 + mp * 8192 + c * 128 + ((g ^ (c >> 1)) << 4);
  const unsigned wa = lds0 + LDS_W + (8 * g + q4) * 128 + (((2 * wn) ^ ((q4 >> 1) | ((g & 1) << 1))) << 5) + (l & 3) * 8;
  f32x4 acc[6][4] = {};
  u32x4 mk0, mk1; unsigned sel1 = ((c & 3) == 0 && ((c >> 2) & 1) == (g & 1)) ? 0x3F803F80u : 0u;
  { const int rr = (int)(l & 31) - 4 * (int)(l >> 5);
#pragma unroll
    for (int i = 0; i < 4; ++i) {
      mk0[i] = (keyidx(2 * i) <= rr ? 0xFFFFu : 0u) | (keyidx(2 * i + 1) <= rr ? 0xFFFF0000u : 0u);
      mk1[i] = (keyidx(8 + 2 * i) <= rr ? 0xFFFFu : 0u) | (keyidx(9 + 2 * i) <= rr ? 0xFFFF0000u : 0u); }
    PIN(mk0); PIN(mk1); PIN(sel1); }
  f32x16 oa, ob; f32x4 Ls; u32x4 p0, p1; bf16x8 v0, v1, sel;
  Frag A, B;
#define RDX(F, xb, s, u) F.x[u] = lds128(((xb) ^ ((s) << 6)) + (u) * 2048)
#define MM(C, n, u) do { const bf16x8 wf_ = cat(C.wl[n], C.wh[n]); if ((n) < 4) MFA(acc[n][u], wf_, C.x[u]); else MFA(acc[n][u], C.x[u], wf_); } while (0)
#define RDWL(F, wb, s, n) F.wl[n] = ldstr(((wb) ^ (((n) & 1) << 5)) + ((n) >> 1) * 8192 + (s) * 4096)
#define RDWH(F, wb, s, n) F.wh[n] = ldstr(((wb) ^ (((n) & 1) << 5)) + ((n) >> 1) * 8192 + (s) * 4096 + 512)
#define RD1(F, xb, wb, s, r) do { if ((r) == 0) RDX(F, xb, s, 0); else if ((r) == 1) RDWL(F, wb, s, 0); else if ((r) == 2) RDWH(F, wb, s, 0); \
    else if ((r) < 6) RDX(F, xb, s, ((r) - 2) & 3); else if (((r) & 1) == 0) RDWL(F, wb, s, (((r) + 8) >> 1) % 6); else RDWH(F, wb, s, (((r) + 8) >> 1) % 6); } while (0)
#define PB 27
#define PFP 36
#define BP(r) ((r) * 3 / 2)
#define AP(r) (29 + (r) * 6 / 5)
#define XP(j) (2 + (j) * 6)
#define WP(j) (28 + (j) * 3)
#define TILE(LDA, DMA, WAITB) do { \
    const unsigned xb = xa + xs, wb = wa + wsx, wd = wdst + wsx; \
    xp = xs; xs = (xs == 2 * XST) ? 0 : xs + XST; wsx ^= WST; \
    const unsigned xn = xa + xs, wn_ = wa + wsx; \
    sfor<0, 48>([&](auto mi) AI { constexpr int m_ = decltype(mi)::v; \
      if constexpr (m_ < 24) MM(A, m_ >> 2, m_ & 3); else MM(B, (m_ - 24) >> 2, m_ & 3); \
      sfor<0, 16>([&](auto ri) AI { constexpr int r_ = decltype(ri)::v; \
        if constexpr (BP(r_) == m_) RD1(B, xb, wb, 1, r_); if constexpr (LDA && AP(r_) == m_) RD1(A, xn, wn_, 0, r_); }); \
      sfor<0, 4>([&](auto ji) AI { constexpr int j_ = decltype(ji)::v; \
        if constexpr (DMA && XP(j_) == m_) dma16x(xd + j_ * 1024, (j_ & 1) ? xv1 : xv0, rx, xso + (j_ >> 1) * 32768); \
        if constexpr (DMA && j_ < 3 && WP(j_) == m_) dma16w(wd + j_ * 8192, wv, rw, wso + j_ * 2048); }); \
      if constexpr (DMA && m_ == PFP) { pf4(lds0 + LDS_SPARE + 8192, pfp); pfp += pinc; } \
      if constexpr (!LDA) {     \
        if constexpr (m_ == 28) { oa = f32x16{}; PIN(oa); } if constexpr (m_ == 34) { ob = f32x16{}; PIN(ob); } \
        if constexpr (m_ == 40) { p0 = u32x4{}; p1 = u32x4{}; v0 = bf16x8{}; v1 = bf16x8{}; PIN(p0); PIN(p1); PIN(v0); PIN(v1); } \
        if constexpr (m_ == 44) { Ls = f32x4{}; sel = asb(u32x4{sel1, sel1, sel1, sel1}); PIN(Ls); PIN(sel); } } \
      SBAR(); \
      if constexpr (m_ == PB) { WAITB; SBAR(); } }); } while (0)
  WAIT_BAR_V(8, obase);
  STAMP(1);
  unsigned xs = 0, wsx = 0, xp = 2 * XST;
  sfor<0, 16>([&](auto ri) AI { constexpr int r_ = decltype(ri)::v; RD1(A, xa, wa, 0, r_); SBAR(); });
  if (w == 0) st32(obase ^ ACKX, rws, (fl_ok && l <= 32 && !(l & 1)) ? WS_ACK + fl_id * 128 + (l < 32 ? ns : 16) * 4 : OOB);
  const unsigned pbase = __builtin_amdgcn_readlane(obase, 32), mybase = __builtin_amdgcn_readlane(obase, 33);
  _Pragma("unroll") for (int i_ = 0; i_ < 24; ++i_) PIN(acc[i_ >> 2][i_ & 3]);
  asm volatile("s_nop 1" ::: "memory");
  SBAR();
  const bool ack_ok = l < 17 && !(l == 16 && R == 7);
  const unsigned ackoff = ack_ok ? WS_ACK + L * 128 + l * 4 : OOB;
  const char* codep;
  { unsigned long long pc; asm volatile("s_getpc_b64 %0" : "=s"(pc)); const unsigned co = (w * 64 + l) * 128; codep = (const char*)(pc & ~127ull) + (co < CODE_BYTES ? co : 0); }
#pragma unroll 1
  for (int t = 0; t < 15; ++t) {
    const unsigned xso = t < 14 ? (t + 2) * 128u : 0x80000000u, wso = t < 14 ? (t + 2) * (64u * 6144u) : OOB;
    const unsigned xd = t < 14 ? xdst + xp : lds0 + LDS_SPARE + 8192;
    if (__builtin_expect(t == 11, 0)) {
      if (pli >= 10 && pq >= 1024) pfp -= pstep;
      pstep = 0;
      dma4_sc1(lds0 + LDS_CTL + w * 256, ackoff, rws, 0);
      pf4(lds0 + LDS_SPARE + 8192, codep);
    }
    const unsigned pinc = pstep;
    TILE(true, true, WAIT_BAR(5));
  }
  bool acked = __all(!ack_ok || *(volatile LAS unsigned*)(size_t)(lds0 + LDS_CTL + w * 256 + l * 4) == (mybase ^ ACKX));
  __builtin_amdgcn_s_setprio(2);
  { const unsigned xso = 0, wso = 0, xd = 0, pinc = 0; TILE(false, false, (void)0); }
  asm volatile("s_nop 7" ::: "memory");
  _Pragma("unroll") for (int i_ = 0; i_ < 24; ++i_) PIN(acc[i_ >> 2][i_ & 3]);
  STAMP(2);
  bf16x8 qa0, qa1, qb0, qb1;
  f32x16 sa, sb, na, nb;
  {
    const unsigned ko = (g & 1) * 1024 + ((g >> 1) * 32 + (c & 7) + 16 * (c >> 3)) * 16;
    const unsigned vo = IMG + (g >> 1) * 1024 + ((g & 1) * 32 + c) * 16;
    const unsigned t0 = wn * 32768 + mp * 2 * TIL;
    const unsigned gs = R < 7 ? WS_KV + L * 65536 + t0 : OOB;
#pragma unroll
    for (int u = 0; u < 4; ++u) {
      const unsigned tb = (u >> 1) * TIL;
      const u32x4 kk = pack44(acc[2][u], acc[3][u]), vv = pack44(acc[4 + (u & 1)][u & 2], acc[4 + (u & 1)][(u & 2) + 1]);
      __builtin_amdgcn_raw_buffer_store_b128(kk, rws, ko + (u & 1) * 128 + tb, gs, SC1);
      __builtin_amdgcn_raw_buffer_store_b128(vv, rws, vo + (u & 1) * 256 + tb, gs, SC1);
      sts128(lds0 + LDS_OWN + t0 + tb + ko + (u & 1) * 128, kk);
      sts128(lds0 + LDS_OWN + t0 + tb + vo + (u & 1) * 256, vv);
    }
    lds_inc(lds0 + LDS_CNT + wn * 4);
    if (mp < 2) __builtin_amdgcn_s_setprio(1);
    u32x4 qf[4]; float sd[4];
#pragma unroll
    for (int u = 0; u < 4; ++u) {
      const f32x4 qx = acc[0][u] * QSCALE, qy = acc[1][u] * QSCALE;
      qf[u] = pack44(qx, qy);
      float d = qx[0] * acc[2][u][0];
#pragma unroll
      for (int i = 1; i < 4; ++i) d = __builtin_fmaf(qx[i], acc[2][u][i], d);
#pragma unroll
      for (int i = 0; i < 4; ++i) d = __builtin_fmaf(qy[i], acc[3][u][i], d);
      sd[u] = d;
    }
#pragma unroll
    for (int p = 0; p < 2; ++p) {
#pragma unroll
      for (int i = 0; i < 4; ++i) { auto r = __builtin_amdgcn_permlane16_swap(qf[2 * p][i], qf[2 * p + 1][i], false, false); qf[2 * p][i] = r[0]; qf[2 * p + 1][i] = r[1]; }
      auto r = __builtin_amdgcn_permlane16_swap(__float_as_uint(sd[2 * p]), __float_as_uint(sd[2 * p + 1]), false, false);
      sd[2 * p] = -swapsum(__uint_as_float(r[0]) + __uint_as_float(r[1]));
    }
    qb0 = asb(qf[0]); qb1 = asb(qf[1]); qa0 = asb(qf[2]); qa1 = asb(qf[3]);
#pragma unroll
    for (int i = 0; i < 16; ++i) { na[i] = sd[2]; nb[i] = sd[0]; }
  }
  float la = 0.f, lb = 0.f;
  bf16x8 k0, k1;
  PIN(na); PIN(nb); PIN(qa0); PIN(qa1); PIN(qb0); PIN(qb1);
  STAMP(3);
  const __amdgpu_buffer_rsrc_t rwo = mkrs((G >> 3) ? woB : woA, D_ * D_ * 2);
  const unsigned wov = (l >> 3) * 2048 + (((l & 7) ^ (((l >> 4) & 3) << 1)) << 4) + ns * 128;
#define WOB(gq) ((gq) == 0 ? 131072u : ((gq) - 1) * 32768u)
#define DMA_WO(j, ww) dma16(lds0 + WOB((j) >> 2) + ((j) & 3) * 8192 + (ww) * 1024, wov, rwo, (j) * (64u * 2048u) + (ww) * 16384u)
  const unsigned lb16 = l * 16, own = lds0 + LDS_OWN + wn * 32768, prv = R > 0 ? lds0 + wn * 98304 : own;
  const unsigned myflag = WS_FLAG + L * 128 + w * 4, pfoff = WS_FLAG + (L - 16) * 128 + wn * 16 + (l & 3) * 4;
#define QK0(S, Q, N) asm volatile("v_mfma_f32_32x32x16_bf16 %0, %1, %2, %3" : "=&v"(S) : "v"(k0), "v"(Q), "v"(N), "v"(k1))
#define QK1(S, Q) asm volatile("v_mfma_f32_32x32x16_bf16 %0, %1, %2, %0" : "+v"(S) : "v"(k1), "v"(Q))
#define PV0(O) asm volatile("v_mfma_f32_32x32x16_bf16 %0, %1, %2, %0" : "+v"(O) : "v"(v0), "v"(p0), "v"(v1))
#define PV1(O) asm volatile("v_mfma_f32_32x32x16_bf16 %0, %1, %2, %0" : "+v"(O) : "v"(v1), "v"(p1))
#define EX(S, i) do { S[i] = __builtin_amdgcn_exp2f(S[i]); } while (0)
#define RDK(a) do { k0 = lds128(a); k1 = lds128((a) + 1024); } while (0)
  unsigned pa = own + (2 * mp + 1) * TIL, pb = pa - TIL, pbp = pb;
  unsigned va = lb16 + pa, vb = lb16 + pb;
  RDK(va);
  asm volatile("s_nop 1" ::: "memory");
  QK0(sa, qa0, na); QK1(sa, qa1);
  RDK(vb);
  asm volatile("s_nop 7\n\ts_nop 1" ::: "memory");
  SBAR();
#define EXP(i) e_[i] = __builtin_amdgcn_exp2f(sx_[i])
#define HALF(SX, SY, NY, LY, OY, QY0, QY1, PY, RD, VX, KNEXT, BAR1, MSK, LSUM) do { \
    const f32x16 sx_ = SX; float e_[16]; u32x4 pn_; \
    EXP(0); EXP(1); EXP(2); EXP(3); EXP(4); asm volatile("" : "+v"(e_[0]), "+v"(e_[1]), "+v"(e_[2]), "+v"(e_[3]), "+v"(e_[4])); SBAR();     \
    EXP(5); asm volatile("" : "+v"(e_[5]), "+v"(Ls)); SBAR(); \
    unsigned long long bad_ = __builtin_amdgcn_ballot_w64(!(Ls[0] <= PBOUND)); asm volatile("" : "+s"(bad_)); SBAR(); \
    pn_[0] = pk2(e_[0], e_[1]); pn_[1] = pk2(e_[2], e_[3]); pn_[2] = pk2(e_[4], e_[5]); EXP(6); \
    asm volatile("" : "+v"(pn_[0]), "+v"(pn_[1]), "+v"(pn_[2]), "+v"(e_[6])); SBAR(); \
    if (__builtin_expect(bad_ != 0, 0)) redo_tile(PY, QY0, QY1, sel, OY, NY, LY, p0, p1, Ls); \
    PV0(OY); SBAR(); \
    EXP(7); EXP(8); LY += Ls[0]; pn_[3] = pk2(e_[6], e_[7]); asm volatile("" : "+v"(pn_[3]), "+v"(e_[8]), "+v"(LY)); SBAR(); \
    PV1(OY); SBAR(); if (RD) { v0 = lds128((VX) + IMG); v1 = lds128((VX) + IMG + 1024); VX = KNEXT; } \
    EXP(9); EXP(10); asm volatile("" : "+v"(e_[9]), "+v"(e_[10]), "+v"(VX)); SBAR(); \
    QK0(SY, QY0, NY); SBAR(); EXP(11); EXP(12); EXP(13); asm volatile("" : "+v"(e_[11]), "+v"(e_[12]), "+v"(e_[13])); SBAR(); \
    QK1(SY, QY1); SBAR(); \
    EXP(14); EXP(15); asm volatile("" : "+v"(e_[14]), "+v"(e_[15])); SBAR(); \
    if (BAR1) { WAITV(9); lds_inc(lds0 + LDS_CNT + 8); lds_wait(lds0 + LDS_CNT + wn * 4, 4); } \
    if (RD) RDK(VX); SBAR(); \
    p0 = pn_; p1[0] = pk2(e_[8], e_[9]); p1[1] = pk2(e_[10], e_[11]); p1[2] = pk2(e_[12], e_[13]); p1[3] = pk2(e_[14], e_[15]); SBAR(); \
    if (MSK) { p0 &= mk0; p1 &= mk1; PIN(p0); PIN(p1); asm volatile("s_nop 0"); } \
    LSUM; SBAR(); } while (0)
#define LMF0 "v_mfma_f32_16x16x32_bf16 %0, %1, %2, 0\n\t"
#define LMF1 "v_mfma_f32_16x16x32_bf16 %0, %1, %3, %0\n\t"
#define LSUM_G() asm volatile(LMF0 "s_nop 0\n\t" LMF1 "s_nop 1" : "=&v"(Ls) : "v"(sel), "v"(p0), "v"(p1))
#define LSUM_A() asm volatile(LMF0 "s_nop 0\n\t" LMF1 "s_nop 0" : "=&v"(Ls) : "v"(sel), "v"(p0), "v"(p1))
#define LSUM_B() asm volatile("v_mfma_f32_16x16x32_bf16 %0, %3, %4, 0\n\ts_mov_b32 %1, %2\n\tv_mfma_f32_16x16x32_bf16 %0, %3, %5, %0\n\ts_add_i32 %2, %2, 0x1000" : "=&v"(Ls), "=&s"(pbp), "+s"(pa) : "v"(sel), "v"(p0), "v"(p1) : "scc")
  unsigned sent = 0;

#define REPF() do {} while (0)
#define SIGNAL() do { WAITV(0); \
    _Pragma("unroll 1") for (unsigned sp = 0; !acked && sp < SPIN_CAP; ++sp) { __builtin_amdgcn_s_sleep(2); acked = __all(!ack_ok || ld32_wait(rws, ackoff) == (mybase ^ ACKX)); } \
    if (l == 0) st32(mybase + 1, rws, myflag); \
    REPF(); \
    sent = 1; if (mp >= 2) __builtin_amdgcn_s_setprio(0); STAMP(13); } while (0)
#define TRYSIG() do { if (__builtin_expect(!sent, 0)) { if ((__builtin_amdgcn_s_getreg(7 | (31 << 11)) & 0x00C0000Fu) == 0) SIGNAL(); } } while (0)
#pragma unroll 1
  for (int seg = 0;; ++seg) {
#pragma unroll 1
    for (int k = seg; k < 2 * seg + 1; ++k) {
      const unsigned pan = k == 0 ? own : prv + (2 * mp + k) * TIL, pbn = k == 1 ? pan - TIL : pan, md = k == 2 ? 2u : (k == 0 ? 1u : 0u);
      HALF(sa, sb, nb, lb, ob, qb0, qb1, pbp, true, va, lb16 + pan, k == 0, k != 1, LSUM_G());
      HALF(sb, sa, na, la, oa, qa0, qa1, pa | md, true, vb, lb16 + pbn, false, k != 1, LSUM_G());
      pbp = pb | md; pa = pan; pb = pbn;
      if (R == 0 && k == 1) break;
    }
    if (R == 0 && seg) break;
    int n = seg ? 6 - 2 * mp : 2 * mp;
    if (n > 0) {
      TRYSIG();
#pragma unroll 1
      do {
        HALF(sa, sb, nb, lb, ob, qb0, qb1, pbp, true, va, va + TIL, false, false, LSUM_A());
        HALF(sb, sa, na, la, oa, qa0, qa1, pa, false, vb, 0, false, false, LSUM_B());
        TRYSIG();
      } while (--n > 0);
    }
    if (seg) break;
    STAMP(4);
    if (!sent) SIGNAL();
    STAMP(5);
    mk0 = ~mk0; mk1 = ~mk1; PIN(mk0); PIN(mk1);
    if (mp < 2) {
      lds_wait(lds0 + LDS_CNT + 8, 8);
      unsigned h_ = wn; asm volatile("" : "+s"(h_));
      if (mp) {
#pragma unroll
        for (int j = 0; j < 12; ++j) DMA_WO(j >> 2, h_ * 4 + (j & 3));
      } else if (R > 0) {
#pragma unroll 1
        for (unsigned sp = 0; sp < SPIN_CAP; ++sp) {
          if (__all(ld32_wait(rws, pfoff) - pbase - 1u < 2u)) break;
          __builtin_amdgcn_s_sleep(1);
        }
        STAMP(14);
#pragma unroll
        for (int j = 0; j < 8; ++j) dma64_sc1(lds0 + h_ * 98304 + j * TIL, lb16, rws, WS_KV + (L - 16) * 65536 + h_ * 32768 + j * TIL);
        WAITV(0);
        lds_inc(lds0 + LDS_CNT + 12 + h_ * 4);
      }
    }
    STAMP(6);
    if (R > 0) lds_wait(lds0 + LDS_CNT + 12 + wn * 4, 1);
    STAMP(7);
    pb = prv + (2 * mp + 1) * TIL; vb = lb16 + pb;
    RDK(vb);
    SBAR();
  }
  asm volatile("s_nop 7" : "+v"(Ls) :: "memory");
  if (R > 0) {
    if (__builtin_expect(__any(!(Ls[0] <= PBOUND)), 0)) redo_tile(pbp, qb0, qb1, sel, ob, nb, lb, p0, p1, Ls);
    lb += Ls[0];
    PV0(ob); PV1(ob);
  }
  __builtin_amdgcn_s_setprio(0);
  STAMP(8);
  { const float ia = __builtin_amdgcn_rcpf(la), ib = __builtin_amdgcn_rcpf(lb);
    oa *= ia;
    const unsigned hh = hp * 2 + wn, T0 = br * 64 + R * 8 + 2 * mp;
    const unsigned ol = ((l >> 4) & 1) * 1024 + ((l >> 5) * 16 + (l & 15)) * 16;
    __builtin_amdgcn_raw_buffer_store_b128(pack8(oa, 0), rws, ol, WS_O + (T0 * 32 + 32 + hh) * IMG, SC1);
    __builtin_amdgcn_raw_buffer_store_b128(pack8(oa, 1), rws, ol + 512, WS_O + (T0 * 32 + 32 + hh) * IMG, SC1);
    SBAR();
    asm volatile("s_nop 7" : "+v"(ob) :: "memory");
    ob *= ib;
    __builtin_amdgcn_raw_buffer_store_b128(pack8(ob, 0), rws, ol, WS_O + (T0 * 32 + hh) * IMG, SC1);
    __builtin_amdgcn_raw_buffer_store_b128(pack8(ob, 1), rws, ol + 512, WS_O + (T0 * 32 + hh) * IMG, SC1); }
  WAITV(0);
  if (l == 0) st32(mybase + 2, rws, myflag);
  STAMP(9);
  asm volatile("s_barrier" ::: "memory");
#pragma unroll
  for (int j = 3; j < 16; ++j) DMA_WO(j, w);
  { const unsigned pw = (l & 1) ? 7 - (w >> 1) : (w >> 1);
    const unsigned fo = WS_FLAG + (G * 16 + ((l & 31) >> 1)) * 128 + pw * 4;
#pragma unroll 1
    for (unsigned sp = 0; sp < SPIN_CAP; ++sp) {
      const unsigned f = ld32_wait(rws, fo);
      if (__all(l >= 32 || f == obase + 2)) break;
      __builtin_amdgcn_s_sleep(1);
    } }
  STAMP(10);
  const unsigned T = G * 8 + w;
  const unsigned obs = WS_O + T * 32 * IMG;
  const unsigned wa2 = lds0 + (16 * (g >> 1) + 4 * (g & 1) + q4) * 128 + ((2 * (g & 1) + (q4 >> 1)) << 5) + (l & 3) * 8;
  f32x4 oc[4][2] = {};
  u32x4 of[16];
  s16x4 wl[2][4], wh[2][4];
#pragma unroll
  for (int i = 0; i < 16; ++i) of[i] = __builtin_amdgcn_raw_buffer_load_b128(rws, l * 16 + (i & 3) * 1024, obs + (i >> 2) * 4096, SC1);
#pragma unroll
  for (int nt = 0; nt < 4; ++nt) { wl[0][nt] = ldstr((wa2 ^ (nt << 5)) + WOB(0)); wh[0][nt] = ldstr((wa2 ^ (nt << 5)) + WOB(0) + 1024); }
  _Pragma("unroll") for (int i_ = 0; i_ < 8; ++i_) PIN(oc[i_ >> 1][i_ & 1]);
  asm volatile("s_nop 1" ::: "memory");
  SBAR();
#pragma unroll 1
  for (int gq = 0; gq < 4; ++gq) {
    const unsigned wb = wa2 + WOB(gq), wbn = wa2 + gq * 32768;
    const unsigned on = gq < 3 ? obs + (gq + 1) * 16384 : OOB;
    sfor<0, 8>([&](auto hi) AI { constexpr int h_ = decltype(hi)::v, cur = h_ & 1, nxt = cur ^ 1;
      if constexpr (h_ == 5) { if (gq == 0) asm volatile("s_barrier" ::: "memory"); }
      sfor<0, 8>([&](auto mi) AI { constexpr int m_ = decltype(mi)::v, nt = m_ >> 1, tt = m_ & 1;
        { const bf16x8 wf = cat(wl[cur][nt], wh[cur][nt]); MFA(oc[nt][tt], wf, of[2 * h_ + tt]); }
        { const unsigned wq_ = ((h_ < 7 ? wb : wbn) ^ (nt << 5)) + ((h_ + 1) & 7) * 4096;
          if constexpr (tt == 0) wl[nxt][nt] = ldstr(wq_); else wh[nxt][nt] = ldstr(wq_ + 1024); }
        if constexpr (m_ >= 6) of[2 * h_ + tt] = __builtin_amdgcn_raw_buffer_load_b128(rws, l * 16 + ((2 * h_ + tt) & 3) * 1024, on + ((2 * h_ + tt) >> 2) * 4096, SC1);
        SBAR(); }); });
  }
  asm volatile("s_nop 7" ::: "memory");
  _Pragma("unroll") for (int i_ = 0; i_ < 8; ++i_) PIN(oc[i_ >> 1][i_ & 1]);
  STAMP(11);
  const __amdgpu_buffer_rsrc_t rout = mkrs(out, S_ * 2 * D_ * 2);
  const unsigned orow = (((G & 7) * 8 + w) * 32 + c) * 4096 + (G >> 3) * 2048 + ns * 128 + (g & 1) * 32 + (g >> 1) * 16;
#pragma unroll
  for (int tt = 0; tt < 2; ++tt) {
#pragma unroll
    for (int np = 0; np < 2; ++np) {
      const f32x4 &a = oc[2 * np][tt], &b = oc[2 * np + 1][tt];
      auto r0 = __builtin_amdgcn_permlane16_swap(pk2(a[0], a[1]), pk2(b[0], b[1]), false, false);
      auto r1 = __builtin_amdgcn_permlane16_swap(pk2(a[2], a[3]), pk2(b[2], b[3]), false, false);
      u32x4 v = {r0[0], r1[0], r0[1], r1[1]};
      __builtin_amdgcn_raw_buffer_store_b128(v, rout, orow + tt * 65536 + np * 64, 0, 0);
    }
  }
}

extern "C" void kernel_launch(void* const* d_in, const int* in_sizes, int n_in, void* d_out, int out_size, void* d_ws, size_t ws_size,
                              hipStream_t stream) {
  const size_t wsoff = (ws_size - WS_END) & ~(size_t)0xfffff;
  k_fused<<<256, 512, 0, stream>>>(d_in[0], d_in[1], d_in[2], d_in[3], d_in[4], d_in[5], (char*)d_ws + wsoff, d_out);
}
```

```cpp
#define POLLS 8
#include <hip/hip_runtime.h>
#include <hip/hip_bf16.h>

typedef short bf16x8 __attribute__((ext_vector_type(8)));
typedef short s16x4 __attribute__((ext_vector_type(4)));
typedef float f32x16 __attribute__((ext_vector_type(16)));
typedef float f32x4 __attribute__((ext_vector_type(4)));
typedef unsigned u32x4 __attribute__((ext_vector_type(4)));
typedef __bf16 bf16x2_t __attribute__((ext_vector_type(2)));
typedef float f32x2 __attribute__((ext_vector_type(2)));
#define LAS __attribute__((address_space(3)))
#define DI __device__ __forceinline__
#define AI __attribute__((always_inline))
#define MFMA(a, b, c) __builtin_amdgcn_mfma_f32_32x32x16_bf16((a), (b), (c), 0, 0, 0)
#define SBAR() __builtin_amdgcn_sched_barrier(0)
#define PIN(x) asm volatile("" : "+v"(x))
#define MFA(acc_, a_, b_) asm volatile("v_mfma_f32_16x16x32_bf16 %0, %1, %2, %0" : "+v"(acc_) : "v"(a_), "v"(b_))

constexpr int S_ = 2048, D_ = 1024;
constexpr float QSCALE = 0.17677669529663687f * 1.4426950408889634f;
constexpr float PBOUND = 1073741824.0f;
constexpr unsigned IMG = 2048;
constexpr unsigned WS_FLAG = 0, WS_ACK = 32768, WS_KV = 65536, WS_O = WS_KV + 256u * 65536u, WS_END = WS_O + 2u * 64u * 32u * IMG;
constexpr unsigned ACKX = 0x5bd1e995u;
constexpr int SC1 = 16;
constexpr unsigned CODE_BYTES = 20224;
constexpr unsigned SPIN_CAP = 1u << 18;

#define HPMAP ((xcd & 1) * 8 + (idx >> 2))
#define RMAP (((xcd >> 1) & 1) * 4 + (idx & 3))
#define NOMEMX 1
template <int I> struct IC { static constexpr int v = I; };
template <int B, int E, class F> DI void sfor(F&& f) { if constexpr (B < E) { f(IC<B>{}); sfor<B + 1, E>(f); } }
DI unsigned rfl(unsigned x) { return (unsigned)__builtin_amdgcn_readfirstlane((int)x); }
DI unsigned pk2(float lo, float hi) { f32x2 v = {lo, hi}; return __builtin_bit_cast(unsigned, __builtin_convertvector(v, bf16x2_t)); }
DI u32x4 pack8(const f32x16& x, int s) {
  u32x4 p;
  p[0] = pk2(x[8 * s + 0], x[8 * s + 1]); p[1] = pk2(x[8 * s + 2], x[8 * s + 3]);
  p[2] = pk2(x[8 * s + 4], x[8 * s + 5]); p[3] = pk2(x[8 * s + 6], x[8 * s + 7]);
  return p;
}
DI u32x4 pack44(const f32x4& a, const f32x4& b) { u32x4 p; p[0] = pk2(a[0], a[1]); p[1] = pk2(a[2], a[3]); p[2] = pk2(b[0], b[1]); p[3] = pk2(b[2], b[3]); return p; }
DI bf16x8 asb(u32x4 v) { return __builtin_bit_cast(bf16x8, v); }
DI bf16x8 lds128(unsigned a) { return *(const LAS bf16x8*)(size_t)a; }
DI void sts128(unsigned a, u32x4 v) { *(LAS u32x4*)(size_t)a = v; }
DI s16x4 ldstr(unsigned a) { return __builtin_amdgcn_ds_read_tr16_b64_v4i16((LAS s16x4*)(size_t)a); }
DI bf16x8 cat(s16x4 lo, s16x4 hi) { return __builtin_shufflevector(lo, hi, 0, 1, 2, 3, 4, 5, 6, 7); }
DI void dma16(unsigned ldsdst, unsigned voff, __amdgpu_buffer_rsrc_t rs, unsigned soff) {
  asm volatile("s_mov_b32 m0, %0\n\ts_nop 0\n\tbuffer_load_dwordx4 %1, %2, %3 offen lds" ::"s"(ldsdst), "v"(voff), "s"(rs), "s"(soff) : "memory");
}
#define XPOL ""
#define WPOL ""
DI void dma16x(unsigned ldsdst, unsigned voff, __amdgpu_buffer_rsrc_t rs, unsigned soff) {
  asm volatile("s_mov_b32 m0, %0\n\ts_nop 0\n\tbuffer_load_dwordx4 %1, %2, %3 offen " XPOL " lds" ::"s"(ldsdst), "v"(voff), "s"(rs), "s"(soff) : "memory");
}
DI void dma16w(unsigned ldsdst, unsigned voff, __amdgpu_buffer_rsrc_t rs, unsigned soff) {
  asm volatile("s_mov_b32 m0, %0\n\ts_nop 0\n\tbuffer_load_dwordx4 %1, %2, %3 offen " WPOL " lds" ::"s"(ldsdst), "v"(voff), "s"(rs), "s"(soff) : "memory");
}
#define PPOL ""
#define POLLW 0
#define PFMODE 0
#define PFD 4
#define PFH 1
DI void pf4(unsigned ldsdst, const char* p) {
  asm volatile("s_mov_b32 m0, %0\n\ts_nop 0\n\tglobal_load_lds_dword %1, off " PPOL ::"s"(ldsdst), "v"(p) : "memory");
}
DI void dma16_sc1(unsigned ldsdst, unsigned voff, __amdgpu_buffer_rsrc_t rs, unsigned soff) {
  asm volatile("s_mov_b32 m0, %0\n\ts_nop 0\n\tbuffer_load_dwordx4 %1, %2, %3 offen sc1 lds" ::"s"(ldsdst), "v"(voff), "s"(rs), "s"(soff) : "memory");
}
#define WAITV(n) asm volatile("s_waitcnt vmcnt(" #n ")" ::: "memory")
#define WAIT_BAR(n) asm volatile("s_waitcnt vmcnt(" #n ") lgkmcnt(0)\n\ts_barrier" ::: "memory")
DI __amdgpu_buffer_rsrc_t mkrs(const void* p, unsigned bytes) { return __builtin_amdgcn_make_buffer_rsrc((void*)p, (short)0, (int)bytes, 0x00020000); }
DI unsigned ld32_issue(__amdgpu_buffer_rsrc_t rs, unsigned off) { unsigned r; asm volatile("buffer_load_dword %0, %1, %2, 0 offen sc1" : "=v"(r) : "v"(off), "s"(rs) : "memory"); return r; }
DI unsigned ld32_wait(__amdgpu_buffer_rsrc_t rs, unsigned off) { unsigned r; asm volatile("buffer_load_dword %0, %1, %2, 0 offen sc1\n\ts_waitcnt vmcnt(0)" : "=&v"(r) : "v"(off), "s"(rs) : "memory"); return r; }
DI u32x4 ld128_issue(__amdgpu_buffer_rsrc_t rs, unsigned off) { u32x4 r; asm volatile("buffer_load_dwordx4 %0, %1, %2, 0 offen sc1" : "=v"(r) : "v"(off), "s"(rs) : "memory"); return r; }
DI u32x4 ld128_wait(__amdgpu_buffer_rsrc_t rs, unsigned off) { u32x4 r; asm volatile("buffer_load_dwordx4 %0, %1, %2, 0 offen sc1\n\ts_waitcnt vmcnt(0)" : "=&v"(r) : "v"(off), "s"(rs) : "memory"); return r; }
DI void dma4(unsigned ldsdst, unsigned voff, __amdgpu_buffer_rsrc_t rs, unsigned soff) {
  asm volatile("s_mov_b32 m0, %0\n\ts_nop 0\n\tbuffer_load_dword %1, %2, %3 offen lds" ::"s"(ldsdst), "v"(voff), "s"(rs), "s"(soff) : "memory");
}
DI void dma4_sc1(unsigned ldsdst, unsigned voff, __amdgpu_buffer_rsrc_t rs, unsigned soff) {
  asm volatile("s_mov_b32 m0, %0\n\ts_nop 0\n\tbuffer_load_dword %1, %2, %3 offen sc1 lds" ::"s"(ldsdst), "v"(voff), "s"(rs), "s"(soff) : "memory");
}
#define WAIT_BAR_V(n, x) asm volatile("s_waitcnt vmcnt(" #n ") lgkmcnt(0)\n\ts_barrier" : "+v"(x) :: "memory")
constexpr unsigned OOB = 0x80000000u;
DI void st32(unsigned v, __amdgpu_buffer_rsrc_t rs, unsigned off) { __builtin_amdgcn_raw_buffer_store_b32(v, rs, off, 0, SC1); }

#define STAMP(i) do {} while (0)

constexpr unsigned XST = 32768, WST = 24576, LDS_W = 3 * XST, LDS_SPARE = LDS_W + 2 * WST, LDS_CTL = 159744;
static_assert(PFD == 4, "prefetch distance");
constexpr unsigned LDS_OWN = 32768, LDS_CNT = 162816, LDS_WOX = 98304, LDS_BYTES = 163840, TIL = 2 * IMG;

struct Frag { bf16x8 x[4]; s16x4 wl[6], wh[6]; };

DI void lds_inc(unsigned a) { asm volatile("s_mov_b64 exec, 1\n\tds_add_u32 %0, %1\n\ts_mov_b64 exec, -1" ::"v"(a), "v"(1u) : "memory"); }
DI unsigned lds_inc_rtn(unsigned a) { unsigned r; asm volatile("s_mov_b64 exec, 1\n\tds_add_rtn_u32 %0, %1, %2\n\ts_mov_b64 exec, -1\n\ts_waitcnt lgkmcnt(0)" : "=&v"(r) : "v"(a), "v"(1u) : "memory"); return rfl(r); }
DI void lds_wait(unsigned a, unsigned n) {
#pragma unroll 1
  for (unsigned sp = 0; sp < SPIN_CAP && rfl(*(volatile LAS unsigned*)(size_t)a) < n; ++sp) __builtin_amdgcn_s_sleep(1);
  asm volatile("" ::: "memory");
}
DI void dma64_sc1(unsigned ldsdst, unsigned voff, __amdgpu_buffer_rsrc_t rs, unsigned soff) {
  asm volatile("s_mov_b32 m0, %0\n\ts_nop 0\n\tbuffer_load_dwordx4 %1, %2, %3 offen sc1 lds\n\tbuffer_load_dwordx4 %1, %2, %3 offen offset:1024 sc1 lds\n\t"
               "buffer_load_dwordx4 %1, %2, %3 offen offset:2048 sc1 lds\n\tbuffer_load_dwordx4 %1, %2, %3 offen offset:3072 sc1 lds" ::"s"(ldsdst), "v"(voff), "s"(rs), "s"(soff) : "memory");
}
DI float swapmax(float m) {
  auto rr = __builtin_amdgcn_permlane32_swap(__float_as_uint(m), __float_as_uint(m), false, false);
  return __builtin_fmaxf(__uint_as_float(rr[0]), __uint_as_float(rr[1]));
}
DI float swapsum(float m) {
  auto rr = __builtin_amdgcn_permlane32_swap(__float_as_uint(m), __float_as_uint(m), false, false);
  return __uint_as_float(rr[0]) + __uint_as_float(rr[1]);
}
#define MX3(a, b, c) __builtin_fmaxf(__builtin_fmaxf((a), (b)), (c))
DI float max16(const f32x16& p) {
  float a = MX3(p[0], p[1], p[2]), b = MX3(p[3], p[4], p[5]);
  a = MX3(a, p[6], p[7]); b = MX3(b, p[8], p[9]); a = MX3(a, p[10], p[11]); b = MX3(b, p[12], p[13]);
  a = MX3(a, p[14], p[15]);
  return __builtin_fmaxf(a, b);
}
DI int keyidx(int i) { return (i & 3) + 16 * ((i >> 2) & 1) + 8 * (i >> 3); }
DI void redo_tile(unsigned tile, bf16x8 q0, bf16x8 q1, bf16x8 sel, f32x16& o, f32x16& negm, float& l, u32x4& p0, u32x4& p1, f32x4& L) {
  int ln = threadIdx.x & 63; PIN(ln);
  const unsigned ka = (tile & ~3u) + ln * 16, mode = tile & 3u;
  const bf16x8 k0 = lds128(ka), k1 = lds128(ka + 1024);
  const int rr = (ln & 31) - 4 * (ln >> 5);
  f32x16 s;
#pragma unroll
  for (int i = 0; i < 16; ++i) s[i] = (mode != 0 && (keyidx(i) <= rr) == (mode == 2)) ? -__builtin_inff() : negm[0];
  s = MFMA(k0, q0, s); s = MFMA(k1, q1, s);
  const float d = __builtin_fmaxf(swapmax(max16(s)), 0.f), f = __builtin_amdgcn_exp2f(-d), nm = negm[0] - d;
  l *= f;
#pragma unroll
  for (int i = 0; i < 16; ++i) { negm[i] = nm; o[i] *= f; s[i] = __builtin_amdgcn_exp2f(s[i] - d); }
  p0 = pack8(s, 0); p1 = pack8(s, 1);
  f32x4 z = {};
  L = __builtin_amdgcn_mfma_f32_16x16x32_bf16(sel, asb(p0), z, 0, 0, 0);
  L = __builtin_amdgcn_mfma_f32_16x16x32_bf16(sel, asb(p1), L, 0, 0, 0);
  float t = L[0]; PIN(t); L[0] = t;
  PIN(p0); PIN(p1); PIN(o); PIN(negm);
  asm volatile("s_nop 1" ::: "memory");
}

__global__ __launch_bounds__(512, 2) void k_fused(const void* xA, const void* wA, const void* woA, const void* xB, const void* wB, const void* woB,
                                                  void* ws, void* out) {
  __shared__ __attribute__((aligned(1024))) char smem[LDS_BYTES];
  const unsigned lds0 = (unsigned)(size_t)(LAS char*)smem;
  const unsigned bid = blockIdx.x, xcd = bid & 7, idx = bid >> 3;
  const unsigned br = xcd >> 2, R = RMAP, hp = HPMAP;
  const unsigned L = (br * 8 + R) * 16 + hp;
  const unsigned G = br * 8 + R, ns = hp, loc = hp >> 3;
  const unsigned tid = threadIdx.x, l = tid & 63, w = rfl(tid >> 6), wm = w & 3, wn = w >> 2;
  const __amdgpu_buffer_rsrc_t rx = mkrs(br ? xB : xA, NOMEMX * S_ * D_ * 2), rw = mkrs(br ? wB : wA, D_ * 3 * D_ * 2), rws = mkrs(ws, WS_END);
  STAMP(0);
  const unsigned fl_id = l < 32 ? G * 16 + (l >> 1) : (l == 32 ? L - 16 : L);
  const bool fl_ok = l < 34 && !(l == 32 && R == 0);
  unsigned obase = ld32_issue(rws, fl_ok ? WS_FLAG + fl_id * 128 : OOB);
  const unsigned mtw = w, mp = wn ? 3 - wm : wm;
  const unsigned xv0 = (R * 256 + mtw * 32 + (l >> 3)) * 2048 + (((l & 7) ^ (l >> 4)) << 4);
  const unsigned xv1 = (R * 256 + mtw * 32 + 8 + (l >> 3)) * 2048 + (((l & 7) ^ (4 + (l >> 4))) << 4);
  const unsigned wv = (8 * w + (l >> 3)) * 6144 + (((l & 7) ^ ((((l >> 4) & 1) | ((w & 1) << 1)) << 1)) << 4) + hp * 128;
  const unsigned xdst = lds0 + w * 4096, wdst = lds0 + LDS_W + w * 1024;
#define DMA_X(t, st) do { const unsigned so_ = (t) * 128u, d_ = xdst + (st); \
    dma16x(d_, xv0, rx, so_); dma16x(d_ + 1024, xv1, rx, so_); dma16x(d_ + 2048, xv0, rx, so_ + 32768); dma16x(d_ + 3072, xv1, rx, so_ + 32768); } while (0)
#define DMA_W(t, st) do { const unsigned so_ = (t) * (64u * 6144u), d_ = wdst + (st); \
    dma16w(d_, wv, rw, so_); dma16w(d_ + 8192, wv, rw, so_ + 2048); dma16w(d_ + 16384, wv, rw, so_ + 4096); } while (0)
  DMA_W(0, 0); DMA_X(0, 0); DMA_W(1, WST); DMA_X(1, XST);
  const unsigned pli = l < 13 ? l : 0;
  const unsigned pwl = ((idx & 3) * 8 + w) * 6 + (pli - 4);
  const unsigned pq = (idx * 8 + w) * 3 + (pli - 10);
  const char* pfp = pli < 4 ? (const char*)(br ? xB : xA) + (R * 256 + ((idx >> 2) * 8 + w) * 4 + pli) * 2048 + (PFD - 1) * 128
                  : pli < 10 ? (const char*)(br ? wB : wA) + (pwl / 3 + (PFD - 1) * 64) * 6144 + (pwl % 3) * 2048 + hp * 128
                             : (const char*)(br ? woB : woA) + (pq >> 3) * 2048 + loc * 1024 + (pq & 7) * 128;
  unsigned pstep = pli < 4 ? 128u : pli < 10 ? 64u * 6144u : 77u * 2048u;
  pf4(lds0 + LDS_CNT + 256, pfp); pfp += pstep;
#define CODEPF(bytes) do { unsigned long long pc; asm volatile("s_getpc_b64 %0" : "=s"(pc));     \
    const unsigned co = (w * 64 + l) * 128; \
    pf4(lds0 + LDS_CNT + 256, (const char*)(pc & ~127ull) + (co < (bytes) ? co : 0)); } while (0)
  if (tid < 8) *(LAS unsigned*)(size_t)(lds0 + LDS_CNT + tid * 4) = 0;
  const unsigned c = l & 15, g = l >> 4, q4 = c >> 2;
  const unsigned xa = lds0 + mp * 8192 + c * 128 + ((g ^ (c >> 1)) << 4);
  const unsigned wa = lds0 + LDS_W + (8 * g + q4) * 128 + (((2 * wn) ^ ((q4 >> 1) | ((g & 1) << 1))) << 5) + (l & 3) * 8;
  f32x4 acc[6][4] = {};
  u32x4 mk0, mk1; unsigned sel1 = ((c & 3) == 0 && ((c >> 2) & 1) == (g & 1)) ? 0x3F803F80u : 0u;
  { const int rr = (int)(l & 31) - 4 * (int)(l >> 5);
#pragma unroll
    for (int i = 0; i < 4; ++i) {
      mk0[i] = (keyidx(2 * i) <= rr ? 0xFFFFu : 0u) | (keyidx(2 * i + 1) <= rr ? 0xFFFF0000u : 0u);
      mk1[i] = (keyidx(8 + 2 * i) <= rr ? 0xFFFFu : 0u) | (keyidx(9 + 2 * i) <= rr ? 0xFFFF0000u : 0u); }
    PIN(mk0); PIN(mk1); PIN(sel1); }
  f32x16 oa, ob; f32x4 Ls; u32x4 p0, p1; bf16x8 v0, v1, sel;
  Frag A, B;
#define RDX(F, xb, s, u) F.x[u] = lds128(((xb) ^ ((s) << 6)) + (u) * 2048)
#define MM(C, n, u) do { const bf16x8 wf_ = cat(C.wl[n], C.wh[n]); if ((n) < 4) MFA(acc[n][u], wf_, C.x[u]); else MFA(acc[n][u], C.x[u], wf_); } while (0)
#define RDWL(F, wb, s, n) F.wl[n] = ldstr(((wb) ^ (((n) & 1) << 5)) + ((n) >> 1) * 8192 + (s) * 4096)
#define RDWH(F, wb, s, n) F.wh[n] = ldstr(((wb) ^ (((n) & 1) << 5)) + ((n) >> 1) * 8192 + (s) * 4096 + 512)
#define RD1(F, xb, wb, s, r) do { if ((r) == 0) RDX(F, xb, s, 0); else if ((r) == 1) RDWL(F, wb, s, 0); else if ((r) == 2) RDWH(F, wb, s, 0); \
    else if ((r) < 6) RDX(F, xb, s, ((r) - 2) & 3); else if (((r) & 1) == 0) RDWL(F, wb, s, (((r) + 8) >> 1) % 6); else RDWH(F, wb, s, (((r) + 8) >> 1) % 6); } while (0)
#define PB 27
#define PFP 36
#define BP(r) ((r) * 3 / 2)
#define AP(r) (29 + (r) * 6 / 5)
#define XP(j) (2 + (j) * 6)
#define WP(j) (28 + (j) * 3)
#define TILE(LDA, DMA, WAITB) do { \
    const unsigned xb = xa + xs, wb = wa + wsx, wd = wdst + wsx; \
    xp = xs; xs = (xs == 2 * XST) ? 0 : xs + XST; wsx ^= WST; \
    const unsigned xn = xa + xs, wn_ = wa + wsx; \
    sfor<0, 48>([&](auto mi) AI { constexpr int m_ = decltype(mi)::v; \
      if constexpr (m_ < 24) MM(A, m_ >> 2, m_ & 3); else MM(B, (m_ - 24) >> 2, m_ & 3); \
      sfor<0, 16>([&](auto ri) AI { constexpr int r_ = decltype(ri)::v; \
        if constexpr (BP(r_) == m_) RD1(B, xb, wb, 1, r_); if constexpr (LDA && AP(r_) == m_) RD1(A, xn, wn_, 0, r_); }); \
      sfor<0, 4>([&](auto ji) AI { constexpr int j_ = decltype(ji)::v; \
        if constexpr (DMA && XP(j_) == m_) dma16x(xd + j_ * 1024, (j_ & 1) ? xv1 : xv0, rx, xso + (j_ >> 1) * 32768); \
        if constexpr (DMA && j_ < 3 && WP(j_) == m_) dma16w(wd + j_ * 8192, wv, rw, wso + j_ * 2048); }); \
      if constexpr (DMA && m_ == PFP) { pf4(lds0 + LDS_CNT + 256, pfp); pfp += pinc; } \
      if constexpr (!LDA) {     \
        if constexpr (m_ == 28) { oa = f32x16{}; PIN(oa); } if constexpr (m_ == 34) { ob = f32x16{}; PIN(ob); } \
        if constexpr (m_ == 40) { p0 = u32x4{}; p1 = u32x4{}; v0 = bf16x8{}; v1 = bf16x8{}; PIN(p0); PIN(p1); PIN(v0); PIN(v1); } \
        if constexpr (m_ == 44) { Ls = f32x4{}; sel = asb(u32x4{sel1, sel1, sel1, sel1}); PIN(Ls); PIN(sel); } } \
      SBAR(); \
      if constexpr (m_ == PB) { WAITB; SBAR(); } }); } while (0)
  WAIT_BAR_V(8, obase);
  STAMP(1);
  unsigned xs = 0, wsx = 0, xp = 2 * XST;
  sfor<0, 16>([&](auto ri) AI { constexpr int r_ = decltype(ri)::v; RD1(A, xa, wa, 0, r_); SBAR(); });
  if (w == 0) st32(obase ^ ACKX, rws, (fl_ok && l <= 32 && !(l & 1)) ? WS_ACK + fl_id * 128 + (l < 32 ? ns : 16) * 4 : OOB);
  const unsigned pbase = __builtin_amdgcn_readlane(obase, 32), mybase = __builtin_amdgcn_readlane(obase, 33);
  _Pragma("unroll") for (int i_ = 0; i_ < 24; ++i_) PIN(acc[i_ >> 2][i_ & 3]);
  asm volatile("s_nop 1" ::: "memory");
  SBAR();
  const bool ack_ok = l < 17 && !(l == 16 && R == 7);
  const unsigned ackoff = ack_ok ? WS_ACK + L * 128 + l * 4 : OOB;
  const char* codep;
  { unsigned long long pc; asm volatile("s_getpc_b64 %0" : "=s"(pc)); const unsigned co = (w * 64 + l) * 128; codep = (const char*)(pc & ~127ull) + (co < CODE_BYTES ? co : 0); }
#pragma unroll 1
  for (int t = 0; t < 15; ++t) {
    const unsigned xso = t < 14 ? (t + 2) * 128u : 0x80000000u, wso = t < 14 ? (t + 2) * (64u * 6144u) : OOB;
    const unsigned xd = t < 14 ? xdst + xp : lds0 + LDS_SPARE;
    if (__builtin_expect((unsigned)(t - 11) < 2u, 0)) {
      if (t == 11) {
        dma4_sc1(lds0 + LDS_CTL + w * 256, ackoff, rws, 0);
        pf4(lds0 + LDS_CNT + 256, codep);
      } else {
        pfp = (const char*)(br ? woB : woA) + ((idx * 8 + w) * 4 + ((l >> 3) & 3)) * 2048 + loc * 1024 + (l & 7) * 128; pstep = 0;
      }
    }
    const unsigned pinc = pstep;
    TILE(true, true, WAIT_BAR(5));
  }
  bool acked = __all(!ack_ok || *(volatile LAS unsigned*)(size_t)(lds0 + LDS_CTL + w * 256 + l * 4) == (mybase ^ ACKX));
  __builtin_amdgcn_s_setprio(2);
  { const unsigned xso = 0, wso = 0, xd = 0, pinc = 0; TILE(false, false, (void)0); }
  asm volatile("s_nop 7" ::: "memory");
  _Pragma("unroll") for (int i_ = 0; i_ < 24; ++i_) PIN(acc[i_ >> 2][i_ & 3]);
  STAMP(2);
  bf16x8 qa0, qa1, qb0, qb1;
  f32x16 sa, sb, na, nb;
  {
    const unsigned ko = (g & 1) * 1024 + ((g >> 1) * 32 + (c & 7) + 16 * (c >> 3)) * 16;
    const unsigned vo = IMG + (g >> 1) * 1024 + ((g & 1) * 32 + c) * 16;
    const unsigned t0 = wn * 32768 + mp * 2 * TIL;
    const unsigned gs = R < 7 ? WS_KV + L * 65536 + t0 : OOB;
#pragma unroll
    for (int u = 0; u < 4; ++u) {
      const unsigned tb = (u >> 1) * TIL;
      const u32x4 kk = pack44(acc[2][u], acc[3][u]), vv = pack44(acc[4 + (u & 1)][u & 2], acc[4 + (u & 1)][(u & 2) + 1]);
      __builtin_amdgcn_raw_buffer_store_b128(kk, rws, ko + (u & 1) * 128 + tb, gs, SC1);
      __builtin_amdgcn_raw_buffer_store_b128(vv, rws, vo + (u & 1) * 256 + tb, gs, SC1);
      sts128(lds0 + LDS_OWN + t0 + tb + ko + (u & 1) * 128, kk);
      sts128(lds0 + LDS_OWN + t0 + tb + vo + (u & 1) * 256, vv);
    }
    lds_inc(lds0 + LDS_CNT + wn * 4);
    if (mp < 2) __builtin_amdgcn_s_setprio(1);
    u32x4 qf[4]; float sd[4];
#pragma unroll
    for (int u = 0; u < 4; ++u) {
      const f32x4 qx = acc[0][u] * QSCALE, qy = acc[1][u] * QSCALE;
      qf[u] = pack44(qx, qy);
      float d = qx[0] * acc[2][u][0];
#pragma unroll
      for (int i = 1; i < 4; ++i) d = __builtin_fmaf(qx[i], acc[2][u][i], d);
#pragma unroll
      for (int i = 0; i < 4; ++i) d = __builtin_fmaf(qy[i], acc[3][u][i], d);
      sd[u] = d;
    }
#pragma unroll
    for (int p = 0; p < 2; ++p) {
#pragma unroll
      for (int i = 0; i < 4; ++i) { auto r = __builtin_amdgcn_permlane16_swap(qf[2 * p][i], qf[2 * p + 1][i], false, false); qf[2 * p][i] = r[0]; qf[2 * p + 1][i] = r[1]; }
      auto r = __builtin_amdgcn_permlane16_swap(__float_as_uint(sd[2 * p]), __float_as_uint(sd[2 * p + 1]), false, false);
      sd[2 * p] = -swapsum(__uint_as_float(r[0]) + __uint_as_float(r[1]));
    }
    qb0 = asb(qf[0]); qb1 = asb(qf[1]); qa0 = asb(qf[2]); qa1 = asb(qf[3]);
#pragma unroll
    for (int i = 0; i < 16; ++i) { na[i] = sd[2]; nb[i] = sd[0]; }
  }
  float la = 0.f, lb = 0.f;
  bf16x8 k0, k1;
  PIN(na); PIN(nb); PIN(qa0); PIN(qa1); PIN(qb0); PIN(qb1);
  STAMP(3);
  const __amdgpu_buffer_rsrc_t rwo = mkrs(br ? woB : woA, D_ * D_ * 2);
  const unsigned wov = (l >> 3) * 2048 + (((l & 7) ^ (((l >> 4) & 3) << 1)) << 4) + ns * 128;
#define WOB(gq) ((gq) == 0 ? 131072u : ((gq) & 3) == 3 ? 0u : (gq) * 32768u)
#define DMA_WO(j, ww) dma16(lds0 + WOB((j) >> 2) + ((j) & 3) * 8192 + (ww) * 1024, wov, rwo, ((j) ^ (loc * 8)) * (64u * 2048u) + (ww) * 16384u)
  const unsigned lb16 = l * 16, own = lds0 + LDS_OWN + wn * 32768, prv = R > 0 ? lds0 + wn * 98304 : own;
  const unsigned myflag = WS_FLAG + L * 128, pfoff = WS_FLAG + (L - 16) * 128 + 4 + wn * 4;
#define QK0(S, Q, N) asm volatile("v_mfma_f32_32x32x16_bf16 %0, %1, %2, %3" : "=&v"(S) : "v"(k0), "v"(Q), "v"(N), "v"(k1))
#define QK1(S, Q) asm volatile("v_mfma_f32_32x32x16_bf16 %0, %1, %2, %0" : "+v"(S) : "v"(k1), "v"(Q))
#define PV0(O) asm volatile("v_mfma_f32_32x32x16_bf16 %0, %1, %2, %0" : "+v"(O) : "v"(v0), "v"(p0), "v"(v1))
#define PV1(O) asm volatile("v_mfma_f32_32x32x16_bf16 %0, %1, %2, %0" : "+v"(O) : "v"(v1), "v"(p1))
#define EX(S, i) do { S[i] = __builtin_amdgcn_exp2f(S[i]); } while (0)
#define RDK(a) do { k0 = lds128(a); k1 = lds128((a) + 1024); } while (0)
  unsigned pa = own + (2 * mp + 1) * TIL, pb = pa - TIL, pbp = pb;
  unsigned va = lb16 + pa, vb = lb16 + pb;
  RDK(va);
  asm volatile("s_nop 1" ::: "memory");
  QK0(sa, qa0, na); QK1(sa, qa1);
  RDK(vb);
  asm volatile("s_nop 7\n\ts_nop 1" ::: "memory");
  SBAR();
#define EXP(i) e_[i] = __builtin_amdgcn_exp2f(sx_[i])
#define HALF(SX, SY, NY, LY, OY, QY0, QY1, PY, RD, VX, KNEXT, BAR1, MSK, LSUM) do { \
    const f32x16 sx_ = SX; float e_[16]; u32x4 pn_; \
    EXP(0); EXP(1); EXP(2); EXP(3); EXP(4); asm volatile("" : "+v"(e_[0]), "+v"(e_[1]), "+v"(e_[2]), "+v"(e_[3]), "+v"(e_[4])); SBAR();     \
    EXP(5); asm volatile("" : "+v"(e_[5]), "+v"(Ls)); SBAR(); \
    unsigned long long bad_ = __builtin_amdgcn_ballot_w64(!(Ls[0] <= PBOUND)); asm volatile("" : "+s"(bad_)); SBAR(); \
    pn_[0] = pk2(e_[0], e_[1]); pn_[1] = pk2(e_[2], e_[3]); pn_[2] = pk2(e_[4], e_[5]); EXP(6); \
    asm volatile("" : "+v"(pn_[0]), "+v"(pn_[1]), "+v"(pn_[2]), "+v"(e_[6])); SBAR(); \
    if (__builtin_expect(bad_ != 0, 0)) redo_tile(PY, QY0, QY1, sel, OY, NY, LY, p0, p1, Ls); \
    PV0(OY); SBAR(); \
    EXP(7); EXP(8); LY += Ls[0]; pn_[3] = pk2(e_[6], e_[7]); asm volatile("" : "+v"(pn_[3]), "+v"(e_[8]), "+v"(LY)); SBAR(); \
    PV1(OY); SBAR(); if (RD) { v0 = lds128((VX) + IMG); v1 = lds128((VX) + IMG + 1024); VX = KNEXT; } \
    EXP(9); EXP(10); asm volatile("" : "+v"(e_[9]), "+v"(e_[10]), "+v"(VX)); SBAR(); \
    QK0(SY, QY0, NY); SBAR(); EXP(11); EXP(12); EXP(13); asm volatile("" : "+v"(e_[11]), "+v"(e_[12]), "+v"(e_[13])); SBAR(); \
    QK1(SY, QY1); SBAR(); \
    EXP(14); EXP(15); asm volatile("" : "+v"(e_[14]), "+v"(e_[15])); SBAR(); \
    if (BAR1) { WAITV(9); lds_inc(lds0 + LDS_CNT + 8); lds_wait(lds0 + LDS_CNT + wn * 4, 4); } \
    if (RD) RDK(VX); SBAR(); \
    p0 = pn_; p1[0] = pk2(e_[8], e_[9]); p1[1] = pk2(e_[10], e_[11]); p1[2] = pk2(e_[12], e_[13]); p1[3] = pk2(e_[14], e_[15]); SBAR(); \
    if (MSK) { p0 &= mk0; p1 &= mk1; PIN(p0); PIN(p1); asm volatile("s_nop 0"); } \
    LSUM; SBAR(); } while (0)
#define LMF0 "v_mfma_f32_16x16x32_bf16 %0, %1, %2, 0\n\t"
#define LMF1 "v_mfma_f32_16x16x32_bf16 %0, %1, %3, %0\n\t"
#define LSUM_G() asm volatile(LMF0 "s_nop 0\n\t" LMF1 "s_nop 1" : "=&v"(Ls) : "v"(sel), "v"(p0), "v"(p1))
#define LSUM_A() asm volatile(LMF0 "s_nop 0\n\t" LMF1 "s_nop 0" : "=&v"(Ls) : "v"(sel), "v"(p0), "v"(p1))
#define LSUM_B() asm volatile("v_mfma_f32_16x16x32_bf16 %0, %3, %4, 0\n\ts_mov_b32 %1, %2\n\tv_mfma_f32_16x16x32_bf16 %0, %3, %5, %0\n\ts_add_i32 %2, %2, 0x1000" : "=&v"(Ls), "=&s"(pbp), "+s"(pa) : "v"(sel), "v"(p0), "v"(p1) : "scc")
  unsigned sent = 0;

#define REPF() do {} while (0)
#define SIGNAL() do { WAITV(0); \
    _Pragma("unroll 1") for (unsigned sp = 0; !acked && sp < SPIN_CAP; ++sp) { __builtin_amdgcn_s_sleep(2); acked = __all(!ack_ok || ld32_wait(rws, ackoff) == (mybase ^ ACKX)); } \
    if (lds_inc_rtn(lds0 + LDS_CNT + 24 + wn * 4) == 3 && l == 0) st32(mybase + 1, rws, myflag + 4 + wn * 4); \
    REPF(); \
    sent = 1; if (mp >= 2) __builtin_amdgcn_s_setprio(0); STAMP(13); } while (0)
#define TRYSIG() do { if (__builtin_expect(!sent, 0)) { if ((__builtin_amdgcn_s_getreg(7 | (31 << 11)) & 0x00C0000Fu) == 0) SIGNAL(); } } while (0)
#pragma unroll 1
  for (int seg = 0;; ++seg) {
#pragma unroll 1
    for (int k = seg; k < 2 * seg + 1; ++k) {
      const unsigned pan = k == 0 ? own : prv + (2 * mp + k) * TIL, pbn = k == 1 ? pan - TIL : pan, md = k == 2 ? 2u : (k == 0 ? 1u : 0u);
      HALF(sa, sb, nb, lb, ob, qb0, qb1, pbp, true, va, lb16 + pan, k == 0, k != 1, LSUM_G());
      HALF(sb, sa, na, la, oa, qa0, qa1, pa | md, true, vb, lb16 + pbn, false, k != 1, LSUM_G());
      pbp = pb | md; pa = pan; pb = pbn;
      if (k == 1) { lds_inc(lds0 + LDS_CNT + 20); if (R == 0) break; }
    }
    if (R == 0 && seg) break;
    int n = seg ? 6 - 2 * mp : 2 * mp;
    if (n > 0) {
      TRYSIG();
#pragma unroll 1
      do {
        HALF(sa, sb, nb, lb, ob, qb0, qb1, pbp, true, va, va + TIL, false, false, LSUM_A());
        HALF(sb, sa, na, la, oa, qa0, qa1, pa, false, vb, 0, false, false, LSUM_B());
        TRYSIG();
      } while (--n > 0);
    }
    if (seg) break;
    STAMP(4);
    if (!sent) SIGNAL();
    STAMP(5);
    mk0 = ~mk0; mk1 = ~mk1; PIN(mk0); PIN(mk1);
    if (mp < 2) {
      lds_wait(lds0 + LDS_CNT + 8, 8);
      unsigned h_ = wn; asm volatile("" : "+s"(h_));
      if (mp) {
#pragma unroll
        for (int j = 0; j < 15; ++j) DMA_WO(j >> 2, h_ * 4 + (j & 3));
        if (!h_) DMA_WO(3, 3);
      } else if (R > 0) {
#pragma unroll 1
        for (unsigned sp = 0; sp < SPIN_CAP; ++sp) {
          if (rfl(ld32_wait(rws, pfoff)) == pbase + 1) break;
          __builtin_amdgcn_s_sleep(1);
        }
        STAMP(14);
#pragma unroll
        for (int j = 0; j < 8; ++j) dma64_sc1(lds0 + h_ * 98304 + j * TIL, lb16, rws, WS_KV + (L - 16) * 65536 + h_ * 32768 + j * TIL);
        WAITV(0);
        lds_inc(lds0 + LDS_CNT + 12 + h_ * 4);
      }
    }
    STAMP(6);
    if (R > 0) lds_wait(lds0 + LDS_CNT + 12 + wn * 4, 1);
    STAMP(7);
    pb = prv + (2 * mp + 1) * TIL; vb = lb16 + pb;
    RDK(vb);
    SBAR();
  }
  asm volatile("s_nop 7" : "+v"(Ls) :: "memory");
  if (R > 0) {
    if (__builtin_expect(__any(!(Ls[0] <= PBOUND)), 0)) redo_tile(pbp, qb0, qb1, sel, ob, nb, lb, p0, p1, Ls);
    lb += Ls[0];
    PV0(ob); PV1(ob);
  }
  __builtin_amdgcn_s_setprio(0);
  STAMP(8);
  { const float ia = __builtin_amdgcn_rcpf(la), ib = __builtin_amdgcn_rcpf(lb);
    oa *= ia;
    const unsigned hh = hp * 2 + wn, T0 = br * 64 + R * 8 + 2 * mp;
    const unsigned ol = ((l >> 4) & 1) * 1024 + ((l >> 5) * 16 + (l & 15)) * 16;
    __builtin_amdgcn_raw_buffer_store_b128(pack8(oa, 0), rws, ol, WS_O + (T0 * 32 + 32 + hh) * IMG, SC1);
    __builtin_amdgcn_raw_buffer_store_b128(pack8(oa, 1), rws, ol + 512, WS_O + (T0 * 32 + 32 + hh) * IMG, SC1);
    SBAR();
    asm volatile("s_nop 7" : "+v"(ob) :: "memory");
    ob *= ib;
    __builtin_amdgcn_raw_buffer_store_b128(pack8(ob, 0), rws, ol, WS_O + (T0 * 32 + hh) * IMG, SC1);
    __builtin_amdgcn_raw_buffer_store_b128(pack8(ob, 1), rws, ol + 512, WS_O + (T0 * 32 + hh) * IMG, SC1); }
  WAITV(0);
  STAMP(9);
  if (mp >= 2) {
    lds_wait(lds0 + LDS_CNT + 20, 8);
    unsigned w2_ = (wn * 2 + (mp & 1)) * 2; asm volatile("" : "+s"(w2_));
#pragma unroll
    for (int j = 0; j < 16; ++j) DMA_WO(4 + (j >> 1), w2_ + (j & 1));
  }
  asm volatile("s_barrier" ::: "memory");
  if (tid == 64) st32(mybase + 2, rws, myflag);
  dma4(lds0 + LDS_WOX + 1024, l < 32 ? (l >> 4) * 65536 + (l & 15) * 128 : OOB, rws, WS_O + ((br * 64 + R * 8 + 2 * mp) * 32 + hp * 2 + wn) * IMG);
  if (w == 7) DMA_WO(3, 7);
  const unsigned fo = l < 32 ? WS_FLAG + (G * 16 + (l >> 1)) * 128 : OOB;
#pragma unroll
  for (int j = 12; j < 16; ++j) DMA_WO(j, w);
  if (w == POLLW) {
    unsigned f;
#pragma unroll 1
    for (unsigned sp = 0; sp < SPIN_CAP; ++sp) {
      f = ld32_wait(rws, fo);
      if (__all((l >> 4) != loc || f == obase + 2)) break;
      __builtin_amdgcn_s_sleep(POLLS);
    }
    *(volatile LAS unsigned*)(size_t)(lds0 + LDS_WOX) = __all(l >= 32 || f == obase + 2);
  }
  asm volatile("s_waitcnt lgkmcnt(0)\n\ts_barrier" ::: "memory");
  STAMP(10);
  const unsigned T = G * 8 + w;
  const unsigned obs = WS_O + T * 32 * IMG;
  const unsigned wa2 = lds0 + (16 * (g >> 1) + 4 * (g & 1) + q4) * 128 + ((2 * (g & 1) + (q4 >> 1)) << 5) + (l & 3) * 8;
  f32x4 oc[4][2] = {};
  u32x4 of[16];
  s16x4 wl[2][4], wh[2][4];
#pragma unroll
  for (int i = 0; i < 16; ++i) of[i] = __builtin_amdgcn_raw_buffer_load_b128(rws, l * 16 + (i & 3) * 1024, obs + loc * 32768 + (i >> 2) * 4096, SC1);
  const unsigned allrdy = rfl(*(volatile LAS unsigned*)(size_t)(lds0 + LDS_WOX));
#pragma unroll
  for (int nt = 0; nt < 4; ++nt) { wl[0][nt] = ldstr((wa2 ^ (nt << 5)) + WOB(0)); wh[0][nt] = ldstr((wa2 ^ (nt << 5)) + WOB(0) + 1024); }
  _Pragma("unroll") for (int i_ = 0; i_ < 8; ++i_) PIN(oc[i_ >> 1][i_ & 1]);
  asm volatile("s_nop 1" ::: "memory");
  SBAR();
#pragma unroll 1
  for (int gq = 0; gq < 4; ++gq) {
    const unsigned wb = wa2 + WOB(gq), wbn = wa2 + WOB(gq + 1);
    if (__builtin_expect(gq == 1 && !allrdy, 0)) {
      if (w == POLLW) {
#pragma unroll 1
        for (unsigned sp = 0; sp < SPIN_CAP; ++sp) { if (__all(l >= 32 || ld32_wait(rws, fo) == obase + 2)) break; __builtin_amdgcn_s_sleep(POLLS); }
      }
      asm volatile("s_barrier" ::: "memory");
    }
    const unsigned on = gq < 3 ? obs + ((gq + 1) ^ (loc * 2)) * 16384 : OOB;
    sfor<0, 8>([&](auto hi) AI { constexpr int h_ = decltype(hi)::v, cur = h_ & 1, nxt = cur ^ 1;
      if constexpr (h_ == 6) { if (gq == 0) asm volatile("s_barrier" ::: "memory"); }
      sfor<0, 8>([&](auto mi) AI { constexpr int m_ = decltype(mi)::v, nt = m_ >> 1, tt = m_ & 1;
        { const bf16x8 wf = cat(wl[cur][nt], wh[cur][nt]); MFA(oc[nt][tt], wf, of[2 * h_ + tt]); }
        { const unsigned wq_ = ((h_ < 7 ? wb : wbn) ^ (nt << 5)) + ((h_ + 1) & 7) * 4096;
          if constexpr (tt == 0) wl[nxt][nt] = ldstr(wq_); else wh[nxt][nt] = ldstr(wq_ + 1024); }
        if constexpr (m_ >= 6) of[2 * h_ + tt] = __builtin_amdgcn_raw_buffer_load_b128(rws, l * 16 + ((2 * h_ + tt) & 3) * 1024, on + ((2 * h_ + tt) >> 2) * 4096, SC1);
        SBAR(); }); });
  }
  asm volatile("s_nop 7" ::: "memory");
  _Pragma("unroll") for (int i_ = 0; i_ < 8; ++i_) PIN(oc[i_ >> 1][i_ & 1]);
  STAMP(11);
  const __amdgpu_buffer_rsrc_t rout = mkrs(out, S_ * 2 * D_ * 2);
  const unsigned orow = ((R * 8 + w) * 32 + c) * 4096 + br * 2048 + ns * 128 + (g & 1) * 32 + (g >> 1) * 16;
#pragma unroll
  for (int tt = 0; tt < 2; ++tt) {
#pragma unroll
    for (int np = 0; np < 2; ++np) {
      const f32x4 &a = oc[2 * np][tt], &b = oc[2 * np + 1][tt];
      auto r0 = __builtin_amdgcn_permlane16_swap(pk2(a[0], a[1]), pk2(b[0], b[1]), false, false);
      auto r1 = __builtin_amdgcn_permlane16_swap(pk2(a[2], a[3]), pk2(b[2], b[3]), false, false);
      u32x4 v = {r0[0], r1[0], r0[1], r1[1]};
      __builtin_amdgcn_raw_buffer_store_b128(v, rout, orow + tt * 65536 + np * 64, 0, 0);
    }
  }
}

extern "C" void kernel_launch(void* const* d_in, const int* in_sizes, int n_in, void* d_out, int out_size, void* d_ws, size_t ws_size,
                              hipStream_t stream) {
  const size_t wsoff = (ws_size - WS_END) & ~(size_t)0xfffff;
  k_fused<<<256, 512, 0, stream>>>(d_in[0], d_in[1], d_in[2], d_in[3], d_in[4], d_in[5], (char*)d_ws + wsoff, d_out);
}
```

```cpp
#define POLLS 8
#include <hip/hip_runtime.h>
#include <hip/hip_bf16.h>

typedef short bf16x8 __attribute__((ext_vector_type(8)));
typedef short s16x4 __attribute__((ext_vector_type(4)));
typedef float f32x16 __attribute__((ext_vector_type(16)));
typedef float f32x4 __attribute__((ext_vector_type(4)));
typedef unsigned u32x4 __attribute__((ext_vector_type(4)));
typedef __bf16 bf16x2_t __attribute__((ext_vector_type(2)));
typedef float f32x2 __attribute__((ext_vector_type(2)));
#define LAS __attribute__((address_space(3)))
#define DI __device__ __forceinline__
#define AI __attribute__((always_inline))
#define MFMA(a, b, c) __builtin_amdgcn_mfma_f32_32x32x16_bf16((a), (b), (c), 0, 0, 0)
#define SBAR() __builtin_amdgcn_sched_barrier(0)
#define PIN(x) asm volatile("" : "+v"(x))
#define MFA(acc_, a_, b_) asm volatile("v_mfma_f32_16x16x32_bf16 %0, %1, %2, %0" : "+v"(acc_) : "v"(a_), "v"(b_))

constexpr int S_ = 2048, D_ = 1024;
constexpr float QSCALE = 0.17677669529663687f * 1.4426950408889634f;
constexpr float PBOUND = 1073741824.0f;
constexpr unsigned IMG = 2048;
constexpr unsigned WS_FLAG = 0, WS_ACK = 32768, WS_KV = 65536, WS_O = WS_KV + 256u * 65536u, WS_END = WS_O + 2u * 64u * 32u * IMG;
constexpr unsigned ACKX = 0x5bd1e995u;
constexpr int SC1 = 16;
constexpr unsigned CODE_BYTES = 25600;
constexpr unsigned SPIN_CAP = 1u << 18;

#define HPMAP ((xcd & 1) * 8 + (idx >> 2))
#define RMAP (((xcd >> 1) & 1) * 4 + (idx & 3))
#define NOMEMX 1
template <int I> struct IC { static constexpr int v = I; };
template <int B, int E, class F> DI void sfor(F&& f) { if constexpr (B < E) { f(IC<B>{}); sfor<B + 1, E>(f); } }
DI unsigned rfl(unsigned x) { return (unsigned)__builtin_amdgcn_readfirstlane((int)x); }
DI unsigned pk2(float lo, float hi) { f32x2 v = {lo, hi}; return __builtin_bit_cast(unsigned, __builtin_convertvector(v, bf16x2_t)); }
DI u32x4 pack8(const f32x16& x, int s) {
  u32x4 p;
  p[0] = pk2(x[8 * s + 0], x[8 * s + 1]); p[1] = pk2(x[8 * s + 2], x[8 * s + 3]);
  p[2] = pk2(x[8 * s + 4], x[8 * s + 5]); p[3] = pk2(x[8 * s + 6], x[8 * s + 7]);
  return p;
}
DI u32x4 pack44(const f32x4& a, const f32x4& b) { u32x4 p; p[0] = pk2(a[0], a[1]); p[1] = pk2(a[2], a[3]); p[2] = pk2(b[0], b[1]); p[3] = pk2(b[2], b[3]); return p; }
DI bf16x8 asb(u32x4 v) { return __builtin_bit_cast(bf16x8, v); }
DI bf16x8 lds128(unsigned a) { return *(const LAS bf16x8*)(size_t)a; }
DI void sts128(unsigned a, u32x4 v) { *(LAS u32x4*)(size_t)a = v; }
DI s16x4 ldstr(unsigned a) { return __builtin_amdgcn_ds_read_tr16_b64_v4i16((LAS s16x4*)(size_t)a); }
DI bf16x8 cat(s16x4 lo, s16x4 hi) { return __builtin_shufflevector(lo, hi, 0, 1, 2, 3, 4, 5, 6, 7); }
DI void dma16(unsigned ldsdst, unsigned voff, __amdgpu_buffer_rsrc_t rs, unsigned soff) {
  asm volatile("s_mov_b32 m0, %0\n\ts_nop 0\n\tbuffer_load_dwordx4 %1, %2, %3 offen lds" ::"s"(ldsdst), "v"(voff), "s"(rs), "s"(soff) : "memory");
}
#define XPOL ""
#define WPOL ""
DI void dma16x(unsigned ldsdst, unsigned voff, __amdgpu_buffer_rsrc_t rs, unsigned soff) {
  asm volatile("s_mov_b32 m0, %0\n\ts_nop 0\n\tbuffer_load_dwordx4 %1, %2, %3 offen " XPOL " lds" ::"s"(ldsdst), "v"(voff), "s"(rs), "s"(soff) : "memory");
}
DI void dma16w(unsigned ldsdst, unsigned voff, __amdgpu_buffer_rsrc_t rs, unsigned soff) {
  asm volatile("s_mov_b32 m0, %0\n\ts_nop 0\n\tbuffer_load_dwordx4 %1, %2, %3 offen " WPOL " lds" ::"s"(ldsdst), "v"(voff), "s"(rs), "s"(soff) : "memory");
}
#define PPOL ""
#define OUTPOL 16
#define KVS 1
#define FILL0 4
#define FILL1 8
#define FILLS 4
#define G2R(r) ((r) * 3 / 4)
#define POLLW 0
#define PFMODE 0
#define PFI true
#define PFD 4
#define PFH 1
DI void pf4(unsigned ldsdst, const char* p) {
  asm volatile("s_mov_b32 m0, %0\n\ts_nop 0\n\tglobal_load_lds_dword %1, off " PPOL ::"s"(ldsdst), "v"(p) : "memory");
}
DI void dma16_sc1(unsigned ldsdst, unsigned voff, __amdgpu_buffer_rsrc_t rs, unsigned soff) {
  asm volatile("s_mov_b32 m0, %0\n\ts_nop 0\n\tbuffer_load_dwordx4 %1, %2, %3 offen sc1 lds" ::"s"(ldsdst), "v"(voff), "s"(rs), "s"(soff) : "memory");
}
#define WAITV(n) asm volatile("s_waitcnt vmcnt(" #n ")" ::: "memory")
#define WAIT_BAR(n) asm volatile("s_waitcnt vmcnt(" #n ") lgkmcnt(0)\n\ts_barrier" ::: "memory")
DI __amdgpu_buffer_rsrc_t mkrs(const void* p, unsigned bytes) { return __builtin_amdgcn_make_buffer_rsrc((void*)p, (short)0, (int)bytes, 0x00020000); }
DI unsigned ld32_issue(__amdgpu_buffer_rsrc_t rs, unsigned off) { unsigned r; asm volatile("buffer_load_dword %0, %1, %2, 0 offen sc1" : "=v"(r) : "v"(off), "s"(rs) : "memory"); return r; }
DI unsigned ld32_wait(__amdgpu_buffer_rsrc_t rs, unsigned off) { unsigned r; asm volatile("buffer_load_dword %0, %1, %2, 0 offen sc1\n\ts_waitcnt vmcnt(0)" : "=&v"(r) : "v"(off), "s"(rs) : "memory"); return r; }
DI u32x4 ld128_issue(__amdgpu_buffer_rsrc_t rs, unsigned off) { u32x4 r; asm volatile("buffer_load_dwordx4 %0, %1, %2, 0 offen sc1" : "=v"(r) : "v"(off), "s"(rs) : "memory"); return r; }
DI u32x4 ld128_wait(__amdgpu_buffer_rsrc_t rs, unsigned off) { u32x4 r; asm volatile("buffer_load_dwordx4 %0, %1, %2, 0 offen sc1\n\ts_waitcnt vmcnt(0)" : "=&v"(r) : "v"(off), "s"(rs) : "memory"); return r; }
DI void dma4(unsigned ldsdst, unsigned voff, __amdgpu_buffer_rsrc_t rs, unsigned soff) {
  asm volatile("s_mov_b32 m0, %0\n\ts_nop 0\n\tbuffer_load_dword %1, %2, %3 offen lds" ::"s"(ldsdst), "v"(voff), "s"(rs), "s"(soff) : "memory");
}
DI void dma4_sc1(unsigned ldsdst, unsigned voff, __amdgpu_buffer_rsrc_t rs, unsigned soff) {
  asm volatile("s_mov_b32 m0, %0\n\ts_nop 0\n\tbuffer_load_dword %1, %2, %3 offen sc1 lds" ::"s"(ldsdst), "v"(voff), "s"(rs), "s"(soff) : "memory");
}
#define WAIT_BAR_V(n, x) asm volatile("s_waitcnt vmcnt(" #n ") lgkmcnt(0)\n\ts_barrier" : "+v"(x) :: "memory")
constexpr unsigned OOB = 0x80000000u;
DI void st32(unsigned v, __amdgpu_buffer_rsrc_t rs, unsigned off) { __builtin_amdgcn_raw_buffer_store_b32(v, rs, off, 0, SC1); }

#define STAMP(i) do {} while (0)

constexpr unsigned XST = 32768, WST = 24576, LDS_W = 3 * XST, LDS_SPARE = LDS_W + 2 * WST, LDS_CTL = 159744;
static_assert(PFD == 4, "prefetch distance");
constexpr unsigned LDS_OWN = 32768, LDS_CNT = 162816, LDS_WOX = 98304, LDS_BYTES = 163840, TIL = 2 * IMG;

struct Frag { bf16x8 x[4]; s16x4 wl[6], wh[6]; };

DI void lds_inc(unsigned a) { asm volatile("s_mov_b64 exec, 1\n\tds_add_u32 %0, %1\n\ts_mov_b64 exec, -1" ::"v"(a), "v"(1u) : "memory"); }
DI unsigned lds_inc_rtn(unsigned a) { unsigned r; asm volatile("s_mov_b64 exec, 1\n\tds_add_rtn_u32 %0, %1, %2\n\ts_mov_b64 exec, -1\n\ts_waitcnt lgkmcnt(0)" : "=&v"(r) : "v"(a), "v"(1u) : "memory"); return rfl(r); }
DI void lds_wait(unsigned a, unsigned n) {
#pragma unroll 1
  for (unsigned sp = 0; sp < SPIN_CAP && rfl(*(volatile LAS unsigned*)(size_t)a) < n; ++sp) __builtin_amdgcn_s_sleep(1);
  asm volatile("" ::: "memory");
}
DI void dma64_sc1(unsigned ldsdst, unsigned voff, __amdgpu_buffer_rsrc_t rs, unsigned soff) {
  asm volatile("s_mov_b32 m0, %0\n\ts_nop 0\n\tbuffer_load_dwordx4 %1, %2, %3 offen sc1 lds\n\tbuffer_load_dwordx4 %1, %2, %3 offen offset:1024 sc1 lds\n\t"
               "buffer_load_dwordx4 %1, %2, %3 offen offset:2048 sc1 lds\n\tbuffer_load_dwordx4 %1, %2, %3 offen offset:3072 sc1 lds" ::"s"(ldsdst), "v"(voff), "s"(rs), "s"(soff) : "memory");
}
DI float swapmax(float m) {
  auto rr = __builtin_amdgcn_permlane32_swap(__float_as_uint(m), __float_as_uint(m), false, false);
  return __builtin_fmaxf(__uint_as_float(rr[0]), __uint_as_float(rr[1]));
}
DI float swapsum(float m) {
  auto rr = __builtin_amdgcn_permlane32_swap(__float_as_uint(m), __float_as_uint(m), false, false);
  return __uint_as_float(rr[0]) + __uint_as_float(rr[1]);
}
#define MX3(a, b, c) __builtin_fmaxf(__builtin_fmaxf((a), (b)), (c))
DI float max16(const f32x16& p) {
  float a = MX3(p[0], p[1], p[2]), b = MX3(p[3], p[4], p[5]);
  a = MX3(a, p[6], p[7]); b = MX3(b, p[8], p[9]); a = MX3(a, p[10], p[11]); b = MX3(b, p[12], p[13]);
  a = MX3(a, p[14], p[15]);
  return __builtin_fmaxf(a, b);
}
DI int keyidx(int i) { return (i & 3) + 16 * ((i >> 2) & 1) + 8 * (i >> 3); }
DI void redo_tile(unsigned tile, bf16x8 q0, bf16x8 q1, bf16x8 sel, f32x16& o, f32x16& negm, float& l, u32x4& p0, u32x4& p1, f32x4& L) {
  int ln = threadIdx.x & 63; PIN(ln);
  const unsigned ka = (tile & ~3u) + ln * 16, mode = tile & 3u;
  const bf16x8 k0 = lds128(ka), k1 = lds128(ka + 1024);
  const int rr = (ln & 31) - 4 * (ln >> 5);
  f32x16 s;
#pragma unroll
  for (int i = 0; i < 16; ++i) s[i] = (mode != 0 && (keyidx(i) <= rr) == (mode == 2)) ? -__builtin_inff() : negm[0];
  s = MFMA(k0, q0, s); s = MFMA(k1, q1, s);
  const float d = __builtin_fmaxf(swapmax(max16(s)), 0.f), f = __builtin_amdgcn_exp2f(-d), nm = negm[0] - d;
  l *= f;
#pragma unroll
  for (int i = 0; i < 16; ++i) { negm[i] = nm; o[i] *= f; s[i] = __builtin_amdgcn_exp2f(s[i] - d); }
  p0 = pack8(s, 0); p1 = pack8(s, 1);
  f32x4 z = {};
  L = __builtin_amdgcn_mfma_f32_16x16x32_bf16(sel, asb(p0), z, 0, 0, 0);
  L = __builtin_amdgcn_mfma_f32_16x16x32_bf16(sel, asb(p1), L, 0, 0, 0);
  float t = L[0]; PIN(t); L[0] = t;
  PIN(p0); PIN(p1); PIN(o); PIN(negm);
  asm volatile("s_nop 1" ::: "memory");
}

typedef unsigned long long u64x8 __attribute__((ext_vector_type(8)));
__global__ __launch_bounds__(512, 2) void k_fused(const void* xA_, const void* wA_, const void* woA_, const void* xB_, const void* wB_, const void* woB_,
                                                  void* ws_, void* out_) {
  u64x8 ka; asm volatile("s_load_dwordx16 %0, %1, 0x0" : "=&s"(ka) : "s"(__builtin_amdgcn_kernarg_segment_ptr()) : "memory");
  __shared__ __attribute__((aligned(1024))) char smem[LDS_BYTES];
  const unsigned lds0 = (unsigned)(size_t)(LAS char*)smem;
  const unsigned bid = blockIdx.x, xcd = bid & 7, idx = bid >> 3;
  const unsigned br = xcd >> 2, R = RMAP, hp = HPMAP;
  const unsigned L = (br * 8 + R) * 16 + hp;
  const unsigned G = br * 8 + R, ns = hp, loc = hp >> 3;
  const unsigned tid = threadIdx.x, l = tid & 63, w = rfl(tid >> 6), wm = w & 3, wn = w >> 2;
  const unsigned mtw = w, mp = wn ? 3 - wm : wm;
  unsigned xv0 = (R * 256 + mtw * 32 + (l >> 3)) * 2048 + (((l & 7) ^ (l >> 4)) << 4);
  unsigned xv1 = (R * 256 + mtw * 32 + 8 + (l >> 3)) * 2048 + (((l & 7) ^ (4 + (l >> 4))) << 4);
  unsigned wv = (8 * w + (l >> 3)) * 6144 + (((l & 7) ^ ((((l >> 4) & 1) | ((w & 1) << 1)) << 1)) << 4) + hp * 128;
  asm volatile("s_waitcnt lgkmcnt(0)" : "+s"(ka), "+v"(xv0), "+v"(xv1), "+v"(wv) :: "memory");
  const void *xA = (const void*)ka[0], *wA = (const void*)ka[1], *woA = (const void*)ka[2], *xB = (const void*)ka[3], *wB = (const void*)ka[4], *woB = (const void*)ka[5];
  void *ws = (void*)ka[6], *out = (void*)ka[7];
  const __amdgpu_buffer_rsrc_t rx = mkrs(br ? xB : xA, NOMEMX * S_ * D_ * 2), rw = mkrs(br ? wB : wA, D_ * 3 * D_ * 2), rws = mkrs(ws, WS_END);
  STAMP(0);
  const unsigned xdst = lds0 + w * 4096, wdst = lds0 + LDS_W + w * 1024;
#define DMA_X(t, st) do { const unsigned so_ = (t) * 128u, d_ = xdst + (st); \
    dma16x(d_, xv0, rx, so_); dma16x(d_ + 1024, xv1, rx, so_); dma16x(d_ + 2048, xv0, rx, so_ + 32768); dma16x(d_ + 3072, xv1, rx, so_ + 32768); } while (0)
#define DMA_W(t, st) do { const unsigned so_ = (t) * (64u * 6144u), d_ = wdst + (st); \
    dma16w(d_, wv, rw, so_); dma16w(d_ + 8192, wv, rw, so_ + 2048); dma16w(d_ + 16384, wv, rw, so_ + 4096); } while (0)
  DMA_W(0, 0); DMA_X(0, 0);
  const unsigned fl_id = l < 32 ? G * 16 + (l >> 1) : (l == 32 ? L - 16 : L);
  const bool fl_ok = l < 34 && !(l == 32 && R == 0);
  unsigned obase = ld32_issue(rws, fl_ok ? WS_FLAG + fl_id * 128 : OOB);
  DMA_W(1, WST); DMA_X(1, XST);
  const unsigned pli = l < 13 ? l : 0;
  const unsigned pwl = ((idx & 3) * 8 + w) * 6 + (pli - 4);
  const unsigned pq = (idx * 8 + w) * 3 + (pli - 10);
  const char* pfp = pli < 4 ? (const char*)(br ? xB : xA) + (R * 256 + ((idx >> 2) * 8 + w) * 4 + pli) * 2048 + (PFD - 1) * 128
                  : pli < 10 ? (const char*)(br ? wB : wA) + (pwl / 3 + (PFD - 1) * 64) * 6144 + (pwl % 3) * 2048 + hp * 128
                             : (const char*)(br ? woB : woA) + (pq >> 3) * 2048 + loc * 1024 + (pq & 7) * 128;
  unsigned pstep = pli < 4 ? 128u : pli < 10 ? 64u * 6144u : 77u * 2048u;
  pf4(lds0 + LDS_CNT + 256, pfp); pfp += pstep;
#define CODEPF(bytes) do { unsigned long long pc; asm volatile("s_getpc_b64 %0" : "=s"(pc));     \
    const unsigned co = (w * 64 + l) * 128; \
    pf4(lds0 + LDS_CNT + 256, (const char*)(pc & ~127ull) + (co < (bytes) ? co : 0)); } while (0)
  if (tid < 8) *(LAS unsigned*)(size_t)(lds0 + LDS_CNT + tid * 4) = 0;
  const unsigned c = l & 15, g = l >> 4, q4 = c >> 2;
  const unsigned xa = lds0 + mp * 8192 + c * 128 + ((g ^ (c >> 1)) << 4);
  const unsigned wa = lds0 + LDS_W + (8 * g + q4) * 128 + (((2 * wn) ^ ((q4 >> 1) | ((g & 1) << 1))) << 5) + (l & 3) * 8;
  f32x4 acc[6][4] = {};
  u32x4 mk0, mk1; unsigned sel1 = ((c & 3) == 0 && ((c >> 2) & 1) == (g & 1)) ? 0x3F803F80u : 0u;
  { const int rr = (int)(l & 31) - 4 * (int)(l >> 5);
#pragma unroll
    for (int i = 0; i < 4; ++i) {
      mk0[i] = (keyidx(2 * i) <= rr ? 0xFFFFu : 0u) | (keyidx(2 * i + 1) <= rr ? 0xFFFF0000u : 0u);
      mk1[i] = (keyidx(8 + 2 * i) <= rr ? 0xFFFFu : 0u) | (keyidx(9 + 2 * i) <= rr ? 0xFFFF0000u : 0u); }
    PIN(mk0); PIN(mk1); PIN(sel1); }
  f32x16 oa, ob; f32x4 Ls; u32x4 p0, p1; bf16x8 v0, v1, sel;
  Frag A, B;
#define RDX(F, xb, s, u) F.x[u] = lds128(((xb) ^ ((s) << 6)) + (u) * 2048)
#define MM(C, n, u) do { const bf16x8 wf_ = cat(C.wl[n], C.wh[n]); if ((n) < 4) MFA(acc[n][u], wf_, C.x[u]); else MFA(acc[n][u], C.x[u], wf_); } while (0)
#define RDWL(F, wb, s, n) F.wl[n] = ldstr(((wb) ^ (((n) & 1) << 5)) + ((n) >> 1) * 8192 + (s) * 4096)
#define RDWH(F, wb, s, n) F.wh[n] = ldstr(((wb) ^ (((n) & 1) << 5)) + ((n) >> 1) * 8192 + (s) * 4096 + 512)
#define RD1(F, xb, wb, s, r) do { if ((r) == 0) RDX(F, xb, s, 0); else if ((r) == 1) RDWL(F, wb, s, 0); else if ((r) == 2) RDWH(F, wb, s, 0); \
    else if ((r) < 6) RDX(F, xb, s, ((r) - 2) & 3); else if (((r) & 1) == 0) RDWL(F, wb, s, (((r) + 8) >> 1) % 6); else RDWH(F, wb, s, (((r) + 8) >> 1) % 6); } while (0)
#define PB 27
#define PFP 36
#define BP(r) ((r) * 3 / 2)
#define AP(r) (29 + (r) * 6 / 5)
#define XP(j) (2 + (j) * 6)
#define WP(j) (28 + (j) * 3)
#define TILE(LDA, DMA, WAITB) do { \
    const unsigned xb = xa + xs, wb = wa + wsx, wd = wdst + wsx; \
    xp = xs; xs = (xs == 2 * XST) ? 0 : xs + XST; wsx ^= WST; \
    const unsigned xn = xa + xs, wn_ = wa + wsx; \
    sfor<0, 48>([&](auto mi) AI { constexpr int m_ = decltype(mi)::v; \
      if constexpr (m_ < 24) MM(A, m_ >> 2, m_ & 3); else MM(B, (m_ - 24) >> 2, m_ & 3); \
      sfor<0, 16>([&](auto ri) AI { constexpr int r_ = decltype(ri)::v; \
        if constexpr (BP(r_) == m_) RD1(B, xb, wb, 1, r_); if constexpr (LDA && AP(r_) == m_) RD1(A, xn, wn_, 0, r_); }); \
      sfor<0, 4>([&](auto ji) AI { constexpr int j_ = decltype(ji)::v; \
        if constexpr (DMA && XP(j_) == m_) dma16x(xd + j_ * 1024, (j_ & 1) ? xv1 : xv0, rx, xso + (j_ >> 1) * 32768); \
        if constexpr (DMA && j_ < 3 && WP(j_) == m_) dma16w(wd + j_ * 8192, wv, rw, wso + j_ * 2048); }); \
      if constexpr (PFI && DMA && m_ == PFP) { pf4(lds0 + LDS_CNT + 256, pfp); pfp += pinc; } \
      if constexpr (!LDA) {     \
        if constexpr (m_ == 28) { oa = f32x16{}; PIN(oa); } if constexpr (m_ == 34) { ob = f32x16{}; PIN(ob); } \
        if constexpr (m_ == 40) { p0 = u32x4{}; p1 = u32x4{}; v0 = bf16x8{}; v1 = bf16x8{}; PIN(p0); PIN(p1); PIN(v0); PIN(v1); } \
        if constexpr (m_ == 44) { Ls = f32x4{}; sel = asb(u32x4{sel1, sel1, sel1, sel1}); PIN(Ls); PIN(sel); } } \
      SBAR(); \
      if constexpr (m_ == PB) { WAITB; SBAR(); } }); } while (0)
  WAIT_BAR_V(8, obase);
  STAMP(1);
  unsigned xs = 0, wsx = 0, xp = 2 * XST;
  sfor<0, 16>([&](auto ri) AI { constexpr int r_ = decltype(ri)::v; RD1(A, xa, wa, 0, r_); SBAR(); });
  if (w == 0) st32(obase ^ ACKX, rws, (fl_ok && l <= 32 && !(l & 1)) ? WS_ACK + fl_id * 128 + (l < 32 ? ns : 16) * 4 : OOB);
  const unsigned pbase = __builtin_amdgcn_readlane(obase, 32), mybase = __builtin_amdgcn_readlane(obase, 33);
  _Pragma("unroll") for (int i_ = 0; i_ < 24; ++i_) PIN(acc[i_ >> 2][i_ & 3]);
  asm volatile("s_nop 1" ::: "memory");
  SBAR();
  const bool ack_ok = l < 17 && !(l == 16 && R == 7);
  const unsigned ackoff = ack_ok ? WS_ACK + L * 128 + l * 4 : OOB;
  const char* codep;
  { unsigned long long pc; asm volatile("s_getpc_b64 %0" : "=s"(pc)); const unsigned co = (w * 64 + l) * 128; codep = (const char*)(pc & ~127ull) + (co < CODE_BYTES ? co : 0); }
#pragma unroll 1
  for (int t = 0; t < 15; ++t) {
    const unsigned xso = t < 14 ? (t + 2) * 128u : 0x80000000u, wso = t < 14 ? (t + 2) * (64u * 6144u) : OOB;
    const unsigned xd = t < 14 ? xdst + xp : lds0 + LDS_SPARE;
    if (__builtin_expect((unsigned)(t - 11) < 2u, 0)) {
      if (t == 11) {
        dma4_sc1(lds0 + LDS_CTL + w * 256, ackoff, rws, 0);
        pf4(lds0 + LDS_CNT + 256, codep);
      } else {
        pfp = (const char*)(br ? woB : woA) + ((idx * 8 + w) * 4 + ((l >> 3) & 3)) * 2048 + loc * 1024 + (l & 7) * 128; pstep = 0;
      }
    }
    const unsigned pinc = pstep;
    TILE(true, true, WAIT_BAR(5));
  }
  bool acked = __all(!ack_ok || *(volatile LAS unsigned*)(size_t)(lds0 + LDS_CTL + w * 256 + l * 4) == (mybase ^ ACKX));
  __builtin_amdgcn_s_setprio(2);
  { const unsigned xso = 0, wso = 0, xd = 0, pinc = 0; TILE(false, false, (void)0); }
  asm volatile("s_nop 7" ::: "memory");
  _Pragma("unroll") for (int i_ = 0; i_ < 24; ++i_) PIN(acc[i_ >> 2][i_ & 3]);
  STAMP(2);
  bf16x8 qa0, qa1, qb0, qb1;
  f32x16 sa, sb, na, nb;
  {
    const unsigned ko = (g & 1) * 1024 + ((g >> 1) * 32 + (c & 7) + 16 * (c >> 3)) * 16;
    const unsigned vo = IMG + (g >> 1) * 1024 + ((g & 1) * 32 + c) * 16;
    const unsigned t0 = wn * 32768 + mp * 2 * TIL;
    const unsigned gs = R < 7 ? WS_KV + L * 65536 + t0 : OOB;
#pragma unroll
    for (int u = 0; u < 4; ++u) {
      const unsigned tb = (u >> 1) * TIL;
      const u32x4 kk = pack44(acc[2][u], acc[3][u]), vv = pack44(acc[4 + (u & 1)][u & 2], acc[4 + (u & 1)][(u & 2) + 1]);
      __builtin_amdgcn_raw_buffer_store_b128(kk, rws, ko + (u & 1) * 128 + tb, gs, SC1);
      __builtin_amdgcn_raw_buffer_store_b128(vv, rws, vo + (u & 1) * 256 + tb, gs, SC1);
      sts128(lds0 + LDS_OWN + t0 + tb + ko + (u & 1) * 128, kk);
      sts128(lds0 + LDS_OWN + t0 + tb + vo + (u & 1) * 256, vv);
    }
    lds_inc(lds0 + LDS_CNT + wn * 4);
    if (mp < 2) __builtin_amdgcn_s_setprio(1);
    u32x4 qf[4]; float sd[4];
#pragma unroll
    for (int u = 0; u < 4; ++u) {
      const f32x4 qx = acc[0][u] * QSCALE, qy = acc[1][u] * QSCALE;
      qf[u] = pack44(qx, qy);
      float d = qx[0] * acc[2][u][0];
#pragma unroll
      for (int i = 1; i < 4; ++i) d = __builtin_fmaf(qx[i], acc[2][u][i], d);
#pragma unroll
      for (int i = 0; i < 4; ++i) d = __builtin_fmaf(qy[i], acc[3][u][i], d);
      sd[u] = d;
    }
#pragma unroll
    for (int p = 0; p < 2; ++p) {
#pragma unroll
      for (int i = 0; i < 4; ++i) { auto r = __builtin_amdgcn_permlane16_swap(qf[2 * p][i], qf[2 * p + 1][i], false, false); qf[2 * p][i] = r[0]; qf[2 * p + 1][i] = r[1]; }
      auto r = __builtin_amdgcn_permlane16_swap(__float_as_uint(sd[2 * p]), __float_as_uint(sd[2 * p + 1]), false, false);
      sd[2 * p] = -swapsum(__uint_as_float(r[0]) + __uint_as_float(r[1]));
    }
    qb0 = asb(qf[0]); qb1 = asb(qf[1]); qa0 = asb(qf[2]); qa1 = asb(qf[3]);
    { const float one = l < 32 ? 1.f : 0.f;
      asm volatile("s_nop 1\n\tv_mfma_f32_32x32x2_f32 %0, %2, %3, 0\n\tv_mfma_f32_32x32x2_f32 %1, %2, %4, 0" : "=&v"(na), "=&v"(nb) : "v"(one), "v"(sd[2]), "v"(sd[0])); }
  }
  float la = 0.f, lb = 0.f;
  bf16x8 k0, k1;
  PIN(qa0); PIN(qa1); PIN(qb0); PIN(qb1);
  STAMP(3);
  const __amdgpu_buffer_rsrc_t rwo = mkrs(br ? woB : woA, D_ * D_ * 2);
  const unsigned wov = (l >> 3) * 2048 + (((l & 7) ^ (((l >> 4) & 3) << 1)) << 4) + ns * 128;
#define WOB(gq) ((gq) == 0 ? 131072u : ((gq) & 3) == 3 ? 0u : (gq) * 32768u)
#define DMA_WO(j, ww) dma16(lds0 + WOB((j) >> 2) + ((j) & 3) * 8192 + (ww) * 1024, wov, rwo, ((j) ^ (loc * 8)) * (64u * 2048u) + (ww) * 16384u)
  const unsigned lb16 = l * 16, own = lds0 + LDS_OWN + wn * 32768, prv = R > 0 ? lds0 + wn * 98304 : own;
  const unsigned myflag = WS_FLAG + L * 128, pfoff = WS_FLAG + (L - 16) * 128 + 4 + wn * 4;
#define QK0(S, Q, N) asm volatile("v_mfma_f32_32x32x16_bf16 %0, %1, %2, %3" : "=&v"(S) : "v"(k0), "v"(Q), "v"(N), "v"(k1))
#define QK1(S, Q) asm volatile("v_mfma_f32_32x32x16_bf16 %0, %1, %2, %0" : "+v"(S) : "v"(k1), "v"(Q))
#define PV0(O) asm volatile("v_mfma_f32_32x32x16_bf16 %0, %1, %2, %0" : "+v"(O) : "v"(v0), "v"(p0), "v"(v1))
#define PV1(O) asm volatile("v_mfma_f32_32x32x16_bf16 %0, %1, %2, %0" : "+v"(O) : "v"(v1), "v"(p1))
#define EX(S, i) do { S[i] = __builtin_amdgcn_exp2f(S[i]); } while (0)
#define RDK(a) do { k0 = lds128(a); k1 = lds128((a) + 1024); } while (0)
  unsigned pa = own + (2 * mp + 1) * TIL, pb = pa - TIL, pbp = pb;
  unsigned va = lb16 + pa, vb = lb16 + pb;
  RDK(va);
  asm volatile("s_nop 1" ::: "memory");
  QK0(sa, qa0, na); QK1(sa, qa1);
  RDK(vb);
  asm volatile("s_nop 7\n\ts_nop 1" ::: "memory");
  SBAR();
#define EXP(i) e_[i] = __builtin_amdgcn_exp2f(sx_[i])
#define HALF(SX, SY, NY, LY, OY, QY0, QY1, PY, RD, VX, KNEXT, BAR1, MSK, LSUM) do { \
    const f32x16 sx_ = SX; float e_[16]; u32x4 pn_; \
    EXP(0); EXP(1); EXP(2); EXP(3); EXP(4); asm volatile("" : "+v"(e_[0]), "+v"(e_[1]), "+v"(e_[2]), "+v"(e_[3]), "+v"(e_[4])); SBAR();     \
    EXP(5); asm volatile("" : "+v"(e_[5]), "+v"(Ls)); SBAR(); \
    unsigned long long bad_ = __builtin_amdgcn_ballot_w64(!(Ls[0] <= PBOUND)); asm volatile("" : "+s"(bad_)); SBAR(); \
    pn_[0] = pk2(e_[0], e_[1]); pn_[1] = pk2(e_[2], e_[3]); pn_[2] = pk2(e_[4], e_[5]); EXP(6); \
    asm volatile("" : "+v"(pn_[0]), "+v"(pn_[1]), "+v"(pn_[2]), "+v"(e_[6])); SBAR(); \
    if (__builtin_expect(bad_ != 0, 0)) redo_tile(PY, QY0, QY1, sel, OY, NY, LY, p0, p1, Ls); \
    PV0(OY); SBAR(); \
    EXP(7); EXP(8); LY += Ls[0]; pn_[3] = pk2(e_[6], e_[7]); asm volatile("" : "+v"(pn_[3]), "+v"(e_[8]), "+v"(LY)); SBAR(); \
    PV1(OY); SBAR(); if (RD) { v0 = lds128((VX) + IMG); v1 = lds128((VX) + IMG + 1024); VX = KNEXT; } \
    EXP(9); EXP(10); asm volatile("" : "+v"(e_[9]), "+v"(e_[10]), "+v"(VX)); SBAR(); \
    QK0(SY, QY0, NY); SBAR(); EXP(11); EXP(12); EXP(13); asm volatile("" : "+v"(e_[11]), "+v"(e_[12]), "+v"(e_[13])); SBAR(); \
    QK1(SY, QY1); SBAR(); \
    EXP(14); EXP(15); asm volatile("" : "+v"(e_[14]), "+v"(e_[15])); SBAR(); \
    if (BAR1) { if (PFI) WAITV(9); else WAITV(8); lds_inc(lds0 + LDS_CNT + 8); if (mp) lds_wait(lds0 + LDS_CNT + wn * 4, 4); } \
    if (RD) RDK(VX); SBAR(); \
    p0 = pn_; p1[0] = pk2(e_[8], e_[9]); p1[1] = pk2(e_[10], e_[11]); p1[2] = pk2(e_[12], e_[13]); p1[3] = pk2(e_[14], e_[15]); SBAR(); \
    if (MSK) { p0 &= mk0; p1 &= mk1; PIN(p0); PIN(p1); asm volatile("s_nop 0"); } \
    LSUM; SBAR(); } while (0)
#define LMF0 "v_mfma_f32_16x16x32_bf16 %0, %1, %2, 0\n\t"
#define LMF1 "v_mfma_f32_16x16x32_bf16 %0, %1, %3, %0\n\t"
#define LSUM_G() asm volatile(LMF0 "s_nop 0\n\t" LMF1 "s_nop 1" : "=&v"(Ls) : "v"(sel), "v"(p0), "v"(p1))
#define LSUM_A() asm volatile(LMF0 "s_nop 0\n\t" LMF1 "s_nop 0" : "=&v"(Ls) : "v"(sel), "v"(p0), "v"(p1))
#define LSUM_B() asm volatile("v_mfma_f32_16x16x32_bf16 %0, %3, %4, 0\n\ts_mov_b32 %1, %2\n\tv_mfma_f32_16x16x32_bf16 %0, %3, %5, %0\n\ts_add_i32 %2, %2, 0x1000" : "=&v"(Ls), "=&s"(pbp), "+s"(pa) : "v"(sel), "v"(p0), "v"(p1) : "scc")
  unsigned sent = 0;

#define REPF() do {} while (0)
#define SIGNAL() do { WAITV(0); \
    _Pragma("unroll 1") for (unsigned sp = 0; !acked && sp < SPIN_CAP; ++sp) { __builtin_amdgcn_s_sleep(2); acked = __all(!ack_ok || ld32_wait(rws, ackoff) == (mybase ^ ACKX)); } \
    if (lds_inc_rtn(lds0 + LDS_CNT + 24 + wn * 4) == 3 && l == 0) st32(mybase + 1, rws, myflag + 4 + wn * 4); \
    REPF(); \
    sent = 1; if (mp >= 2) __builtin_amdgcn_s_setprio(0); STAMP(13); } while (0)
#define TRYSIG() do { if (__builtin_expect(!sent, 0)) { if ((__builtin_amdgcn_s_getreg(7 | (31 << 11)) & 0x00C0000Fu) == 0) SIGNAL(); } } while (0)
#define PAIR(PAN, PBN, MD, FIRST) do { const unsigned pan = (PAN), pbn = (PBN); \
      HALF(sa, sb, nb, lb, ob, qb0, qb1, pbp, true, va, lb16 + pan, FIRST, (MD) != 0, LSUM_G()); \
      HALF(sb, sa, na, la, oa, qa0, qa1, pa | (MD), true, vb, lb16 + pbn, false, (MD) != 0, LSUM_G()); \
      pbp = pb | (MD); pa = pan; pb = pbn; } while (0)
#define HOT(N, SIG) do { _Pragma("unroll 1") for (int n = (N); n > 0; --n) {     \
      if (SIG) TRYSIG(); \
      HALF(sa, sb, nb, lb, ob, qb0, qb1, pbp, true, va, va + TIL, false, false, LSUM_A()); \
      HALF(sb, sa, na, la, oa, qa0, qa1, pa, false, vb, 0, false, false, LSUM_B()); } } while (0)
  PAIR(own, own, 1u, true);
  HOT(2 * mp, true);
  {
    STAMP(4);
    if (!sent) SIGNAL();
    STAMP(5);
    mk0 = ~mk0; mk1 = ~mk1; PIN(mk0); PIN(mk1);
    if (mp < 2) {
      lds_wait(lds0 + LDS_CNT + 8, 8);
      unsigned h_ = wn; asm volatile("" : "+s"(h_));
      if (mp) {
#pragma unroll
        for (int j = 0; j < 15; ++j) DMA_WO(j >> 2, h_ * 4 + (j & 3));
        if (!h_) DMA_WO(3, 3);
      } else if (R > 0) {
#pragma unroll 1
        for (unsigned sp = 0; sp < SPIN_CAP; ++sp) {
          if (rfl(ld32_wait(rws, pfoff)) == pbase + 1) break;
          __builtin_amdgcn_s_sleep(KVS);
        }
        STAMP(14);
#pragma unroll
        for (int j = 0; j < 8; ++j) dma64_sc1(lds0 + h_ * 98304 + j * TIL, lb16, rws, WS_KV + (L - 16) * 65536 + h_ * 32768 + j * TIL);
        WAITV(12);
        lds_inc(lds0 + LDS_CNT + 12 + h_ * 4);
      }
    }
    STAMP(6);
    if (R > 0) lds_wait(lds0 + LDS_CNT + 12 + wn * 4, mp < 2 ? 1 : 2);
    STAMP(7);
    pb = prv + (2 * mp + 1) * TIL; vb = lb16 + pb;
    RDK(vb);
    SBAR();
  }
  PAIR(prv + (2 * mp + 1) * TIL, prv + 2 * mp * TIL, 0u, false);
  lds_inc(lds0 + LDS_CNT + 20);
  if (R > 0) {
    if (mp == 0) { WAITV(0); lds_inc(lds0 + LDS_CNT + 12 + wn * 4); }
    PAIR(prv + (2 * mp + 2) * TIL, prv + (2 * mp + 2) * TIL, 2u, false);
    if (mp == 1) lds_wait(lds0 + LDS_CNT + 12 + wn * 4, 2);
    HOT(6 - 2 * mp, false);
    asm volatile("s_nop 7" : "+v"(Ls) :: "memory");
    if (__builtin_expect(__any(!(Ls[0] <= PBOUND)), 0)) redo_tile(pbp, qb0, qb1, sel, ob, nb, lb, p0, p1, Ls);
    lb += Ls[0];
    PV0(ob); PV1(ob);
  } else {
    asm volatile("s_nop 7" : "+v"(Ls), "+v"(sa) :: "memory");
  }
  __builtin_amdgcn_s_setprio(0);
  STAMP(8);
  { const float ia = __builtin_amdgcn_rcpf(la), ib = __builtin_amdgcn_rcpf(lb);
    oa *= ia;
    const unsigned hh = hp * 2 + wn, T0 = br * 64 + R * 8 + 2 * mp;
    const unsigned ol = ((l >> 4) & 1) * 1024 + ((l >> 5) * 16 + (l & 15)) * 16;
    __builtin_amdgcn_raw_buffer_store_b128(pack8(oa, 0), rws, ol, WS_O + (T0 * 32 + 32 + hh) * IMG, SC1);
    __builtin_amdgcn_raw_buffer_store_b128(pack8(oa, 1), rws, ol + 512, WS_O + (T0 * 32 + 32 + hh) * IMG, SC1);
    SBAR();
    asm volatile("s_nop 7" : "+v"(ob) :: "memory");
    ob *= ib;
    __builtin_amdgcn_raw_buffer_store_b128(pack8(ob, 0), rws, ol, WS_O + (T0 * 32 + hh) * IMG, SC1);
    __builtin_amdgcn_raw_buffer_store_b128(pack8(ob, 1), rws, ol + 512, WS_O + (T0 * 32 + hh) * IMG, SC1); }
  WAITV(0);
  STAMP(9);
  asm volatile("s_barrier" ::: "memory");
  if (tid == 64) st32(mybase + 2, rws, myflag);
  dma4(lds0 + LDS_WOX + 1024, l < 32 ? (l >> 4) * 65536 + (l & 15) * 128 : OOB, rws, WS_O + ((br * 64 + R * 8 + 2 * mp) * 32 + hp * 2 + wn) * IMG);
  if (w == 7) DMA_WO(3, 7);
  const unsigned fo = l < 32 ? WS_FLAG + (G * 16 + (l >> 1)) * 128 : OOB;
#pragma unroll
  for (int j = 4; j < 16; ++j) DMA_WO(j, w);
  if (w == POLLW) {
    unsigned f;
#pragma unroll 1
    for (unsigned sp = 0; sp < SPIN_CAP; ++sp) {
      f = ld32_wait(rws, fo);
      if (__all((l >> 4) != loc || f == obase + 2)) break;
      __builtin_amdgcn_s_sleep(POLLS);
    }
    *(volatile LAS unsigned*)(size_t)(lds0 + LDS_WOX) = __all(l >= 32 || f == obase + 2);
  }
  WAIT_BAR(0);
  STAMP(10);
  const unsigned T = G * 8 + w;
  const unsigned obs = WS_O + T * 32 * IMG;
  const unsigned wa2 = lds0 + (16 * (g >> 1) + 4 * (g & 1) + q4) * 128 + ((2 * (g & 1) + (q4 >> 1)) << 5) + (l & 3) * 8;
  f32x4 oc[4][2] = {};
  u32x4 of[16];
  s16x4 wl[2][4], wh[2][4];
#pragma unroll
  for (int i = 0; i < 16; ++i) {
    of[i] = __builtin_amdgcn_raw_buffer_load_b128(rws, l * 16 + (i & 3) * 1024, obs + loc * 32768 + (i >> 2) * 4096, SC1);
    if (i == FILL0 - 1 || i == FILL1 - 1) { SBAR(); __builtin_amdgcn_s_sleep(FILLS); SBAR(); }
  }
  const unsigned allrdy = rfl(*(volatile LAS unsigned*)(size_t)(lds0 + LDS_WOX));
#pragma unroll
  for (int nt = 0; nt < 4; ++nt) { wl[0][nt] = ldstr((wa2 ^ (nt << 5)) + WOB(0)); wh[0][nt] = ldstr((wa2 ^ (nt << 5)) + WOB(0) + 1024); }
  _Pragma("unroll") for (int i_ = 0; i_ < 8; ++i_) PIN(oc[i_ >> 1][i_ & 1]);
  asm volatile("s_nop 1" ::: "memory");
  SBAR();
#pragma unroll 1
  for (int gq = 0; gq < 4; ++gq) {
    const unsigned wb = wa2 + WOB(gq), wbn = wa2 + WOB(gq + 1);
    if (__builtin_expect(gq == 1 && !allrdy, 0)) {
      if (w == POLLW) {
#pragma unroll 1
        for (unsigned sp = 0; sp < SPIN_CAP; ++sp) { if (__all(l >= 32 || ld32_wait(rws, fo) == obase + 2)) break; __builtin_amdgcn_s_sleep(POLLS); }
      }
      asm volatile("s_barrier" ::: "memory");
    }
    const unsigned on = gq < 3 ? obs + ((gq + 1) ^ (loc * 2)) * 16384 : OOB;
    if (gq == 0) __builtin_amdgcn_s_setprio(3); else if (gq == 1) __builtin_amdgcn_s_setprio(2); else if (gq == 2) __builtin_amdgcn_s_setprio(1); else __builtin_amdgcn_s_setprio(0);
    sfor<0, 8>([&](auto hi) AI { constexpr int h_ = decltype(hi)::v, cur = h_ & 1, nxt = cur ^ 1;
      sfor<0, 8>([&](auto mi) AI { constexpr int m_ = decltype(mi)::v, nt = m_ >> 1, tt = m_ & 1;
        { const bf16x8 wf = cat(wl[cur][nt], wh[cur][nt]);
          if constexpr (m_ == 0) {
            const bf16x8 w3 = cat(wl[cur][3], wh[cur][3]);
            asm volatile("v_mfma_f32_16x16x32_bf16 %0, %1, %2, %0" : "+v"(oc[0][0]) : "v"(wf), "v"(of[2 * h_]), "v"(w3), "v"(of[2 * h_ + 1]));
          } else MFA(oc[nt][tt], wf, of[2 * h_ + tt]); }
        sfor<0, 8>([&](auto ri) AI { constexpr int r_ = decltype(ri)::v, rn = r_ >> 1;
          if constexpr (G2R(r_) == m_) {
            const unsigned wq_ = ((h_ < 7 ? wb : wbn) ^ (rn << 5)) + ((h_ + 1) & 7) * 4096;
            if constexpr ((r_ & 1) == 0) wl[nxt][rn] = ldstr(wq_); else wh[nxt][rn] = ldstr(wq_ + 1024); } });
        if constexpr (m_ >= 6) of[2 * h_ + tt] = __builtin_amdgcn_raw_buffer_load_b128(rws, l * 16 + ((2 * h_ + tt) & 3) * 1024, on + ((2 * h_ + tt) >> 2) * 4096, SC1);
        SBAR(); }); });
  }
  asm volatile("s_nop 7" ::: "memory");
  _Pragma("unroll") for (int i_ = 0; i_ < 8; ++i_) PIN(oc[i_ >> 1][i_ & 1]);
  STAMP(11);
  const __amdgpu_buffer_rsrc_t rout = mkrs(out, S_ * 2 * D_ * 2);
  const unsigned orow = ((R * 8 + w) * 32 + c) * 4096 + br * 2048 + ns * 128 + (g & 1) * 32 + (g >> 1) * 16;
  const unsigned orow2 = orow - (c & 8) * 4096 + (c & 8) * 8;
#pragma unroll
  for (int tt = 0; tt < 2; ++tt) {
    u32x4 v[2], va_, vb_;
#pragma unroll
    for (int np = 0; np < 2; ++np) {
      const f32x4 &a = oc[2 * np][tt], &b = oc[2 * np + 1][tt];
      auto r0 = __builtin_amdgcn_permlane16_swap(pk2(a[0], a[1]), pk2(b[0], b[1]), false, false);
      auto r1 = __builtin_amdgcn_permlane16_swap(pk2(a[2], a[3]), pk2(b[2], b[3]), false, false);
      v[np] = u32x4{r0[0], r1[0], r0[1], r1[1]};
    }
#pragma unroll
    for (int i = 0; i < 4; ++i) {
      const unsigned y = c < 8 ? v[1][i] : v[0][i];
      const unsigned r = (unsigned)__builtin_amdgcn_update_dpp(0, (int)y, 0x128, 0xf, 0xf, false);
      va_[i] = c < 8 ? v[0][i] : r; vb_[i] = c < 8 ? r : v[1][i];
    }
    __builtin_amdgcn_raw_buffer_store_b128(va_, rout, orow2 + tt * 65536, 0, OUTPOL);
    __builtin_amdgcn_raw_buffer_store_b128(vb_, rout, orow2 + tt * 65536 + 32768, 0, OUTPOL);
  }
}

extern "C" void kernel_launch(void* const* d_in, const int* in_sizes, int n_in, void* d_out, int out_size, void* d_ws, size_t ws_size,
                              hipStream_t stream) {
  const size_t wsoff = (ws_size - WS_END) & ~(size_t)0xfffff;
  k_fused<<<256, 512, 0, stream>>>(d_in[0], d_in[1], d_in[2], d_in[3], d_in[4], d_in[5], (char*)d_ws + wsoff, d_out);
}
```

```cpp
#define DIAG 99
#define POLLS 8
#include <hip/hip_runtime.h>
#include <hip/hip_bf16.h>

typedef short bf16x8 __attribute__((ext_vector_type(8)));
typedef short s16x4 __attribute__((ext_vector_type(4)));
typedef float f32x16 __attribute__((ext_vector_type(16)));
typedef float f32x4 __attribute__((ext_vector_type(4)));
typedef unsigned u32x4 __attribute__((ext_vector_type(4)));
typedef __bf16 bf16x2_t __attribute__((ext_vector_type(2)));
typedef float f32x2 __attribute__((ext_vector_type(2)));
#define LAS __attribute__((address_space(3)))
#define DI __device__ __forceinline__
#define AI __attribute__((always_inline))
#define MFMA(a, b, c) __builtin_amdgcn_mfma_f32_32x32x16_bf16((a), (b), (c), 0, 0, 0)
#define SBAR() __builtin_amdgcn_sched_barrier(0)
#define PIN(x) asm volatile("" : "+v"(x))
#define MFA(acc_, a_, b_) asm volatile("v_mfma_f32_16x16x32_bf16 %0, %1, %2, %0" : "+v"(acc_) : "v"(a_), "v"(b_))

constexpr int S_ = 2048, D_ = 1024;
constexpr float QSCALE = 0.17677669529663687f * 1.4426950408889634f;
constexpr float PBOUND = 1073741824.0f;
constexpr unsigned IMG = 2048;
constexpr unsigned WS_FLAG = 0, WS_ACK = 32768, WS_KV = 65536, WS_O = WS_KV + 256u * 65536u, WS_END = WS_O + 2u * 64u * 32u * IMG;
constexpr unsigned ACKX = 0x5bd1e995u;
constexpr int SC1 = 16;
constexpr unsigned CODE_BYTES = 25600;
constexpr unsigned SPIN_CAP = 1u << 18;

#define HPMAP ((xcd & 1) * 8 + (idx >> 2))
#define RMAP (((xcd >> 1) & 1) * 4 + (idx & 3))
#define NOMEMX 1
template <int I> struct IC { static constexpr int v = I; };
template <int B, int E, class F> DI void sfor(F&& f) { if constexpr (B < E) { f(IC<B>{}); sfor<B + 1, E>(f); } }
DI unsigned rfl(unsigned x) { return (unsigned)__builtin_amdgcn_readfirstlane((int)x); }
DI unsigned pk2(float lo, float hi) { f32x2 v = {lo, hi}; return __builtin_bit_cast(unsigned, __builtin_convertvector(v, bf16x2_t)); }
DI u32x4 pack8(const f32x16& x, int s) {
  u32x4 p;
  p[0] = pk2(x[8 * s + 0], x[8 * s + 1]); p[1] = pk2(x[8 * s + 2], x[8 * s + 3]);
  p[2] = pk2(x[8 * s + 4], x[8 * s + 5]); p[3] = pk2(x[8 * s + 6], x[8 * s + 7]);
  return p;
}
DI u32x4 pack44(const f32x4& a, const f32x4& b) { u32x4 p; p[0] = pk2(a[0], a[1]); p[1] = pk2(a[2], a[3]); p[2] = pk2(b[0], b[1]); p[3] = pk2(b[2], b[3]); return p; }
DI bf16x8 asb(u32x4 v) { return __builtin_bit_cast(bf16x8, v); }
DI bf16x8 lds128(unsigned a) { return *(const LAS bf16x8*)(size_t)a; }
DI void sts128(unsigned a, u32x4 v) { *(LAS u32x4*)(size_t)a = v; }
DI s16x4 ldstr(unsigned a) { return __builtin_amdgcn_ds_read_tr16_b64_v4i16((LAS s16x4*)(size_t)a); }
DI bf16x8 cat(s16x4 lo, s16x4 hi) { return __builtin_shufflevector(lo, hi, 0, 1, 2, 3, 4, 5, 6, 7); }
DI void dma16(unsigned ldsdst, unsigned voff, __amdgpu_buffer_rsrc_t rs, unsigned soff) {
  asm volatile("s_mov_b32 m0, %0\n\ts_nop 0\n\tbuffer_load_dwordx4 %1, %2, %3 offen lds" ::"s"(ldsdst), "v"(voff), "s"(rs), "s"(soff) : "memory");
}
#define XPOL ""
#define WPOL ""
DI void dma16x(unsigned ldsdst, unsigned voff, __amdgpu_buffer_rsrc_t rs, unsigned soff) {
  asm volatile("s_mov_b32 m0, %0\n\ts_nop 0\n\tbuffer_load_dwordx4 %1, %2, %3 offen " XPOL " lds" ::"s"(ldsdst), "v"(voff), "s"(rs), "s"(soff) : "memory");
}
DI void dma16w(unsigned ldsdst, unsigned voff, __amdgpu_buffer_rsrc_t rs, unsigned soff) {
  asm volatile("s_mov_b32 m0, %0\n\ts_nop 0\n\tbuffer_load_dwordx4 %1, %2, %3 offen " WPOL " lds" ::"s"(ldsdst), "v"(voff), "s"(rs), "s"(soff) : "memory");
}
#define PPOL ""
#define OUTPOL 16
#define KVS 1
#define FILL0 4
#define FILL1 8
#define FILLS 4
#define G2R(r) ((r) * 3 / 4)
#define POLLW 0
#define PFMODE 0
#define PFI true
#define PFD 4
#define PFH 1
DI void pf4(unsigned ldsdst, const char* p) {
  asm volatile("s_mov_b32 m0, %0\n\ts_nop 0\n\tglobal_load_lds_dword %1, off " PPOL ::"s"(ldsdst), "v"(p) : "memory");
}
DI void dma16_sc1(unsigned ldsdst, unsigned voff, __amdgpu_buffer_rsrc_t rs, unsigned soff) {
  asm volatile("s_mov_b32 m0, %0\n\ts_nop 0\n\tbuffer_load_dwordx4 %1, %2, %3 offen sc1 lds" ::"s"(ldsdst), "v"(voff), "s"(rs), "s"(soff) : "memory");
}
#define WAITV(n) asm volatile("s_waitcnt vmcnt(" #n ")" ::: "memory")
#define WAIT_BAR(n) asm volatile("s_waitcnt vmcnt(" #n ") lgkmcnt(0)\n\ts_barrier" ::: "memory")
DI __amdgpu_buffer_rsrc_t mkrs(const void* p, unsigned bytes) { return __builtin_amdgcn_make_buffer_rsrc((void*)p, (short)0, (int)bytes, 0x00020000); }
DI unsigned ld32_issue(__amdgpu_buffer_rsrc_t rs, unsigned off) { unsigned r; asm volatile("buffer_load_dword %0, %1, %2, 0 offen sc1" : "=v"(r) : "v"(off), "s"(rs) : "memory"); return r; }
DI unsigned ld32_wait(__amdgpu_buffer_rsrc_t rs, unsigned off) { unsigned r; asm volatile("buffer_load_dword %0, %1, %2, 0 offen sc1\n\ts_waitcnt vmcnt(0)" : "=&v"(r) : "v"(off), "s"(rs) : "memory"); return r; }
DI u32x4 ld128_issue(__amdgpu_buffer_rsrc_t rs, unsigned off) { u32x4 r; asm volatile("buffer_load_dwordx4 %0, %1, %2, 0 offen sc1" : "=v"(r) : "v"(off), "s"(rs) : "memory"); return r; }
DI u32x4 ld128_wait(__amdgpu_buffer_rsrc_t rs, unsigned off) { u32x4 r; asm volatile("buffer_load_dwordx4 %0, %1, %2, 0 offen sc1\n\ts_waitcnt vmcnt(0)" : "=&v"(r) : "v"(off), "s"(rs) : "memory"); return r; }
DI void dma4(unsigned ldsdst, unsigned voff, __amdgpu_buffer_rsrc_t rs, unsigned soff) {
  asm volatile("s_mov_b32 m0, %0\n\ts_nop 0\n\tbuffer_load_dword %1, %2, %3 offen lds" ::"s"(ldsdst), "v"(voff), "s"(rs), "s"(soff) : "memory");
}
DI void dma4_sc1(unsigned ldsdst, unsigned voff, __amdgpu_buffer_rsrc_t rs, unsigned soff) {
  asm volatile("s_mov_b32 m0, %0\n\ts_nop 0\n\tbuffer_load_dword %1, %2, %3 offen sc1 lds" ::"s"(ldsdst), "v"(voff), "s"(rs), "s"(soff) : "memory");
}
#define WAIT_BAR_V(n, x) asm volatile("s_waitcnt vmcnt(" #n ") lgkmcnt(0)\n\ts_barrier" : "+v"(x) :: "memory")
constexpr unsigned OOB = 0x80000000u;
DI void st32(unsigned v, __amdgpu_buffer_rsrc_t rs, unsigned off) { __builtin_amdgcn_raw_buffer_store_b32(v, rs, off, 0, SC1); }

#define STAMP(i) do { if ((i) == DIAG) { dg1 = __builtin_amdgcn_s_memrealtime(); __builtin_amdgcn_s_waitcnt(0xC07F); } } while (0)

constexpr unsigned XST = 32768, WST = 24576, LDS_W = 3 * XST, LDS_SPARE = LDS_W + 2 * WST, LDS_CTL = 159744;
static_assert(PFD == 4, "prefetch distance");
constexpr unsigned LDS_OWN = 32768, LDS_CNT = 162816, LDS_WOX = 98304, LDS_BYTES = 163840, TIL = 2 * IMG;

struct Frag { bf16x8 x[4]; s16x4 wl[6], wh[6]; };

DI void lds_inc(unsigned a) { asm volatile("s_mov_b64 exec, 1\n\tds_add_u32 %0, %1\n\ts_mov_b64 exec, -1" ::"v"(a), "v"(1u) : "memory"); }
DI unsigned lds_inc_rtn(unsigned a) { unsigned r; asm volatile("s_mov_b64 exec, 1\n\tds_add_rtn_u32 %0, %1, %2\n\ts_mov_b64 exec, -1\n\ts_waitcnt lgkmcnt(0)" : "=&v"(r) : "v"(a), "v"(1u) : "memory"); return rfl(r); }
DI void lds_wait(unsigned a, unsigned n) {
#pragma unroll 1
  for (unsigned sp = 0; sp < SPIN_CAP && rfl(*(volatile LAS unsigned*)(size_t)a) < n; ++sp) __builtin_amdgcn_s_sleep(1);
  asm volatile("" ::: "memory");
}
DI void dma64_sc1(unsigned ldsdst, unsigned voff, __amdgpu_buffer_rsrc_t rs, unsigned soff) {
  asm volatile("s_mov_b32 m0, %0\n\ts_nop 0\n\tbuffer_load_dwordx4 %1, %2, %3 offen sc1 lds\n\tbuffer_load_dwordx4 %1, %2, %3 offen offset:1024 sc1 lds\n\t"
               "buffer_load_dwordx4 %1, %2, %3 offen offset:2048 sc1 lds\n\tbuffer_load_dwordx4 %1, %2, %3 offen offset:3072 sc1 lds" ::"s"(ldsdst), "v"(voff), "s"(rs), "s"(soff) : "memory");
}
DI float swapmax(float m) {
  auto rr = __builtin_amdgcn_permlane32_swap(__float_as_uint(m), __float_as_uint(m), false, false);
  return __builtin_fmaxf(__uint_as_float(rr[0]), __uint_as_float(rr[1]));
}
DI float swapsum(float m) {
  auto rr = __builtin_amdgcn_permlane32_swap(__float_as_uint(m), __float_as_uint(m), false, false);
  return __uint_as_float(rr[0]) + __uint_as_float(rr[1]);
}
#define MX3(a, b, c) __builtin_fmaxf(__builtin_fmaxf((a), (b)), (c))
DI float max16(const f32x16& p) {
  float a = MX3(p[0], p[1], p[2]), b = MX3(p[3], p[4], p[5]);
  a = MX3(a, p[6], p[7]); b = MX3(b, p[8], p[9]); a = MX3(a, p[10], p[11]); b = MX3(b, p[12], p[13]);
  a = MX3(a, p[14], p[15]);
  return __builtin_fmaxf(a, b);
}
DI int keyidx(int i) { return (i & 3) + 16 * ((i >> 2) & 1) + 8 * (i >> 3); }
DI void redo_tile(unsigned tile, bf16x8 q0, bf16x8 q1, bf16x8 sel, f32x16& o, f32x16& negm, float& l, u32x4& p0, u32x4& p1, f32x4& L) {
  int ln = threadIdx.x & 63; PIN(ln);
  const unsigned ka = (tile & ~3u) + ln * 16, mode = tile & 3u;
  const bf16x8 k0 = lds128(ka), k1 = lds128(ka + 1024);
  const int rr = (ln & 31) - 4 * (ln >> 5);
  f32x16 s;
#pragma unroll
  for (int i = 0; i < 16; ++i) s[i] = (mode != 0 && (keyidx(i) <= rr) == (mode == 2)) ? -__builtin_inff() : negm[0];
  s = MFMA(k0, q0, s); s = MFMA(k1, q1, s);
  const float d = __builtin_fmaxf(swapmax(max16(s)), 0.f), f = __builtin_amdgcn_exp2f(-d), nm = negm[0] - d;
  l *= f;
#pragma unroll
  for (int i = 0; i < 16; ++i) { negm[i] = nm; o[i] *= f; s[i] = __builtin_amdgcn_exp2f(s[i] - d); }
  p0 = pack8(s, 0); p1 = pack8(s, 1);
  f32x4 z = {};
  L = __builtin_amdgcn_mfma_f32_16x16x32_bf16(sel, asb(p0), z, 0, 0, 0);
  L = __builtin_amdgcn_mfma_f32_16x16x32_bf16(sel, asb(p1), L, 0, 0, 0);
  float t = L[0]; PIN(t); L[0] = t;
  PIN(p0); PIN(p1); PIN(o); PIN(negm);
  asm volatile("s_nop 1" ::: "memory");
}

typedef unsigned long long u64x8 __attribute__((ext_vector_type(8)));
__global__ __launch_bounds__(512, 2) void k_fused(const void* xA_, const void* wA_, const void* woA_, const void* xB_, const void* wB_, const void* woB_,
                                                  void* ws_, void* out_) {
  unsigned long long dg0, dg1 = 0; asm volatile("s_memrealtime %0\n\ts_waitcnt lgkmcnt(0)" : "=s"(dg0) :: "memory"); if (DIAG == 99) dg1 = dg0;
  u64x8 ka; asm volatile("s_load_dwordx16 %0, %1, 0x0" : "=&s"(ka) : "s"(__builtin_amdgcn_kernarg_segment_ptr()) : "memory");
  __shared__ __attribute__((aligned(1024))) char smem[LDS_BYTES];
  const unsigned lds0 = (unsigned)(size_t)(LAS char*)smem;
  const unsigned bid = blockIdx.x, xcd = bid & 7, idx = bid >> 3;
  const unsigned br = xcd >> 2, R = RMAP, hp = HPMAP;
  const unsigned L = (br * 8 + R) * 16 + hp;
  const unsigned G = br * 8 + R, ns = hp, loc = hp >> 3;
  const unsigned tid = threadIdx.x, l = tid & 63, w = rfl(tid >> 6), wm = w & 3, wn = w >> 2;
  const unsigned mtw = w, mp = wn ? 3 - wm : wm;
  unsigned xv0 = (R * 256 + mtw * 32 + (l >> 3)) * 2048 + (((l & 7) ^ (l >> 4)) << 4);
  unsigned xv1 = (R * 256 + mtw * 32 + 8 + (l >> 3)) * 2048 + (((l & 7) ^ (4 + (l >> 4))) << 4);
  unsigned wv = (8 * w + (l >> 3)) * 6144 + (((l & 7) ^ ((((l >> 4) & 1) | ((w & 1) << 1)) << 1)) << 4) + hp * 128;
  asm volatile("s_waitcnt lgkmcnt(0)" : "+s"(ka), "+v"(xv0), "+v"(xv1), "+v"(wv) :: "memory");
  const void *xA = (const void*)ka[0], *wA = (const void*)ka[1], *woA = (const void*)ka[2], *xB = (const void*)ka[3], *wB = (const void*)ka[4], *woB = (const void*)ka[5];
  void *ws = (void*)ka[6], *out = (void*)ka[7];
  const __amdgpu_buffer_rsrc_t rx = mkrs(br ? xB : xA, NOMEMX * S_ * D_ * 2), rw = mkrs(br ? wB : wA, D_ * 3 * D_ * 2), rws = mkrs(ws, WS_END);
  STAMP(0);
  const unsigned xdst = lds0 + w * 4096, wdst = lds0 + LDS_W + w * 1024;
#define DMA_X(t, st) do { const unsigned so_ = (t) * 128u, d_ = xdst + (st); \
    dma16x(d_, xv0, rx, so_); dma16x(d_ + 1024, xv1, rx, so_); dma16x(d_ + 2048, xv0, rx, so_ + 32768); dma16x(d_ + 3072, xv1, rx, so_ + 32768); } while (0)
#define DMA_W(t, st) do { const unsigned so_ = (t) * (64u * 6144u), d_ = wdst + (st); \
    dma16w(d_, wv, rw, so_); dma16w(d_ + 8192, wv, rw, so_ + 2048); dma16w(d_ + 16384, wv, rw, so_ + 4096); } while (0)
  DMA_W(0, 0); DMA_X(0, 0);
  const unsigned fl_id = l < 32 ? G * 16 + (l >> 1) : (l == 32 ? L - 16 : L);
  const bool fl_ok = l < 34 && !(l == 32 && R == 0);
  unsigned obase = ld32_issue(rws, fl_ok ? WS_FLAG + fl_id * 128 : OOB);
  DMA_W(1, WST); DMA_X(1, XST);
  const unsigned pli = l < 13 ? l : 0;
  const unsigned pwl = ((idx & 3) * 8 + w) * 6 + (pli - 4);
  const unsigned pq = (idx * 8 + w) * 3 + (pli - 10);
  const char* pfp = pli < 4 ? (const char*)(br ? xB : xA) + (R * 256 + ((idx >> 2) * 8 + w) * 4 + pli) * 2048 + (PFD - 1) * 128
                  : pli < 10 ? (const char*)(br ? wB : wA) + (pwl / 3 + (PFD - 1) * 64) * 6144 + (pwl % 3) * 2048 + hp * 128
                             : (const char*)(br ? woB : woA) + (pq >> 3) * 2048 + loc * 1024 + (pq & 7) * 128;
  unsigned pstep = pli < 4 ? 128u : pli < 10 ? 64u * 6144u : 77u * 2048u;
  pf4(lds0 + LDS_CNT + 256, pfp); pfp += pstep;
#define CODEPF(bytes) do { unsigned long long pc; asm volatile("s_getpc_b64 %0" : "=s"(pc));     \
    const unsigned co = (w * 64 + l) * 128; \
    pf4(lds0 + LDS_CNT + 256, (const char*)(pc & ~127ull) + (co < (bytes) ? co : 0)); } while (0)
  if (tid < 8) *(LAS unsigned*)(size_t)(lds0 + LDS_CNT + tid * 4) = 0;
  const unsigned c = l & 15, g = l >> 4, q4 = c >> 2;
  const unsigned xa = lds0 + mp * 8192 + c * 128 + ((g ^ (c >> 1)) << 4);
  const unsigned wa = lds0 + LDS_W + (8 * g + q4) * 128 + (((2 * wn) ^ ((q4 >> 1) | ((g & 1) << 1))) << 5) + (l & 3) * 8;
  f32x4 acc[6][4] = {};
  u32x4 mk0, mk1; unsigned sel1 = ((c & 3) == 0 && ((c >> 2) & 1) == (g & 1)) ? 0x3F803F80u : 0u;
  { const int rr = (int)(l & 31) - 4 * (int)(l >> 5);
#pragma unroll
    for (int i = 0; i < 4; ++i) {
      mk0[i] = (keyidx(2 * i) <= rr ? 0xFFFFu : 0u) | (keyidx(2 * i + 1) <= rr ? 0xFFFF0000u : 0u);
      mk1[i] = (keyidx(8 + 2 * i) <= rr ? 0xFFFFu : 0u) | (keyidx(9 + 2 * i) <= rr ? 0xFFFF0000u : 0u); }
    PIN(mk0); PIN(mk1); PIN(sel1); }
  f32x16 oa, ob; f32x4 Ls; u32x4 p0, p1; bf16x8 v0, v1, sel;
  Frag A, B;
#define RDX(F, xb, s, u) F.x[u] = lds128(((xb) ^ ((s) << 6)) + (u) * 2048)
#define MM(C, n, u) do { const bf16x8 wf_ = cat(C.wl[n], C.wh[n]); if ((n) < 4) MFA(acc[n][u], wf_, C.x[u]); else MFA(acc[n][u], C.x[u], wf_); } while (0)
#define RDWL(F, wb, s, n) F.wl[n] = ldstr(((wb) ^ (((n) & 1) << 5)) + ((n) >> 1) * 8192 + (s) * 4096)
#define RDWH(F, wb, s, n) F.wh[n] = ldstr(((wb) ^ (((n) & 1) << 5)) + ((n) >> 1) * 8192 + (s) * 4096 + 512)
#define RD1(F, xb, wb, s, r) do { if ((r) == 0) RDX(F, xb, s, 0); else if ((r) == 1) RDWL(F, wb, s, 0); else if ((r) == 2) RDWH(F, wb, s, 0); \
    else if ((r) < 6) RDX(F, xb, s, ((r) - 2) & 3); else if (((r) & 1) == 0) RDWL(F, wb, s, (((r) + 8) >> 1) % 6); else RDWH(F, wb, s, (((r) + 8) >> 1) % 6); } while (0)
#define PB 27
#define PFP 36
#define BP(r) ((r) * 3 / 2)
#define AP(r) (29 + (r) * 6 / 5)
#define XP(j) (2 + (j) * 6)
#define WP(j) (28 + (j) * 3)
#define TILE(LDA, DMA, WAITB) do { \
    const unsigned xb = xa + xs, wb = wa + wsx, wd = wdst + wsx; \
    xp = xs; xs = (xs == 2 * XST) ? 0 : xs + XST; wsx ^= WST; \
    const unsigned xn = xa + xs, wn_ = wa + wsx; \
    sfor<0, 48>([&](auto mi) AI { constexpr int m_ = decltype(mi)::v; \
      if constexpr (m_ < 24) MM(A, m_ >> 2, m_ & 3); else MM(B, (m_ - 24) >> 2, m_ & 3); \
      sfor<0, 16>([&](auto ri) AI { constexpr int r_ = decltype(ri)::v; \
        if constexpr (BP(r_) == m_) RD1(B, xb, wb, 1, r_); if constexpr (LDA && AP(r_) == m_) RD1(A, xn, wn_, 0, r_); }); \
      sfor<0, 4>([&](auto ji) AI { constexpr int j_ = decltype(ji)::v; \
        if constexpr (DMA && XP(j_) == m_) dma16x(xd + j_ * 1024, (j_ & 1) ? xv1 : xv0, rx, xso + (j_ >> 1) * 32768); \
        if constexpr (DMA && j_ < 3 && WP(j_) == m_) dma16w(wd + j_ * 8192, wv, rw, wso + j_ * 2048); }); \
      if constexpr (PFI && DMA && m_ == PFP) { pf4(lds0 + LDS_CNT + 256, pfp); pfp += pinc; } \
      if constexpr (!LDA) {     \
        if constexpr (m_ == 28) { oa = f32x16{}; PIN(oa); } if constexpr (m_ == 34) { ob = f32x16{}; PIN(ob); } \
        if constexpr (m_ == 40) { p0 = u32x4{}; p1 = u32x4{}; v0 = bf16x8{}; v1 = bf16x8{}; PIN(p0); PIN(p1); PIN(v0); PIN(v1); } \
        if constexpr (m_ == 44) { Ls = f32x4{}; sel = asb(u32x4{sel1, sel1, sel1, sel1}); PIN(Ls); PIN(sel); } } \
      SBAR(); \
      if constexpr (m_ == PB) { WAITB; SBAR(); } }); } while (0)
  WAIT_BAR_V(8, obase);
  STAMP(1);
  unsigned xs = 0, wsx = 0, xp = 2 * XST;
  sfor<0, 16>([&](auto ri) AI { constexpr int r_ = decltype(ri)::v; RD1(A, xa, wa, 0, r_); SBAR(); });
  if (w == 0) st32(obase ^ ACKX, rws, (fl_ok && l <= 32 && !(l & 1)) ? WS_ACK + fl_id * 128 + (l < 32 ? ns : 16) * 4 : OOB);
  const unsigned pbase = __builtin_amdgcn_readlane(obase, 32), mybase = __builtin_amdgcn_readlane(obase, 33);
  _Pragma("unroll") for (int i_ = 0; i_ < 24; ++i_) PIN(acc[i_ >> 2][i_ & 3]);
  asm volatile("s_nop 1" ::: "memory");
  SBAR();
  const bool ack_ok = l < 17 && !(l == 16 && R == 7);
  const unsigned ackoff = ack_ok ? WS_ACK + L * 128 + l * 4 : OOB;
  const char* codep;
  { unsigned long long pc; asm volatile("s_getpc_b64 %0" : "=s"(pc)); const unsigned co = (w * 64 + l) * 128; codep = (const char*)(pc & ~127ull) + (co < CODE_BYTES ? co : 0); }
#pragma unroll 1
  for (int t = 0; t < 15; ++t) {
    const unsigned xso = t < 14 ? (t + 2) * 128u : 0x80000000u, wso = t < 14 ? (t + 2) * (64u * 6144u) : OOB;
    const unsigned xd = t < 14 ? xdst + xp : lds0 + LDS_SPARE;
    if (__builtin_expect((unsigned)(t - 11) < 2u, 0)) {
      if (t == 11) {
        dma4_sc1(lds0 + LDS_CTL + w * 256, ackoff, rws, 0);
        pf4(lds0 + LDS_CNT + 256, codep);
      } else {
        pfp = (const char*)(br ? woB : woA) + ((idx * 8 + w) * 4 + ((l >> 3) & 3)) * 2048 + loc * 1024 + (l & 7) * 128; pstep = 0;
      }
    }
    const unsigned pinc = pstep;
    TILE(true, true, WAIT_BAR(5));
  }
  bool acked = __all(!ack_ok || *(volatile LAS unsigned*)(size_t)(lds0 + LDS_CTL + w * 256 + l * 4) == (mybase ^ ACKX));
  __builtin_amdgcn_s_setprio(2);
  { const unsigned xso = 0, wso = 0, xd = 0, pinc = 0; TILE(false, false, (void)0); }
  asm volatile("s_nop 7" ::: "memory");
  _Pragma("unroll") for (int i_ = 0; i_ < 24; ++i_) PIN(acc[i_ >> 2][i_ & 3]);
  STAMP(2);
  bf16x8 qa0, qa1, qb0, qb1;
  f32x16 sa, sb, na, nb;
  {
    const unsigned ko = (g & 1) * 1024 + ((g >> 1) * 32 + (c & 7) + 16 * (c >> 3)) * 16;
    const unsigned vo = IMG + (g >> 1) * 1024 + ((g & 1) * 32 + c) * 16;
    const unsigned t0 = wn * 32768 + mp * 2 * TIL;
    const unsigned gs = R < 7 ? WS_KV + L * 65536 + t0 : OOB;
#pragma unroll
    for (int u = 0; u < 4; ++u) {
      const unsigned tb = (u >> 1) * TIL;
      const u32x4 kk = pack44(acc[2][u], acc[3][u]), vv = pack44(acc[4 + (u & 1)][u & 2], acc[4 + (u & 1)][(u & 2) + 1]);
      __builtin_amdgcn_raw_buffer_store_b128(kk, rws, ko + (u & 1) * 128 + tb, gs, SC1);
      __builtin_amdgcn_raw_buffer_store_b128(vv, rws, vo + (u & 1) * 256 + tb, gs, SC1);
      sts128(lds0 + LDS_OWN + t0 + tb + ko + (u & 1) * 128, kk);
      sts128(lds0 + LDS_OWN + t0 + tb + vo + (u & 1) * 256, vv);
    }
    lds_inc(lds0 + LDS_CNT + wn * 4);
    if (mp < 2) __builtin_amdgcn_s_setprio(1);
    u32x4 qf[4]; float sd[4];
#pragma unroll
    for (int u = 0; u < 4; ++u) {
      const f32x4 qx = acc[0][u] * QSCALE, qy = acc[1][u] * QSCALE;
      qf[u] = pack44(qx, qy);
      float d = qx[0] * acc[2][u][0];
#pragma unroll
      for (int i = 1; i < 4; ++i) d = __builtin_fmaf(qx[i], acc[2][u][i], d);
#pragma unroll
      for (int i = 0; i < 4; ++i) d = __builtin_fmaf(qy[i], acc[3][u][i], d);
      sd[u] = d;
    }
#pragma unroll
    for (int p = 0; p < 2; ++p) {
#pragma unroll
      for (int i = 0; i < 4; ++i) { auto r = __builtin_amdgcn_permlane16_swap(qf[2 * p][i], qf[2 * p + 1][i], false, false); qf[2 * p][i] = r[0]; qf[2 * p + 1][i] = r[1]; }
      auto r = __builtin_amdgcn_permlane16_swap(__float_as_uint(sd[2 * p]), __float_as_uint(sd[2 * p + 1]), false, false);
      sd[2 * p] = -swapsum(__uint_as_float(r[0]) + __uint_as_float(r[1]));
    }
    qb0 = asb(qf[0]); qb1 = asb(qf[1]); qa0 = asb(qf[2]); qa1 = asb(qf[3]);
    { const float one = l < 32 ? 1.f : 0.f;
      asm volatile("s_nop 1\n\tv_mfma_f32_32x32x2_f32 %0, %2, %3, 0\n\tv_mfma_f32_32x32x2_f32 %1, %2, %4, 0" : "=&v"(na), "=&v"(nb) : "v"(one), "v"(sd[2]), "v"(sd[0])); }
  }
  float la = 0.f, lb = 0.f;
  bf16x8 k0, k1;
  PIN(qa0); PIN(qa1); PIN(qb0); PIN(qb1);
  STAMP(3);
  const __amdgpu_buffer_rsrc_t rwo = mkrs(br ? woB : woA, D_ * D_ * 2);
  const unsigned wov = (l >> 3) * 2048 + (((l & 7) ^ (((l >> 4) & 3) << 1)) << 4) + ns * 128;
#define WOB(gq) ((gq) == 0 ? 131072u : ((gq) & 3) == 3 ? 0u : (gq) * 32768u)
#define DMA_WO(j, ww) dma16(lds0 + WOB((j) >> 2) + ((j) & 3) * 8192 + (ww) * 1024, wov, rwo, ((j) ^ (loc * 8)) * (64u * 2048u) + (ww) * 16384u)
  const unsigned lb16 = l * 16, own = lds0 + LDS_OWN + wn * 32768, prv = R > 0 ? lds0 + wn * 98304 : own;
  const unsigned myflag = WS_FLAG + L * 128, pfoff = WS_FLAG + (L - 16) * 128 + 4 + wn * 4;
#define QK0(S, Q, N) asm volatile("v_mfma_f32_32x32x16_bf16 %0, %1, %2, %3" : "=&v"(S) : "v"(k0), "v"(Q), "v"(N), "v"(k1))
#define QK1(S, Q) asm volatile("v_mfma_f32_32x32x16_bf16 %0, %1, %2, %0" : "+v"(S) : "v"(k1), "v"(Q))
#define PV0(O) asm volatile("v_mfma_f32_32x32x16_bf16 %0, %1, %2, %0" : "+v"(O) : "v"(v0), "v"(p0), "v"(v1))
#define PV1(O) asm volatile("v_mfma_f32_32x32x16_bf16 %0, %1, %2, %0" : "+v"(O) : "v"(v1), "v"(p1))
#define EX(S, i) do { S[i] = __builtin_amdgcn_exp2f(S[i]); } while (0)
#define RDK(a) do { k0 = lds128(a); k1 = lds128((a) + 1024); } while (0)
  unsigned pa = own + (2 * mp + 1) * TIL, pb = pa - TIL, pbp = pb;
  unsigned va = lb16 + pa, vb = lb16 + pb;
  RDK(va);
  asm volatile("s_nop 1" ::: "memory");
  QK0(sa, qa0, na); QK1(sa, qa1);
  RDK(vb);
  asm volatile("s_nop 7\n\ts_nop 1" ::: "memory");
  SBAR();
#define EXP(i) e_[i] = __builtin_amdgcn_exp2f(sx_[i])
#define HALF(SX, SY, NY, LY, OY, QY0, QY1, PY, RD, VX, KNEXT, BAR1, MSK, LSUM) do { \
    const f32x16 sx_ = SX; float e_[16]; u32x4 pn_; \
    EXP(0); EXP(1); EXP(2); EXP(3); EXP(4); asm volatile("" : "+v"(e_[0]), "+v"(e_[1]), "+v"(e_[2]), "+v"(e_[3]), "+v"(e_[4])); SBAR();     \
    EXP(5); asm volatile("" : "+v"(e_[5]), "+v"(Ls)); SBAR(); \
    unsigned long long bad_ = __builtin_amdgcn_ballot_w64(!(Ls[0] <= PBOUND)); asm volatile("" : "+s"(bad_)); SBAR(); \
    pn_[0] = pk2(e_[0], e_[1]); pn_[1] = pk2(e_[2], e_[3]); pn_[2] = pk2(e_[4], e_[5]); EXP(6); \
    asm volatile("" : "+v"(pn_[0]), "+v"(pn_[1]), "+v"(pn_[2]), "+v"(e_[6])); SBAR(); \
    if (__builtin_expect(bad_ != 0, 0)) redo_tile(PY, QY0, QY1, sel, OY, NY, LY, p0, p1, Ls); \
    PV0(OY); SBAR(); \
    EXP(7); EXP(8); LY += Ls[0]; pn_[3] = pk2(e_[6], e_[7]); asm volatile("" : "+v"(pn_[3]), "+v"(e_[8]), "+v"(LY)); SBAR(); \
    PV1(OY); SBAR(); if (RD) { v0 = lds128((VX) + IMG); v1 = lds128((VX) + IMG + 1024); VX = KNEXT; } \
    EXP(9); EXP(10); asm volatile("" : "+v"(e_[9]), "+v"(e_[10]), "+v"(VX)); SBAR(); \
    QK0(SY, QY0, NY); SBAR(); EXP(11); EXP(12); EXP(13); asm volatile("" : "+v"(e_[11]), "+v"(e_[12]), "+v"(e_[13])); SBAR(); \
    QK1(SY, QY1); SBAR(); \
    EXP(14); EXP(15); asm volatile("" : "+v"(e_[14]), "+v"(e_[15])); SBAR(); \
    if (BAR1) { if (PFI) WAITV(9); else WAITV(8); lds_inc(lds0 + LDS_CNT + 8); if (mp) lds_wait(lds0 + LDS_CNT + wn * 4, 4); } \
    if (RD) RDK(VX); SBAR(); \
    p0 = pn_; p1[0] = pk2(e_[8], e_[9]); p1[1] = pk2(e_[10], e_[11]); p1[2] = pk2(e_[12], e_[13]); p1[3] = pk2(e_[14], e_[15]); SBAR(); \
    if (MSK) { p0 &= mk0; p1 &= mk1; PIN(p0); PIN(p1); asm volatile("s_nop 0"); } \
    LSUM; SBAR(); } while (0)
#define LMF0 "v_mfma_f32_16x16x32_bf16 %0, %1, %2, 0\n\t"
#define LMF1 "v_mfma_f32_16x16x32_bf16 %0, %1, %3, %0\n\t"
#define LSUM_G() asm volatile(LMF0 "s_nop 0\n\t" LMF1 "s_nop 1" : "=&v"(Ls) : "v"(sel), "v"(p0), "v"(p1))
#define LSUM_A() asm volatile(LMF0 "s_nop 0\n\t" LMF1 "s_nop 0" : "=&v"(Ls) : "v"(sel), "v"(p0), "v"(p1))
#define LSUM_B() asm volatile("v_mfma_f32_16x16x32_bf16 %0, %3, %4, 0\n\ts_mov_b32 %1, %2\n\tv_mfma_f32_16x16x32_bf16 %0, %3, %5, %0\n\ts_add_i32 %2, %2, 0x1000" : "=&v"(Ls), "=&s"(pbp), "+s"(pa) : "v"(sel), "v"(p0), "v"(p1) : "scc")
  unsigned sent = 0;

#define REPF() do {} while (0)
#define SIGNAL() do { WAITV(0); \
    _Pragma("unroll 1") for (unsigned sp = 0; !acked && sp < SPIN_CAP; ++sp) { __builtin_amdgcn_s_sleep(2); acked = __all(!ack_ok || ld32_wait(rws, ackoff) == (mybase ^ ACKX)); } \
    if (lds_inc_rtn(lds0 + LDS_CNT + 24 + wn * 4) == 3 && l == 0) st32(mybase + 1, rws, myflag + 4 + wn * 4); \
    REPF(); \
    sent = 1; if (mp >= 2) __builtin_amdgcn_s_setprio(0); STAMP(13); } while (0)
#define TRYSIG() do { if (__builtin_expect(!sent, 0)) { if ((__builtin_amdgcn_s_getreg(7 | (31 << 11)) & 0x00C0000Fu) == 0) SIGNAL(); } } while (0)
#define PAIR(PAN, PBN, MD, FIRST) do { const unsigned pan = (PAN), pbn = (PBN); \
      HALF(sa, sb, nb, lb, ob, qb0, qb1, pbp, true, va, lb16 + pan, FIRST, (MD) != 0, LSUM_G()); \
      HALF(sb, sa, na, la, oa, qa0, qa1, pa | (MD), true, vb, lb16 + pbn, false, (MD) != 0, LSUM_G()); \
      pbp = pb | (MD); pa = pan; pb = pbn; } while (0)
#define HOT(N, SIG) do { _Pragma("unroll 1") for (int n = (N); n > 0; --n) {     \
      if (SIG) TRYSIG(); \
      HALF(sa, sb, nb, lb, ob, qb0, qb1, pbp, true, va, va + TIL, false, false, LSUM_A()); \
      HALF(sb, sa, na, la, oa, qa0, qa1, pa, false, vb, 0, false, false, LSUM_B()); } } while (0)
  PAIR(own, own, 1u, true);
  HOT(2 * mp, true);
  {
    STAMP(4);
    if (!sent) SIGNAL();
    STAMP(5);
    mk0 = ~mk0; mk1 = ~mk1; PIN(mk0); PIN(mk1);
    if (mp < 2) {
      lds_wait(lds0 + LDS_CNT + 8, 8);
      unsigned h_ = wn; asm volatile("" : "+s"(h_));
      if (mp) {
#pragma unroll
        for (int j = 0; j < 15; ++j) DMA_WO(j >> 2, h_ * 4 + (j & 3));
        if (!h_) DMA_WO(3, 3);
      } else if (R > 0) {
#pragma unroll 1
        for (unsigned sp = 0; sp < SPIN_CAP; ++sp) {
          if (rfl(ld32_wait(rws, pfoff)) == pbase + 1) break;
          __builtin_amdgcn_s_sleep(KVS);
        }
        STAMP(14);
#pragma unroll
        for (int j = 0; j < 8; ++j) dma64_sc1(lds0 + h_ * 98304 + j * TIL, lb16, rws, WS_KV + (L - 16) * 65536 + h_ * 32768 + j * TIL);
        WAITV(12);
        lds_inc(lds0 + LDS_CNT + 12 + h_ * 4);
      }
    }
    STAMP(6);
    if (R > 0) lds_wait(lds0 + LDS_CNT + 12 + wn * 4, mp < 2 ? 1 : 2);
    STAMP(7);
    pb = prv + (2 * mp + 1) * TIL; vb = lb16 + pb;
    RDK(vb);
    SBAR();
  }
  PAIR(prv + (2 * mp + 1) * TIL, prv + 2 * mp * TIL, 0u, false);
  lds_inc(lds0 + LDS_CNT + 20);
  if (R > 0) {
    if (mp == 0) { WAITV(0); lds_inc(lds0 + LDS_CNT + 12 + wn * 4); }
    PAIR(prv + (2 * mp + 2) * TIL, prv + (2 * mp + 2) * TIL, 2u, false);
    if (mp == 1) lds_wait(lds0 + LDS_CNT + 12 + wn * 4, 2);
    HOT(6 - 2 * mp, false);
    asm volatile("s_nop 7" : "+v"(Ls) :: "memory");
    if (__builtin_expect(__any(!(Ls[0] <= PBOUND)), 0)) redo_tile(pbp, qb0, qb1, sel, ob, nb, lb, p0, p1, Ls);
    lb += Ls[0];
    PV0(ob); PV1(ob);
  } else {
    asm volatile("s_nop 7" : "+v"(Ls), "+v"(sa) :: "memory");
  }
  __builtin_amdgcn_s_setprio(0);
  STAMP(8);
  { const float ia = __builtin_amdgcn_rcpf(la), ib = __builtin_amdgcn_rcpf(lb);
    oa *= ia;
    const unsigned hh = hp * 2 + wn, T0 = br * 64 + R * 8 + 2 * mp;
    const unsigned ol = ((l >> 4) & 1) * 1024 + ((l >> 5) * 16 + (l & 15)) * 16;
    __builtin_amdgcn_raw_buffer_store_b128(pack8(oa, 0), rws, ol, WS_O + (T0 * 32 + 32 + hh) * IMG, SC1);
    __builtin_amdgcn_raw_buffer_store_b128(pack8(oa, 1), rws, ol + 512, WS_O + (T0 * 32 + 32 + hh) * IMG, SC1);
    SBAR();
    asm volatile("s_nop 7" : "+v"(ob) :: "memory");
    ob *= ib;
    __builtin_amdgcn_raw_buffer_store_b128(pack8(ob, 0), rws, ol, WS_O + (T0 * 32 + hh) * IMG, SC1);
    __builtin_amdgcn_raw_buffer_store_b128(pack8(ob, 1), rws, ol + 512, WS_O + (T0 * 32 + hh) * IMG, SC1); }
  WAITV(0);
  STAMP(9);
  asm volatile("s_barrier" ::: "memory");
  if (tid == 64) st32(mybase + 2, rws, myflag);
  dma4(lds0 + LDS_WOX + 1024, l < 32 ? (l >> 4) * 65536 + (l & 15) * 128 : OOB, rws, WS_O + ((br * 64 + R * 8 + 2 * mp) * 32 + hp * 2 + wn) * IMG);
  if (w == 7) DMA_WO(3, 7);
  const unsigned fo = l < 32 ? WS_FLAG + (G * 16 + (l >> 1)) * 128 : OOB;
#pragma unroll
  for (int j = 4; j < 16; ++j) DMA_WO(j, w);
  if (w == POLLW) {
    unsigned f;
#pragma unroll 1
    for (unsigned sp = 0; sp < SPIN_CAP; ++sp) {
      f = ld32_wait(rws, fo);
      if (__all((l >> 4) != loc || f == obase + 2)) break;
      __builtin_amdgcn_s_sleep(POLLS);
    }
    *(volatile LAS unsigned*)(size_t)(lds0 + LDS_WOX) = __all(l >= 32 || f == obase + 2);
  }
  WAIT_BAR(0);
  STAMP(10);
  const unsigned T = G * 8 + w;
  const unsigned obs = WS_O + T * 32 * IMG;
  const unsigned wa2 = lds0 + (16 * (g >> 1) + 4 * (g & 1) + q4) * 128 + ((2 * (g & 1) + (q4 >> 1)) << 5) + (l & 3) * 8;
  f32x4 oc[4][2] = {};
  u32x4 of[16];
  s16x4 wl[2][4], wh[2][4];
#pragma unroll
  for (int i = 0; i < 16; ++i) {
    of[i] = __builtin_amdgcn_raw_buffer_load_b128(rws, l * 16 + (i & 3) * 1024, obs + loc * 32768 + (i >> 2) * 4096, SC1);
    if (i == FILL0 - 1 || i == FILL1 - 1) { SBAR(); __builtin_amdgcn_s_sleep(FILLS); SBAR(); }
  }
  const unsigned allrdy = rfl(*(volatile LAS unsigned*)(size_t)(lds0 + LDS_WOX));
#pragma unroll
  for (int nt = 0; nt < 4; ++nt) { wl[0][nt] = ldstr((wa2 ^ (nt << 5)) + WOB(0)); wh[0][nt] = ldstr((wa2 ^ (nt << 5)) + WOB(0) + 1024); }
  _Pragma("unroll") for (int i_ = 0; i_ < 8; ++i_) PIN(oc[i_ >> 1][i_ & 1]);
  asm volatile("s_nop 1" ::: "memory");
  SBAR();
#pragma unroll 1
  for (int gq = 0; gq < 4; ++gq) {
    const unsigned wb = wa2 + WOB(gq), wbn = wa2 + WOB(gq + 1);
    if (__builtin_expect(gq == 1 && !allrdy, 0)) {
      if (w == POLLW) {
#pragma unroll 1
        for (unsigned sp = 0; sp < SPIN_CAP; ++sp) { if (__all(l >= 32 || ld32_wait(rws, fo) == obase + 2)) break; __builtin_amdgcn_s_sleep(POLLS); }
      }
      asm volatile("s_barrier" ::: "memory");
    }
    const unsigned on = gq < 3 ? obs + ((gq + 1) ^ (loc * 2)) * 16384 : OOB;
    if (gq == 0) __builtin_amdgcn_s_setprio(3); else if (gq == 1) __builtin_amdgcn_s_setprio(2); else if (gq == 2) __builtin_amdgcn_s_setprio(1); else __builtin_amdgcn_s_setprio(0);
    sfor<0, 8>([&](auto hi) AI { constexpr int h_ = decltype(hi)::v, cur = h_ & 1, nxt = cur ^ 1;
      sfor<0, 8>([&](auto mi) AI { constexpr int m_ = decltype(mi)::v, nt = m_ >> 1, tt = m_ & 1;
        { const bf16x8 wf = cat(wl[cur][nt], wh[cur][nt]);
          if constexpr (m_ == 0) {
            const bf16x8 w3 = cat(wl[cur][3], wh[cur][3]);
            asm volatile("v_mfma_f32_16x16x32_bf16 %0, %1, %2, %0" : "+v"(oc[0][0]) : "v"(wf), "v"(of[2 * h_]), "v"(w3), "v"(of[2 * h_ + 1]));
          } else MFA(oc[nt][tt], wf, of[2 * h_ + tt]); }
        sfor<0, 8>([&](auto ri) AI { constexpr int r_ = decltype(ri)::v, rn = r_ >> 1;
          if constexpr (G2R(r_) == m_) {
            const unsigned wq_ = ((h_ < 7 ? wb : wbn) ^ (rn << 5)) + ((h_ + 1) & 7) * 4096;
            if constexpr ((r_ & 1) == 0) wl[nxt][rn] = ldstr(wq_); else wh[nxt][rn] = ldstr(wq_ + 1024); } });
        if constexpr (m_ >= 6) of[2 * h_ + tt] = __builtin_amdgcn_raw_buffer_load_b128(rws, l * 16 + ((2 * h_ + tt) & 3) * 1024, on + ((2 * h_ + tt) >> 2) * 4096, SC1);
        SBAR(); }); });
  }
  asm volatile("s_nop 7" ::: "memory");
  _Pragma("unroll") for (int i_ = 0; i_ < 8; ++i_) PIN(oc[i_ >> 1][i_ & 1]);
  STAMP(11);
  const __amdgpu_buffer_rsrc_t rout = mkrs(out, S_ * 2 * D_ * 2);
  const unsigned orow = ((R * 8 + w) * 32 + c) * 4096 + br * 2048 + ns * 128 + (g & 1) * 32 + (g >> 1) * 16;
  const unsigned orow2 = orow - (c & 8) * 4096 + (c & 8) * 8;
#pragma unroll
  for (int tt = 0; tt < 2; ++tt) {
    u32x4 v[2], va_, vb_;
#pragma unroll
    for (int np = 0; np < 2; ++np) {
      const f32x4 &a = oc[2 * np][tt], &b = oc[2 * np + 1][tt];
      auto r0 = __builtin_amdgcn_permlane16_swap(pk2(a[0], a[1]), pk2(b[0], b[1]), false, false);
      auto r1 = __builtin_amdgcn_permlane16_swap(pk2(a[2], a[3]), pk2(b[2], b[3]), false, false);
      v[np] = u32x4{r0[0], r1[0], r0[1], r1[1]};
    }
#pragma unroll
    for (int i = 0; i < 4; ++i) {
      const unsigned y = c < 8 ? v[1][i] : v[0][i];
      const unsigned r = (unsigned)__builtin_amdgcn_update_dpp(0, (int)y, 0x128, 0xf, 0xf, false);
      va_[i] = c < 8 ? v[0][i] : r; vb_[i] = c < 8 ? r : v[1][i];
    }
    __builtin_amdgcn_raw_buffer_store_b128(va_, rout, orow2 + tt * 65536, 0, OUTPOL);
    __builtin_amdgcn_raw_buffer_store_b128(vb_, rout, orow2 + tt * 65536 + 32768, 0, OUTPOL);
  }
  { WAITV(0); STAMP(12); const unsigned long long te = __builtin_amdgcn_s_memrealtime() + (dg1 - dg0); while (__builtin_amdgcn_s_memrealtime() < te) __builtin_amdgcn_s_sleep(1); }
}

extern "C" void kernel_launch(void* const* d_in, const int* in_sizes, int n_in, void* d_out, int out_size, void* d_ws, size_t ws_size,
                              hipStream_t stream) {
  const size_t wsoff = (ws_size - WS_END) & ~(size_t)0xfffff;
  k_fused<<<256, 512, 0, stream>>>(d_in[0], d_in[1], d_in[2], d_in[3], d_in[4], d_in[5], (char*)d_ws + wsoff, d_out);
}
```

```cpp
#define PFD 3
#define POLLS 8
#include <hip/hip_runtime.h>
#include <hip/hip_bf16.h>

typedef short bf16x8 __attribute__((ext_vector_type(8)));
typedef short s16x4 __attribute__((ext_vector_type(4)));
typedef float f32x16 __attribute__((ext_vector_type(16)));
typedef float f32x4 __attribute__((ext_vector_type(4)));
typedef unsigned u32x4 __attribute__((ext_vector_type(4)));
typedef __bf16 bf16x2_t __attribute__((ext_vector_type(2)));
typedef float f32x2 __attribute__((ext_vector_type(2)));
#define LAS __attribute__((address_space(3)))
#define DI __device__ __forceinline__
#define AI __attribute__((always_inline))
#define MFMA(a, b, c) __builtin_amdgcn_mfma_f32_32x32x16_bf16((a), (b), (c), 0, 0, 0)
#define SBAR() __builtin_amdgcn_sched_barrier(0)
#define PIN(x) asm volatile("" : "+v"(x))
#define MFA(acc_, a_, b_) asm volatile("v_mfma_f32_16x16x32_bf16 %0, %1, %2, %0" : "+v"(acc_) : "v"(a_), "v"(b_))

constexpr int S_ = 2048, D_ = 1024;
constexpr float QSCALE = 0.17677669529663687f * 1.4426950408889634f;
constexpr float PBOUND = 1073741824.0f;
constexpr unsigned IMG = 2048;
constexpr unsigned WS_FLAG = 0, WS_ACK = 32768, WS_KV = 65536, WS_O = WS_KV + 256u * 65536u, WS_END = WS_O + 2u * 64u * 32u * IMG;
constexpr unsigned ACKX = 0x5bd1e995u;
constexpr int SC1 = 16;
constexpr unsigned CODE_BYTES = 25600;
constexpr unsigned SPIN_CAP = 1u << 18;

#define HPMAP ((xcd & 1) * 8 + (idx >> 2))
#define RMAP (((xcd >> 1) & 1) * 4 + (idx & 3))
#define NOMEMX 1
template <int I> struct IC { static constexpr int v = I; };
template <int B, int E, class F> DI void sfor(F&& f) { if constexpr (B < E) { f(IC<B>{}); sfor<B + 1, E>(f); } }
DI unsigned rfl(unsigned x) { return (unsigned)__builtin_amdgcn_readfirstlane((int)x); }
DI unsigned pk2(float lo, float hi) { f32x2 v = {lo, hi}; return __builtin_bit_cast(unsigned, __builtin_convertvector(v, bf16x2_t)); }
DI u32x4 pack8(const f32x16& x, int s) {
  u32x4 p;
  p[0] = pk2(x[8 * s + 0], x[8 * s + 1]); p[1] = pk2(x[8 * s + 2], x[8 * s + 3]);
  p[2] = pk2(x[8 * s + 4], x[8 * s + 5]); p[3] = pk2(x[8 * s + 6], x[8 * s + 7]);
  return p;
}
DI u32x4 pack44(const f32x4& a, const f32x4& b) { u32x4 p; p[0] = pk2(a[0], a[1]); p[1] = pk2(a[2], a[3]); p[2] = pk2(b[0], b[1]); p[3] = pk2(b[2], b[3]); return p; }
DI bf16x8 asb(u32x4 v) { return __builtin_bit_cast(bf16x8, v); }
DI bf16x8 lds128(unsigned a) { return *(const LAS bf16x8*)(size_t)a; }
DI void sts128(unsigned a, u32x4 v) { *(LAS u32x4*)(size_t)a = v; }
DI s16x4 ldstr(unsigned a) { return __builtin_amdgcn_ds_read_tr16_b64_v4i16((LAS s16x4*)(size_t)a); }
DI bf16x8 cat(s16x4 lo, s16x4 hi) { return __builtin_shufflevector(lo, hi, 0, 1, 2, 3, 4, 5, 6, 7); }
DI void dma16(unsigned ldsdst, unsigned voff, __amdgpu_buffer_rsrc_t rs, unsigned soff) {
  asm volatile("s_mov_b32 m0, %0\n\ts_nop 0\n\tbuffer_load_dwordx4 %1, %2, %3 offen lds" ::"s"(ldsdst), "v"(voff), "s"(rs), "s"(soff) : "memory");
}
#define XPOL ""
#define WPOL ""
DI void dma16x(unsigned ldsdst, unsigned voff, __amdgpu_buffer_rsrc_t rs, unsigned soff) {
  asm volatile("s_mov_b32 m0, %0\n\ts_nop 0\n\tbuffer_load_dwordx4 %1, %2, %3 offen " XPOL " lds" ::"s"(ldsdst), "v"(voff), "s"(rs), "s"(soff) : "memory");
}
DI void dma16w(unsigned ldsdst, unsigned voff, __amdgpu_buffer_rsrc_t rs, unsigned soff) {
  asm volatile("s_mov_b32 m0, %0\n\ts_nop 0\n\tbuffer_load_dwordx4 %1, %2, %3 offen " WPOL " lds" ::"s"(ldsdst), "v"(voff), "s"(rs), "s"(soff) : "memory");
}
#define PPOL ""
#define OUTPOL 16
#define KVS 1
#define FILL0 4
#define FILL1 8
#define FILLS 4
#define G2R(r) ((r) * 3 / 4)
#define POLLW 0
#define PFMODE 0
#define PFI true
#define PFH 1
DI void pf4(unsigned ldsdst, const char* p) {
  asm volatile("s_mov_b32 m0, %0\n\ts_nop 0\n\tglobal_load_lds_dword %1, off " PPOL ::"s"(ldsdst), "v"(p) : "memory");
}
DI void dma16_sc1(unsigned ldsdst, unsigned voff, __amdgpu_buffer_rsrc_t rs, unsigned soff) {
  asm volatile("s_mov_b32 m0, %0\n\ts_nop 0\n\tbuffer_load_dwordx4 %1, %2, %3 offen sc1 lds" ::"s"(ldsdst), "v"(voff), "s"(rs), "s"(soff) : "memory");
}
#define WAITV(n) asm volatile("s_waitcnt vmcnt(" #n ")" ::: "memory")
#define WAIT_BAR(n) asm volatile("s_waitcnt vmcnt(" #n ") lgkmcnt(0)\n\ts_barrier" ::: "memory")
DI __amdgpu_buffer_rsrc_t mkrs(const void* p, unsigned bytes) { return __builtin_amdgcn_make_buffer_rsrc((void*)p, (short)0, (int)bytes, 0x00020000); }
DI unsigned ld32_issue(__amdgpu_buffer_rsrc_t rs, unsigned off) { unsigned r; asm volatile("buffer_load_dword %0, %1, %2, 0 offen sc1" : "=v"(r) : "v"(off), "s"(rs) : "memory"); return r; }
DI unsigned ld32_wait(__amdgpu_buffer_rsrc_t rs, unsigned off) { unsigned r; asm volatile("buffer_load_dword %0, %1, %2, 0 offen sc1\n\ts_waitcnt vmcnt(0)" : "=&v"(r) : "v"(off), "s"(rs) : "memory"); return r; }
DI u32x4 ld128_issue(__amdgpu_buffer_rsrc_t rs, unsigned off) { u32x4 r; asm volatile("buffer_load_dwordx4 %0, %1, %2, 0 offen sc1" : "=v"(r) : "v"(off), "s"(rs) : "memory"); return r; }
DI u32x4 ld128_wait(__amdgpu_buffer_rsrc_t rs, unsigned off) { u32x4 r; asm volatile("buffer_load_dwordx4 %0, %1, %2, 0 offen sc1\n\ts_waitcnt vmcnt(0)" : "=&v"(r) : "v"(off), "s"(rs) : "memory"); return r; }
DI void dma4(unsigned ldsdst, unsigned voff, __amdgpu_buffer_rsrc_t rs, unsigned soff) {
  asm volatile("s_mov_b32 m0, %0\n\ts_nop 0\n\tbuffer_load_dword %1, %2, %3 offen lds" ::"s"(ldsdst), "v"(voff), "s"(rs), "s"(soff) : "memory");
}
DI void dma4_sc1(unsigned ldsdst, unsigned voff, __amdgpu_buffer_rsrc_t rs, unsigned soff) {
  asm volatile("s_mov_b32 m0, %0\n\ts_nop 0\n\tbuffer_load_dword %1, %2, %3 offen sc1 lds" ::"s"(ldsdst), "v"(voff), "s"(rs), "s"(soff) : "memory");
}
#define WAIT_BAR_V(n, x) asm volatile("s_waitcnt vmcnt(" #n ") lgkmcnt(0)\n\ts_barrier" : "+v"(x) :: "memory")
constexpr unsigned OOB = 0x80000000u;
DI void st32(unsigned v, __amdgpu_buffer_rsrc_t rs, unsigned off) { __builtin_amdgcn_raw_buffer_store_b32(v, rs, off, 0, SC1); }

#define STAMP(i) do {} while (0)

constexpr unsigned XST = 32768, WST = 24576, LDS_W = 3 * XST, LDS_SPARE = LDS_W + 2 * WST, LDS_CTL = 159744;

constexpr unsigned LDS_OWN = 32768, LDS_CNT = 162816, LDS_WOX = 98304, LDS_BYTES = 163840, TIL = 2 * IMG;

struct Frag { bf16x8 x[4]; s16x4 wl[6], wh[6]; };

DI void lds_inc(unsigned a) { asm volatile("s_mov_b64 exec, 1\n\tds_add_u32 %0, %1\n\ts_mov_b64 exec, -1" ::"v"(a), "v"(1u) : "memory"); }
DI unsigned lds_inc_rtn(unsigned a) { unsigned r; asm volatile("s_mov_b64 exec, 1\n\tds_add_rtn_u32 %0, %1, %2\n\ts_mov_b64 exec, -1\n\ts_waitcnt lgkmcnt(0)" : "=&v"(r) : "v"(a), "v"(1u) : "memory"); return rfl(r); }
DI void lds_wait(unsigned a, unsigned n) {
#pragma unroll 1
  for (unsigned sp = 0; sp < SPIN_CAP && rfl(*(volatile LAS unsigned*)(size_t)a) < n; ++sp) __builtin_amdgcn_s_sleep(1);
  asm volatile("" ::: "memory");
}
DI void dma64_sc1(unsigned ldsdst, unsigned voff, __amdgpu_buffer_rsrc_t rs, unsigned soff) {
  asm volatile("s_mov_b32 m0, %0\n\ts_nop 0\n\tbuffer_load_dwordx4 %1, %2, %3 offen sc1 lds\n\tbuffer_load_dwordx4 %1, %2, %3 offen offset:1024 sc1 lds\n\t"
               "buffer_load_dwordx4 %1, %2, %3 offen offset:2048 sc1 lds\n\tbuffer_load_dwordx4 %1, %2, %3 offen offset:3072 sc1 lds" ::"s"(ldsdst), "v"(voff), "s"(rs), "s"(soff) : "memory");
}
DI float swapmax(float m) {
  auto rr = __builtin_amdgcn_permlane32_swap(__float_as_uint(m), __float_as_uint(m), false, false);
  return __builtin_fmaxf(__uint_as_float(rr[0]), __uint_as_float(rr[1]));
}
DI float swapsum(float m) {
  auto rr = __builtin_amdgcn_permlane32_swap(__float_as_uint(m), __float_as_uint(m), false, false);
  return __uint_as_float(rr[0]) + __uint_as_float(rr[1]);
}
#define MX3(a, b, c) __builtin_fmaxf(__builtin_fmaxf((a), (b)), (c))
DI float max16(const f32x16& p) {
  float a = MX3(p[0], p[1], p[2]), b = MX3(p[3], p[4], p[5]);
  a = MX3(a, p[6], p[7]); b = MX3(b, p[8], p[9]); a = MX3(a, p[10], p[11]); b = MX3(b, p[12], p[13]);
  a = MX3(a, p[14], p[15]);
  return __builtin_fmaxf(a, b);
}
DI int keyidx(int i) { return (i & 3) + 16 * ((i >> 2) & 1) + 8 * (i >> 3); }
DI void redo_tile(unsigned tile, bf16x8 q0, bf16x8 q1, bf16x8 sel, f32x16& o, f32x16& negm, float& l, u32x4& p0, u32x4& p1, f32x4& L) {
  int ln = threadIdx.x & 63; PIN(ln);
  const unsigned ka = (tile & ~3u) + ln * 16, mode = tile & 3u;
  const bf16x8 k0 = lds128(ka), k1 = lds128(ka + 1024);
  const int rr = (ln & 31) - 4 * (ln >> 5);
  f32x16 s;
#pragma unroll
  for (int i = 0; i < 16; ++i) s[i] = (mode != 0 && (keyidx(i) <= rr) == (mode == 2)) ? -__builtin_inff() : negm[0];
  s = MFMA(k0, q0, s); s = MFMA(k1, q1, s);
  const float d = __builtin_fmaxf(swapmax(max16(s)), 0.f), f = __builtin_amdgcn_exp2f(-d), nm = negm[0] - d;
  l *= f;
#pragma unroll
  for (int i = 0; i < 16; ++i) { negm[i] = nm; o[i] *= f; s[i] = __builtin_amdgcn_exp2f(s[i] - d); }
  p0 = pack8(s, 0); p1 = pack8(s, 1);
  f32x4 z = {};
  L = __builtin_amdgcn_mfma_f32_16x16x32_bf16(sel, asb(p0), z, 0, 0, 0);
  L = __builtin_amdgcn_mfma_f32_16x16x32_bf16(sel, asb(p1), L, 0, 0, 0);
  float t = L[0]; PIN(t); L[0] = t;
  PIN(p0); PIN(p1); PIN(o); PIN(negm);
  asm volatile("s_nop 1" ::: "memory");
}

typedef unsigned long long u64x8 __attribute__((ext_vector_type(8)));
__global__ __launch_bounds__(512, 2) void k_fused(const void* xA_, const void* wA_, const void* woA_, const void* xB_, const void* wB_, const void* woB_,
                                                  void* ws_, void* out_) {
  u64x8 ka; asm volatile("s_load_dwordx16 %0, %1, 0x0" : "=&s"(ka) : "s"(__builtin_amdgcn_kernarg_segment_ptr()) : "memory");
  __shared__ __attribute__((aligned(1024))) char smem[LDS_BYTES];
  const unsigned lds0 = (unsigned)(size_t)(LAS char*)smem;
  const unsigned bid = blockIdx.x, xcd = bid & 7, idx = bid >> 3;
  const unsigned br = xcd >> 2, R = RMAP, hp = HPMAP;
  const unsigned L = (br * 8 + R) * 16 + hp;
  const unsigned G = br * 8 + R, ns = hp, loc = hp >> 3;
  const unsigned tid = threadIdx.x, l = tid & 63, w = rfl(tid >> 6), wm = w & 3, wn = w >> 2;
  const unsigned mtw = w, mp = wn ? 3 - wm : wm;
  unsigned xv0 = (R * 256 + mtw * 32 + (l >> 3)) * 2048 + (((l & 7) ^ (l >> 4)) << 4);
  unsigned xv1 = (R * 256 + mtw * 32 + 8 + (l >> 3)) * 2048 + (((l & 7) ^ (4 + (l >> 4))) << 4);
  unsigned wv = (8 * w + (l >> 3)) * 6144 + (((l & 7) ^ ((((l >> 4) & 1) | ((w & 1) << 1)) << 1)) << 4) + hp * 128;
  asm volatile("s_waitcnt lgkmcnt(0)" : "+s"(ka), "+v"(xv0), "+v"(xv1), "+v"(wv) :: "memory");
  const void *xA = (const void*)ka[0], *wA = (const void*)ka[1], *woA = (const void*)ka[2], *xB = (const void*)ka[3], *wB = (const void*)ka[4], *woB = (const void*)ka[5];
  void *ws = (void*)ka[6], *out = (void*)ka[7];
  const __amdgpu_buffer_rsrc_t rx = mkrs(br ? xB : xA, NOMEMX * S_ * D_ * 2), rw = mkrs(br ? wB : wA, D_ * 3 * D_ * 2), rws = mkrs(ws, WS_END);
  STAMP(0);
  const unsigned xdst = lds0 + w * 4096, wdst = lds0 + LDS_W + w * 1024;
#define DMA_X(t, st) do { const unsigned so_ = (t) * 128u, d_ = xdst + (st); \
    dma16x(d_, xv0, rx, so_); dma16x(d_ + 1024, xv1, rx, so_); dma16x(d_ + 2048, xv0, rx, so_ + 32768); dma16x(d_ + 3072, xv1, rx, so_ + 32768); } while (0)
#define DMA_W(t, st) do { const unsigned so_ = (t) * (64u * 6144u), d_ = wdst + (st); \
    dma16w(d_, wv, rw, so_); dma16w(d_ + 8192, wv, rw, so_ + 2048); dma16w(d_ + 16384, wv, rw, so_ + 4096); } while (0)
  DMA_W(0, 0); DMA_X(0, 0);
  const unsigned fl_id = l < 32 ? G * 16 + (l >> 1) : (l == 32 ? L - 16 : L);
  const bool fl_ok = l < 34 && !(l == 32 && R == 0);
  unsigned obase = ld32_issue(rws, fl_ok ? WS_FLAG + fl_id * 128 : OOB);
  DMA_W(1, WST); DMA_X(1, XST);
  const unsigned pli = l < 13 ? l : 0;
  const unsigned pwl = ((idx & 3) * 8 + w) * 6 + (pli - 4);
  const unsigned pq = (idx * 8 + w) * 3 + (pli - 10);
  const char* pfp = pli < 4 ? (const char*)(br ? xB : xA) + (R * 256 + ((idx >> 2) * 8 + w) * 4 + pli) * 2048 + (PFD - 1) * 128
                  : pli < 10 ? (const char*)(br ? wB : wA) + (pwl / 3 + (PFD - 1) * 64) * 6144 + (pwl % 3) * 2048 + hp * 128
                             : (const char*)(br ? woB : woA) + (pq >> 3) * 2048 + loc * 1024 + (pq & 7) * 128;
  unsigned pstep = pli < 4 ? 128u : pli < 10 ? 64u * 6144u : (unsigned)((1023 - 95) / (16 - PFD) < 96 ? (1023 - 95) / (16 - PFD) : 96) * 2048u;
  pf4(lds0 + LDS_CNT + 256, pfp); pfp += pstep;
#define CODEPF(bytes) do { unsigned long long pc; asm volatile("s_getpc_b64 %0" : "=s"(pc));     \
    const unsigned co = (w * 64 + l) * 128; \
    pf4(lds0 + LDS_CNT + 256, (const char*)(pc & ~127ull) + (co < (bytes) ? co : 0)); } while (0)
  if (tid < 8) *(LAS unsigned*)(size_t)(lds0 + LDS_CNT + tid * 4) = 0;
  const unsigned c = l & 15, g = l >> 4, q4 = c >> 2;
  const unsigned xa = lds0 + mp * 8192 + c * 128 + ((g ^ (c >> 1)) << 4);
  const unsigned wa = lds0 + LDS_W + (8 * g + q4) * 128 + (((2 * wn) ^ ((q4 >> 1) | ((g & 1) << 1))) << 5) + (l & 3) * 8;
  f32x4 acc[6][4] = {};
  u32x4 mk0, mk1; unsigned sel1 = ((c & 3) == 0 && ((c >> 2) & 1) == (g & 1)) ? 0x3F803F80u : 0u;
  { const int rr = (int)(l & 31) - 4 * (int)(l >> 5);
#pragma unroll
    for (int i = 0; i < 4; ++i) {
      mk0[i] = (keyidx(2 * i) <= rr ? 0xFFFFu : 0u) | (keyidx(2 * i + 1) <= rr ? 0xFFFF0000u : 0u);
      mk1[i] = (keyidx(8 + 2 * i) <= rr ? 0xFFFFu : 0u) | (keyidx(9 + 2 * i) <= rr ? 0xFFFF0000u : 0u); }
    PIN(mk0); PIN(mk1); PIN(sel1); }
  f32x16 oa, ob; f32x4 Ls; u32x4 p0, p1; bf16x8 v0, v1, sel;
  Frag A, B;
#define RDX(F, xb, s, u) F.x[u] = lds128(((xb) ^ ((s) << 6)) + (u) * 2048)
#define MM(C, n, u) do { const bf16x8 wf_ = cat(C.wl[n], C.wh[n]); if ((n) < 4) MFA(acc[n][u], wf_, C.x[u]); else MFA(acc[n][u], C.x[u], wf_); } while (0)
#define RDWL(F, wb, s, n) F.wl[n] = ldstr(((wb) ^ (((n) & 1) << 5)) + ((n) >> 1) * 8192 + (s) * 4096)
#define RDWH(F, wb, s, n) F.wh[n] = ldstr(((wb) ^ (((n) & 1) << 5)) + ((n) >> 1) * 8192 + (s) * 4096 + 512)
#define RD1(F, xb, wb, s, r) do { if ((r) == 0) RDX(F, xb, s, 0); else if ((r) == 1) RDWL(F, wb, s, 0); else if ((r) == 2) RDWH(F, wb, s, 0); \
    else if ((r) < 6) RDX(F, xb, s, ((r) - 2) & 3); else if (((r) & 1) == 0) RDWL(F, wb, s, (((r) + 8) >> 1) % 6); else RDWH(F, wb, s, (((r) + 8) >> 1) % 6); } while (0)
#define PB 27
#define PFP 36
#define BP(r) ((r) * 3 / 2)
#define AP(r) (29 + (r) * 6 / 5)
#define XP(j) (2 + (j) * 6)
#define WP(j) (28 + (j) * 3)
#define TILE(LDA, DMA, WAITB) do { \
    const unsigned xb = xa + xs, wb = wa + wsx, wd = wdst + wsx; \
    xp = xs; xs = (xs == 2 * XST) ? 0 : xs + XST; wsx ^= WST; \
    const unsigned xn = xa + xs, wn_ = wa + wsx; \
    sfor<0, 48>([&](auto mi) AI { constexpr int m_ = decltype(mi)::v; \
      if constexpr (m_ < 24) MM(A, m_ >> 2, m_ & 3); else MM(B, (m_ - 24) >> 2, m_ & 3); \
      sfor<0, 16>([&](auto ri) AI { constexpr int r_ = decltype(ri)::v; \
        if constexpr (BP(r_) == m_) RD1(B, xb, wb, 1, r_); if constexpr (LDA && AP(r_) == m_) RD1(A, xn, wn_, 0, r_); }); \
      sfor<0, 4>([&](auto ji) AI { constexpr int j_ = decltype(ji)::v; \
        if constexpr (DMA && XP(j_) == m_) dma16x(xd + j_ * 1024, (j_ & 1) ? xv1 : xv0, rx, xso + (j_ >> 1) * 32768); \
        if constexpr (DMA && j_ < 3 && WP(j_) == m_) dma16w(wd + j_ * 8192, wv, rw, wso + j_ * 2048); }); \
      if constexpr (PFI && DMA && m_ == PFP) { pf4(lds0 + LDS_CNT + 256, pfp); pfp += pinc; } \
      if constexpr (!LDA) {     \
        if constexpr (m_ == 28) { oa = f32x16{}; PIN(oa); } if constexpr (m_ == 34) { ob = f32x16{}; PIN(ob); } \
        if constexpr (m_ == 40) { p0 = u32x4{}; p1 = u32x4{}; v0 = bf16x8{}; v1 = bf16x8{}; PIN(p0); PIN(p1); PIN(v0); PIN(v1); } \
        if constexpr (m_ == 44) { Ls = f32x4{}; sel = asb(u32x4{sel1, sel1, sel1, sel1}); PIN(Ls); PIN(sel); } } \
      SBAR(); \
      if constexpr (m_ == PB) { WAITB; SBAR(); } }); } while (0)
  WAIT_BAR_V(8, obase);
  STAMP(1);
  unsigned xs = 0, wsx = 0, xp = 2 * XST;
  sfor<0, 16>([&](auto ri) AI { constexpr int r_ = decltype(ri)::v; RD1(A, xa, wa, 0, r_); SBAR(); });
  if (w == 0) st32(obase ^ ACKX, rws, (fl_ok && l <= 32 && !(l & 1)) ? WS_ACK + fl_id * 128 + (l < 32 ? ns : 16) * 4 : OOB);
  const unsigned pbase = __builtin_amdgcn_readlane(obase, 32), mybase = __builtin_amdgcn_readlane(obase, 33);
  _Pragma("unroll") for (int i_ = 0; i_ < 24; ++i_) PIN(acc[i_ >> 2][i_ & 3]);
  asm volatile("s_nop 1" ::: "memory");
  SBAR();
  const bool ack_ok = l < 17 && !(l == 16 && R == 7);
  const unsigned ackoff = ack_ok ? WS_ACK + L * 128 + l * 4 : OOB;
  const char* codep;
  { unsigned long long pc; asm volatile("s_getpc_b64 %0" : "=s"(pc)); const unsigned co = (w * 64 + l) * 128; codep = (const char*)(pc & ~127ull) + (co < CODE_BYTES ? co : 0); }
#pragma unroll 1
  for (int t = 0; t < 15; ++t) {
    const unsigned xso = t < 14 ? (t + 2) * 128u : 0x80000000u, wso = t < 14 ? (t + 2) * (64u * 6144u) : OOB;
    const unsigned xd = t < 14 ? xdst + xp : lds0 + LDS_SPARE;
    if (__builtin_expect((unsigned)(t - (15 - PFD)) < 2u, 0)) {
      if (t == 15 - PFD) {
        dma4_sc1(lds0 + LDS_CTL + w * 256, ackoff, rws, 0);
        pf4(lds0 + LDS_CNT + 256, codep);
      } else {
        pfp = (const char*)(br ? woB : woA) + ((idx * 8 + w) * 4 + ((l >> 3) & 3)) * 2048 + loc * 1024 + (l & 7) * 128; pstep = 0;
      }
    }
    const unsigned pinc = pstep;
    TILE(true, true, WAIT_BAR(5));
  }
  bool acked = __all(!ack_ok || *(volatile LAS unsigned*)(size_t)(lds0 + LDS_CTL + w * 256 + l * 4) == (mybase ^ ACKX));
  __builtin_amdgcn_s_setprio(2);
  { const unsigned xso = 0, wso = 0, xd = 0, pinc = 0; TILE(false, false, (void)0); }
  asm volatile("s_nop 7" ::: "memory");
  _Pragma("unroll") for (int i_ = 0; i_ < 24; ++i_) PIN(acc[i_ >> 2][i_ & 3]);
  STAMP(2);
  bf16x8 qa0, qa1, qb0, qb1;
  f32x16 sa, sb, na, nb;
  {
    const unsigned ko = (g & 1) * 1024 + ((g >> 1) * 32 + (c & 7) + 16 * (c >> 3)) * 16;
    const unsigned vo = IMG + (g >> 1) * 1024 + ((g & 1) * 32 + c) * 16;
    const unsigned t0 = wn * 32768 + mp * 2 * TIL;
    const unsigned gs = R < 7 ? WS_KV + L * 65536 + t0 : OOB;
#pragma unroll
    for (int u = 0; u < 4; ++u) {
      const unsigned tb = (u >> 1) * TIL;
      const u32x4 kk = pack44(acc[2][u], acc[3][u]), vv = pack44(acc[4 + (u & 1)][u & 2], acc[4 + (u & 1)][(u & 2) + 1]);
      __builtin_amdgcn_raw_buffer_store_b128(kk, rws, ko + (u & 1) * 128 + tb, gs, SC1);
      __builtin_amdgcn_raw_buffer_store_b128(vv, rws, vo + (u & 1) * 256 + tb, gs, SC1);
      sts128(lds0 + LDS_OWN + t0 + tb + ko + (u & 1) * 128, kk);
      sts128(lds0 + LDS_OWN + t0 + tb + vo + (u & 1) * 256, vv);
    }
    lds_inc(lds0 + LDS_CNT + wn * 4);
    if (mp < 2) __builtin_amdgcn_s_setprio(1);
    u32x4 qf[4]; float sd[4];
#pragma unroll
    for (int u = 0; u < 4; ++u) {
      const f32x4 qx = acc[0][u] * QSCALE, qy = acc[1][u] * QSCALE;
      qf[u] = pack44(qx, qy);
      float d = qx[0] * acc[2][u][0];
#pragma unroll
      for (int i = 1; i < 4; ++i) d = __builtin_fmaf(qx[i], acc[2][u][i], d);
#pragma unroll
      for (int i = 0; i < 4; ++i) d = __builtin_fmaf(qy[i], acc[3][u][i], d);
      sd[u] = d;
    }
#pragma unroll
    for (int p = 0; p < 2; ++p) {
#pragma unroll
      for (int i = 0; i < 4; ++i) { auto r = __builtin_amdgcn_permlane16_swap(qf[2 * p][i], qf[2 * p + 1][i], false, false); qf[2 * p][i] = r[0]; qf[2 * p + 1][i] = r[1]; }
      auto r = __builtin_amdgcn_permlane16_swap(__float_as_uint(sd[2 * p]), __float_as_uint(sd[2 * p + 1]), false, false);
      sd[2 * p] = -swapsum(__uint_as_float(r[0]) + __uint_as_float(r[1]));
    }
    qb0 = asb(qf[0]); qb1 = asb(qf[1]); qa0 = asb(qf[2]); qa1 = asb(qf[3]);
    { const float one = l < 32 ? 1.f : 0.f;
      asm volatile("s_nop 1\n\tv_mfma_f32_32x32x2_f32 %0, %2, %3, 0\n\tv_mfma_f32_32x32x2_f32 %1, %2, %4, 0" : "=&v"(na), "=&v"(nb) : "v"(one), "v"(sd[2]), "v"(sd[0])); }
  }
  float la = 0.f, lb = 0.f;
  bf16x8 k0, k1;
  PIN(qa0); PIN(qa1); PIN(qb0); PIN(qb1);
  STAMP(3);
  const __amdgpu_buffer_rsrc_t rwo = mkrs(br ? woB : woA, D_ * D_ * 2);
  const unsigned wov = (l >> 3) * 2048 + (((l & 7) ^ (((l >> 4) & 3) << 1)) << 4) + ns * 128;
#define WOB(gq) ((gq) == 0 ? 131072u : ((gq) & 3) == 3 ? 0u : (gq) * 32768u)
#define DMA_WO(j, ww) dma16(lds0 + WOB((j) >> 2) + ((j) & 3) * 8192 + (ww) * 1024, wov, rwo, ((j) ^ (loc * 8)) * (64u * 2048u) + (ww) * 16384u)
  const unsigned lb16 = l * 16, own = lds0 + LDS_OWN + wn * 32768, prv = R > 0 ? lds0 + wn * 98304 : own;
  const unsigned myflag = WS_FLAG + L * 128, pfoff = WS_FLAG + (L - 16) * 128 + 4 + wn * 4;
#define QK0(S, Q, N) asm volatile("v_mfma_f32_32x32x16_bf16 %0, %1, %2, %3" : "=&v"(S) : "v"(k0), "v"(Q), "v"(N), "v"(k1))
#define QK1(S, Q) asm volatile("v_mfma_f32_32x32x16_bf16 %0, %1, %2, %0" : "+v"(S) : "v"(k1), "v"(Q))
#define PV0(O) asm volatile("v_mfma_f32_32x32x16_bf16 %0, %1, %2, %0" : "+v"(O) : "v"(v0), "v"(p0), "v"(v1))
#define PV1(O) asm volatile("v_mfma_f32_32x32x16_bf16 %0, %1, %2, %0" : "+v"(O) : "v"(v1), "v"(p1))
#define EX(S, i) do { S[i] = __builtin_amdgcn_exp2f(S[i]); } while (0)
#define RDK(a) do { k0 = lds128(a); k1 = lds128((a) + 1024); } while (0)
  unsigned pa = own + (2 * mp + 1) * TIL, pb = pa - TIL, pbp = pb;
  unsigned va = lb16 + pa, vb = lb16 + pb;
  RDK(va);
  asm volatile("s_nop 1" ::: "memory");
  QK0(sa, qa0, na); QK1(sa, qa1);
  RDK(vb);
  asm volatile("s_nop 7\n\ts_nop 1" ::: "memory");
  SBAR();
#define EXP(i) e_[i] = __builtin_amdgcn_exp2f(sx_[i])
#define HALF(SX, SY, NY, LY, OY, QY0, QY1, PY, RD, VX, KNEXT, BAR1, MSK, LSUM) do { \
    const f32x16 sx_ = SX; float e_[16]; u32x4 pn_; \
    EXP(0); EXP(1); EXP(2); EXP(3); EXP(4); asm volatile("" : "+v"(e_[0]), "+v"(e_[1]), "+v"(e_[2]), "+v"(e_[3]), "+v"(e_[4])); SBAR();     \
    EXP(5); asm volatile("" : "+v"(e_[5]), "+v"(Ls)); SBAR(); \
    unsigned long long bad_ = __builtin_amdgcn_ballot_w64(!(Ls[0] <= PBOUND)); asm volatile("" : "+s"(bad_)); SBAR(); \
    pn_[0] = pk2(e_[0], e_[1]); pn_[1] = pk2(e_[2], e_[3]); pn_[2] = pk2(e_[4], e_[5]); EXP(6); \
    asm volatile("" : "+v"(pn_[0]), "+v"(pn_[1]), "+v"(pn_[2]), "+v"(e_[6])); SBAR(); \
    if (__builtin_expect(bad_ != 0, 0)) redo_tile(PY, QY0, QY1, sel, OY, NY, LY, p0, p1, Ls); \
    PV0(OY); SBAR(); \
    EXP(7); EXP(8); LY += Ls[0]; pn_[3] = pk2(e_[6], e_[7]); asm volatile("" : "+v"(pn_[3]), "+v"(e_[8]), "+v"(LY)); SBAR(); \
    PV1(OY); SBAR(); if (RD) { v0 = lds128((VX) + IMG); v1 = lds128((VX) + IMG + 1024); VX = KNEXT; } \
    EXP(9); EXP(10); asm volatile("" : "+v"(e_[9]), "+v"(e_[10]), "+v"(VX)); SBAR(); \
    QK0(SY, QY0, NY); SBAR(); EXP(11); EXP(12); EXP(13); asm volatile("" : "+v"(e_[11]), "+v"(e_[12]), "+v"(e_[13])); SBAR(); \
    QK1(SY, QY1); SBAR(); \
    EXP(14); EXP(15); asm volatile("" : "+v"(e_[14]), "+v"(e_[15])); SBAR(); \
    if (BAR1) { if (PFI) WAITV(9); else WAITV(8); lds_inc(lds0 + LDS_CNT + 8); if (mp) lds_wait(lds0 + LDS_CNT + wn * 4, 4); } \
    if (RD) RDK(VX); SBAR(); \
    p0 = pn_; p1[0] = pk2(e_[8], e_[9]); p1[1] = pk2(e_[10], e_[11]); p1[2] = pk2(e_[12], e_[13]); p1[3] = pk2(e_[14], e_[15]); SBAR(); \
    if (MSK) { p0 &= mk0; p1 &= mk1; PIN(p0); PIN(p1); asm volatile("s_nop 0"); } \
    LSUM; SBAR(); } while (0)
#define LMF0 "v_mfma_f32_16x16x32_bf16 %0, %1, %2, 0\n\t"
#define LMF1 "v_mfma_f32_16x16x32_bf16 %0, %1, %3, %0\n\t"
#define LSUM_G() asm volatile(LMF0 "s_nop 0\n\t" LMF1 "s_nop 1" : "=&v"(Ls) : "v"(sel), "v"(p0), "v"(p1))
#define LSUM_A() asm volatile(LMF0 "s_nop 0\n\t" LMF1 "s_nop 0" : "=&v"(Ls) : "v"(sel), "v"(p0), "v"(p1))
#define LSUM_B() asm volatile("v_mfma_f32_16x16x32_bf16 %0, %3, %4, 0\n\ts_mov_b32 %1, %2\n\tv_mfma_f32_16x16x32_bf16 %0, %3, %5, %0\n\ts_add_i32 %2, %2, 0x1000" : "=&v"(Ls), "=&s"(pbp), "+s"(pa) : "v"(sel), "v"(p0), "v"(p1) : "scc")
  unsigned sent = 0;

#define REPF() do {} while (0)
#define SIGNAL() do { WAITV(0); \
    _Pragma("unroll 1") for (unsigned sp = 0; !acked && sp < SPIN_CAP; ++sp) { __builtin_amdgcn_s_sleep(2); acked = __all(!ack_ok || ld32_wait(rws, ackoff) == (mybase ^ ACKX)); } \
    if (lds_inc_rtn(lds0 + LDS_CNT + 24 + wn * 4) == 3 && l == 0) st32(mybase + 1, rws, myflag + 4 + wn * 4); \
    REPF(); \
    sent = 1; if (mp >= 2) __builtin_amdgcn_s_setprio(0); STAMP(13); } while (0)
#define TRYSIG() do { if (__builtin_expect(!sent, 0)) { if ((__builtin_amdgcn_s_getreg(7 | (31 << 11)) & 0x00C0000Fu) == 0) SIGNAL(); } } while (0)
#define PAIR(PAN, PBN, MD, FIRST) do { const unsigned pan = (PAN), pbn = (PBN); \
      HALF(sa, sb, nb, lb, ob, qb0, qb1, pbp, true, va, lb16 + pan, FIRST, (MD) != 0, LSUM_G()); \
      HALF(sb, sa, na, la, oa, qa0, qa1, pa | (MD), true, vb, lb16 + pbn, false, (MD) != 0, LSUM_G()); \
      pbp = pb | (MD); pa = pan; pb = pbn; } while (0)
#define HOT(N, SIG) do { _Pragma("unroll 1") for (int n = (N); n > 0; --n) {     \
      if (SIG) TRYSIG(); \
      HALF(sa, sb, nb, lb, ob, qb0, qb1, pbp, true, va, va + TIL, false, false, LSUM_A()); \
      HALF(sb, sa, na, la, oa, qa0, qa1, pa, false, vb, 0, false, false, LSUM_B()); } } while (0)
  PAIR(own, own, 1u, true);
  HOT(2 * mp, true);
  {
    STAMP(4);
    if (!sent) SIGNAL();
    STAMP(5);
    mk0 = ~mk0; mk1 = ~mk1; PIN(mk0); PIN(mk1);
    if (mp < 2) {
      lds_wait(lds0 + LDS_CNT + 8, 8);
      unsigned h_ = wn; asm volatile("" : "+s"(h_));
      if (mp) {
#pragma unroll
        for (int j = 0; j < 15; ++j) DMA_WO(j >> 2, h_ * 4 + (j & 3));
        if (!h_) DMA_WO(3, 3);
      } else if (R > 0) {
#pragma unroll 1
        for (unsigned sp = 0; sp < SPIN_CAP; ++sp) {
          if (rfl(ld32_wait(rws, pfoff)) == pbase + 1) break;
          __builtin_amdgcn_s_sleep(KVS);
        }
        STAMP(14);
#pragma unroll
        for (int j = 0; j < 8; ++j) dma64_sc1(lds0 + h_ * 98304 + j * TIL, lb16, rws, WS_KV + (L - 16) * 65536 + h_ * 32768 + j * TIL);
        WAITV(12);
        lds_inc(lds0 + LDS_CNT + 12 + h_ * 4);
      }
    }
    STAMP(6);
    if (R > 0) lds_wait(lds0 + LDS_CNT + 12 + wn * 4, mp < 2 ? 1 : 2);
    STAMP(7);
    pb = prv + (2 * mp + 1) * TIL; vb = lb16 + pb;
    RDK(vb);
    SBAR();
  }
  PAIR(prv + (2 * mp + 1) * TIL, prv + 2 * mp * TIL, 0u, false);
  lds_inc(lds0 + LDS_CNT + 20);
  if (R > 0) {
    if (mp == 0) { WAITV(0); lds_inc(lds0 + LDS_CNT + 12 + wn * 4); }
    PAIR(prv + (2 * mp + 2) * TIL, prv + (2 * mp + 2) * TIL, 2u, false);
    if (mp == 1) lds_wait(lds0 + LDS_CNT + 12 + wn * 4, 2);
    HOT(6 - 2 * mp, false);
    asm volatile("s_nop 7" : "+v"(Ls) :: "memory");
    if (__builtin_expect(__any(!(Ls[0] <= PBOUND)), 0)) redo_tile(pbp, qb0, qb1, sel, ob, nb, lb, p0, p1, Ls);
    lb += Ls[0];
    PV0(ob); PV1(ob);
  } else {
    asm volatile("s_nop 7" : "+v"(Ls), "+v"(sa) :: "memory");
  }
  __builtin_amdgcn_s_setprio(0);
  STAMP(8);
  { const float ia = __builtin_amdgcn_rcpf(la), ib = __builtin_amdgcn_rcpf(lb);
    oa *= ia;
    const unsigned hh = hp * 2 + wn, T0 = br * 64 + R * 8 + 2 * mp;
    const unsigned ol = ((l >> 4) & 1) * 1024 + ((l >> 5) * 16 + (l & 15)) * 16;
    __builtin_amdgcn_raw_buffer_store_b128(pack8(oa, 0), rws, ol, WS_O + (T0 * 32 + 32 + hh) * IMG, SC1);
    __builtin_amdgcn_raw_buffer_store_b128(pack8(oa, 1), rws, ol + 512, WS_O + (T0 * 32 + 32 + hh) * IMG, SC1);
    SBAR();
    asm volatile("s_nop 7" : "+v"(ob) :: "memory");
    ob *= ib;
    __builtin_amdgcn_raw_buffer_store_b128(pack8(ob, 0), rws, ol, WS_O + (T0 * 32 + hh) * IMG, SC1);
    __builtin_amdgcn_raw_buffer_store_b128(pack8(ob, 1), rws, ol + 512, WS_O + (T0 * 32 + hh) * IMG, SC1); }
  WAITV(0);
  STAMP(9);
  asm volatile("s_barrier" ::: "memory");
  if (tid == 64) st32(mybase + 2, rws, myflag);
  dma4(lds0 + LDS_WOX + 1024, l < 32 ? (l >> 4) * 65536 + (l & 15) * 128 : OOB, rws, WS_O + ((br * 64 + R * 8 + 2 * mp) * 32 + hp * 2 + wn) * IMG);
  if (w == 7) DMA_WO(3, 7);
  const unsigned fo = l < 32 ? WS_FLAG + (G * 16 + (l >> 1)) * 128 : OOB;
#pragma unroll
  for (int j = 4; j < 16; ++j) DMA_WO(j, w);
  if (w == POLLW) {
    unsigned f;
#pragma unroll 1
    for (unsigned sp = 0; sp < SPIN_CAP; ++sp) {
      f = ld32_wait(rws, fo);
      if (__all((l >> 4) != loc || f == obase + 2)) break;
      __builtin_amdgcn_s_sleep(POLLS);
    }
    *(volatile LAS unsigned*)(size_t)(lds0 + LDS_WOX) = __all(l >= 32 || f == obase + 2);
  }
  WAIT_BAR(0);
  STAMP(10);
  const unsigned T = G * 8 + w;
  const unsigned obs = WS_O + T * 32 * IMG;
  const unsigned wa2 = lds0 + (16 * (g >> 1) + 4 * (g & 1) + q4) * 128 + ((2 * (g & 1) + (q4 >> 1)) << 5) + (l & 3) * 8;
  f32x4 oc[4][2] = {};
  u32x4 of[16];
  s16x4 wl[2][4], wh[2][4];
#pragma unroll
  for (int i = 0; i < 16; ++i) {
    of[i] = __builtin_amdgcn_raw_buffer_load_b128(rws, l * 16 + (i & 3) * 1024, obs + loc * 32768 + (i >> 2) * 4096, SC1);
    if (i == FILL0 - 1 || i == FILL1 - 1) { SBAR(); __builtin_amdgcn_s_sleep(FILLS); SBAR(); }
  }
  const unsigned allrdy = rfl(*(volatile LAS unsigned*)(size_t)(lds0 + LDS_WOX));
#pragma unroll
  for (int nt = 0; nt < 4; ++nt) { wl[0][nt] = ldstr((wa2 ^ (nt << 5)) + WOB(0)); wh[0][nt] = ldstr((wa2 ^ (nt << 5)) + WOB(0) + 1024); }
  _Pragma("unroll") for (int i_ = 0; i_ < 8; ++i_) PIN(oc[i_ >> 1][i_ & 1]);
  asm volatile("s_nop 1" ::: "memory");
  SBAR();
#pragma unroll 1
  for (int gq = 0; gq < 4; ++gq) {
    const unsigned wb = wa2 + WOB(gq), wbn = wa2 + WOB(gq + 1);
    if (__builtin_expect(gq == 1 && !allrdy, 0)) {
      if (w == POLLW) {
#pragma unroll 1
        for (unsigned sp = 0; sp < SPIN_CAP; ++sp) { if (__all(l >= 32 || ld32_wait(rws, fo) == obase + 2)) break; __builtin_amdgcn_s_sleep(POLLS); }
      }
      asm volatile("s_barrier" ::: "memory");
    }
    const unsigned on = gq < 3 ? obs + ((gq + 1) ^ (loc * 2)) * 16384 : OOB;
    if (gq == 0) __builtin_amdgcn_s_setprio(3); else if (gq == 1) __builtin_amdgcn_s_setprio(2); else if (gq == 2) __builtin_amdgcn_s_setprio(1); else __builtin_amdgcn_s_setprio(0);
    sfor<0, 8>([&](auto hi) AI { constexpr int h_ = decltype(hi)::v, cur = h_ & 1, nxt = cur ^ 1;
      sfor<0, 8>([&](auto mi) AI { constexpr int m_ = decltype(mi)::v, nt = m_ >> 1, tt = m_ & 1;
        { const bf16x8 wf = cat(wl[cur][nt], wh[cur][nt]);
          if constexpr (m_ == 0) {
            const bf16x8 w3 = cat(wl[cur][3], wh[cur][3]);
            asm volatile("v_mfma_f32_16x16x32_bf16 %0, %1, %2, %0" : "+v"(oc[0][0]) : "v"(wf), "v"(of[2 * h_]), "v"(w3), "v"(of[2 * h_ + 1]));
          } else MFA(oc[nt][tt], wf, of[2 * h_ + tt]); }
        sfor<0, 8>([&](auto ri) AI { constexpr int r_ = decltype(ri)::v, rn = r_ >> 1;
          if constexpr (G2R(r_) == m_) {
            const unsigned wq_ = ((h_ < 7 ? wb : wbn) ^ (rn << 5)) + ((h_ + 1) & 7) * 4096;
            if constexpr ((r_ & 1) == 0) wl[nxt][rn] = ldstr(wq_); else wh[nxt][rn] = ldstr(wq_ + 1024); } });
        if constexpr (m_ >= 6) of[2 * h_ + tt] = __builtin_amdgcn_raw_buffer_load_b128(rws, l * 16 + ((2 * h_ + tt) & 3) * 1024, on + ((2 * h_ + tt) >> 2) * 4096, SC1);
        SBAR(); }); });
  }
  asm volatile("s_nop 7" ::: "memory");
  _Pragma("unroll") for (int i_ = 0; i_ < 8; ++i_) PIN(oc[i_ >> 1][i_ & 1]);
  STAMP(11);
  const __amdgpu_buffer_rsrc_t rout = mkrs(out, S_ * 2 * D_ * 2);
  const unsigned orow = ((R * 8 + w) * 32 + c) * 4096 + br * 2048 + ns * 128 + (g & 1) * 32 + (g >> 1) * 16;
  const unsigned orow2 = orow - (c & 8) * 4096 + (c & 8) * 8;
#pragma unroll
  for (int tt = 0; tt < 2; ++tt) {
    u32x4 v[2], va_, vb_;
#pragma unroll
    for (int np = 0; np < 2; ++np) {
      const f32x4 &a = oc[2 * np][tt], &b = oc[2 * np + 1][tt];
      auto r0 = __builtin_amdgcn_permlane16_swap(pk2(a[0], a[1]), pk2(b[0], b[1]), false, false);
      auto r1 = __builtin_amdgcn_permlane16_swap(pk2(a[2], a[3]), pk2(b[2], b[3]), false, false);
      v[np] = u32x4{r0[0], r1[0], r0[1], r1[1]};
    }
#pragma unroll
    for (int i = 0; i < 4; ++i) {
      const unsigned y = c < 8 ? v[1][i] : v[0][i];
      const unsigned r = (unsigned)__builtin_amdgcn_update_dpp(0, (int)y, 0x128, 0xf, 0xf, false);
      va_[i] = c < 8 ? v[0][i] : r; vb_[i] = c < 8 ? r : v[1][i];
    }
    __builtin_amdgcn_raw_buffer_store_b128(va_, rout, orow2 + tt * 65536, 0, OUTPOL);
    __builtin_amdgcn_raw_buffer_store_b128(vb_, rout, orow2 + tt * 65536 + 32768, 0, OUTPOL);
  }
}

extern "C" void kernel_launch(void* const* d_in, const int* in_sizes, int n_in, void* d_out, int out_size, void* d_ws, size_t ws_size,
                              hipStream_t stream) {
  const size_t wsoff = (ws_size - WS_END) & ~(size_t)0xfffff;
  k_fused<<<256, 512, 0, stream>>>(d_in[0], d_in[1], d_in[2], d_in[3], d_in[4], d_in[5], (char*)d_ws + wsoff, d_out);
}
```

```cpp
#define POLLS 8
#include <hip/hip_runtime.h>
#include <hip/hip_bf16.h>

typedef short bf16x8 __attribute__((ext_vector_type(8)));
typedef short s16x4 __attribute__((ext_vector_type(4)));
typedef float f32x16 __attribute__((ext_vector_type(16)));
typedef float f32x4 __attribute__((ext_vector_type(4)));
typedef unsigned u32x4 __attribute__((ext_vector_type(4)));
typedef __bf16 bf16x2_t __attribute__((ext_vector_type(2)));
typedef float f32x2 __attribute__((ext_vector_type(2)));
#define LAS __attribute__((address_space(3)))
#define DI __device__ __forceinline__
#define AI __attribute__((always_inline))
#define MFMA(a, b, c) __builtin_amdgcn_mfma_f32_32x32x16_bf16((a), (b), (c), 0, 0, 0)
#define SBAR() __builtin_amdgcn_sched_barrier(0)
#define PIN(x) asm volatile("" : "+v"(x))
#define MFA(acc_, a_, b_) asm volatile("v_mfma_f32_16x16x32_bf16 %0, %1, %2, %0" : "+v"(acc_) : "v"(a_), "v"(b_))

constexpr int S_ = 2048, D_ = 1024;
constexpr float QSCALE = 0.17677669529663687f * 1.4426950408889634f;
constexpr unsigned SUM_LO = 0x2B800000u, SUM_HI = 0x53800000u;
constexpr unsigned IMG = 2048;
constexpr unsigned WS_FLAG = 0, WS_ACK = 32768, WS_KV = 65536, WS_O = WS_KV + 256u * 65536u, WS_END = WS_O + 2u * 64u * 32u * IMG;
constexpr unsigned ACKX = 0x5bd1e995u;
constexpr int SC1 = 16;
constexpr unsigned CODE_BYTES = 25600;
constexpr unsigned SPIN_CAP = 1u << 18;

#define HPMAP ((xcd & 1) * 8 + (idx >> 2))
#define RMAP (((xcd >> 1) & 1) * 4 + (idx & 3))
#define NOMEMX 1
template <int I> struct IC { static constexpr int v = I; };
template <int B, int E, class F> DI void sfor(F&& f) { if constexpr (B < E) { f(IC<B>{}); sfor<B + 1, E>(f); } }
DI unsigned rfl(unsigned x) { return (unsigned)__builtin_amdgcn_readfirstlane((int)x); }
DI unsigned pk2(float lo, float hi) { f32x2 v = {lo, hi}; return __builtin_bit_cast(unsigned, __builtin_convertvector(v, bf16x2_t)); }
DI u32x4 pack8(const f32x16& x, int s) {
  u32x4 p;
  p[0] = pk2(x[8 * s + 0], x[8 * s + 1]); p[1] = pk2(x[8 * s + 2], x[8 * s + 3]);
  p[2] = pk2(x[8 * s + 4], x[8 * s + 5]); p[3] = pk2(x[8 * s + 6], x[8 * s + 7]);
  return p;
}
DI u32x4 pack44(const f32x4& a, const f32x4& b) { u32x4 p; p[0] = pk2(a[0], a[1]); p[1] = pk2(a[2], a[3]); p[2] = pk2(b[0], b[1]); p[3] = pk2(b[2], b[3]); return p; }
DI bf16x8 asb(u32x4 v) { return __builtin_bit_cast(bf16x8, v); }
DI bf16x8 lds128(unsigned a) { return *(const LAS bf16x8*)(size_t)a; }
DI void sts128(unsigned a, u32x4 v) { *(LAS u32x4*)(size_t)a = v; }
DI s16x4 ldstr(unsigned a) { return __builtin_amdgcn_ds_read_tr16_b64_v4i16((LAS s16x4*)(size_t)a); }
DI bf16x8 cat(s16x4 lo, s16x4 hi) { return __builtin_shufflevector(lo, hi, 0, 1, 2, 3, 4, 5, 6, 7); }
DI void dma16(unsigned ldsdst, unsigned voff, __amdgpu_buffer_rsrc_t rs, unsigned soff) {
  asm volatile("s_mov_b32 m0, %0\n\ts_nop 0\n\tbuffer_load_dwordx4 %1, %2, %3 offen lds" ::"s"(ldsdst), "v"(voff), "s"(rs), "s"(soff) : "memory");
}
#define XPOL ""
#define WPOL ""
DI void dma16x(unsigned ldsdst, unsigned voff, __amdgpu_buffer_rsrc_t rs, unsigned soff) {
  asm volatile("s_mov_b32 m0, %0\n\ts_nop 0\n\tbuffer_load_dwordx4 %1, %2, %3 offen " XPOL " lds" ::"s"(ldsdst), "v"(voff), "s"(rs), "s"(soff) : "memory");
}
DI void dma16w(unsigned ldsdst, unsigned voff, __amdgpu_buffer_rsrc_t rs, unsigned soff) {
  asm volatile("s_mov_b32 m0, %0\n\ts_nop 0\n\tbuffer_load_dwordx4 %1, %2, %3 offen " WPOL " lds" ::"s"(ldsdst), "v"(voff), "s"(rs), "s"(soff) : "memory");
}
#define PPOL ""
#define OUTPOL 16
#define KVS 1
#define FILL0 4
#define FILL1 8
#define FILLS 4
#define G2R(r) ((r) * 3 / 4)
#define POLLW 0
#define PFMODE 0
#define PFI true
#define PFD 3
#define PFH 1
DI void pf4(unsigned ldsdst, const char* p) {
  asm volatile("s_mov_b32 m0, %0\n\ts_nop 0\n\tglobal_load_lds_dword %1, off " PPOL ::"s"(ldsdst), "v"(p) : "memory");
}
DI void dma16_sc1(unsigned ldsdst, unsigned voff, __amdgpu_buffer_rsrc_t rs, unsigned soff) {
  asm volatile("s_mov_b32 m0, %0\n\ts_nop 0\n\tbuffer_load_dwordx4 %1, %2, %3 offen sc1 lds" ::"s"(ldsdst), "v"(voff), "s"(rs), "s"(soff) : "memory");
}
#define WAITV(n) asm volatile("s_waitcnt vmcnt(" #n ")" ::: "memory")
#define WAIT_BAR(n) asm volatile("s_waitcnt vmcnt(" #n ") lgkmcnt(0)\n\ts_barrier" ::: "memory")
DI __amdgpu_buffer_rsrc_t mkrs(const void* p, unsigned bytes) { return __builtin_amdgcn_make_buffer_rsrc((void*)p, (short)0, (int)bytes, 0x00020000); }
DI unsigned ld32_issue(__amdgpu_buffer_rsrc_t rs, unsigned off) { unsigned r; asm volatile("buffer_load_dword %0, %1, %2, 0 offen sc1" : "=v"(r) : "v"(off), "s"(rs) : "memory"); return r; }
DI unsigned ld32_wait(__amdgpu_buffer_rsrc_t rs, unsigned off) { unsigned r; asm volatile("buffer_load_dword %0, %1, %2, 0 offen sc1\n\ts_waitcnt vmcnt(0)" : "=&v"(r) : "v"(off), "s"(rs) : "memory"); return r; }
DI u32x4 ld128_issue(__amdgpu_buffer_rsrc_t rs, unsigned off) { u32x4 r; asm volatile("buffer_load_dwordx4 %0, %1, %2, 0 offen sc1" : "=v"(r) : "v"(off), "s"(rs) : "memory"); return r; }
DI u32x4 ld128_wait(__amdgpu_buffer_rsrc_t rs, unsigned off) { u32x4 r; asm volatile("buffer_load_dwordx4 %0, %1, %2, 0 offen sc1\n\ts_waitcnt vmcnt(0)" : "=&v"(r) : "v"(off), "s"(rs) : "memory"); return r; }
DI void dma4(unsigned ldsdst, unsigned voff, __amdgpu_buffer_rsrc_t rs, unsigned soff) {
  asm volatile("s_mov_b32 m0, %0\n\ts_nop 0\n\tbuffer_load_dword %1, %2, %3 offen lds" ::"s"(ldsdst), "v"(voff), "s"(rs), "s"(soff) : "memory");
}
DI void dma4_sc1(unsigned ldsdst, unsigned voff, __amdgpu_buffer_rsrc_t rs, unsigned soff) {
  asm volatile("s_mov_b32 m0, %0\n\ts_nop 0\n\tbuffer_load_dword %1, %2, %3 offen sc1 lds" ::"s"(ldsdst), "v"(voff), "s"(rs), "s"(soff) : "memory");
}
#define WAIT_BAR_V(n, x) asm volatile("s_waitcnt vmcnt(" #n ") lgkmcnt(0)\n\ts_barrier" : "+v"(x) :: "memory")
constexpr unsigned OOB = 0x80000000u;
DI void st32(unsigned v, __amdgpu_buffer_rsrc_t rs, unsigned off) { __builtin_amdgcn_raw_buffer_store_b32(v, rs, off, 0, SC1); }

#define STAMP(i) do {} while (0)

constexpr unsigned XST = 32768, WST = 24576, LDS_W = 3 * XST, LDS_SPARE = LDS_W + 2 * WST, LDS_CTL = 159744;

constexpr unsigned LDS_OWN = 32768, LDS_CNT = 162816, LDS_WOX = 98304, LDS_BYTES = 163840, TIL = 2 * IMG;

struct Frag { bf16x8 x[4]; s16x4 wl[6], wh[6]; };

DI void lds_inc(unsigned a) { asm volatile("s_mov_b64 exec, 1\n\tds_add_u32 %0, %1\n\ts_mov_b64 exec, -1" ::"v"(a), "v"(1u) : "memory"); }
DI unsigned lds_inc_rtn(unsigned a) { unsigned r; asm volatile("s_mov_b64 exec, 1\n\tds_add_rtn_u32 %0, %1, %2\n\ts_mov_b64 exec, -1\n\ts_waitcnt lgkmcnt(0)" : "=&v"(r) : "v"(a), "v"(1u) : "memory"); return rfl(r); }
DI void lds_wait(unsigned a, unsigned n) {
#pragma unroll 1
  for (unsigned sp = 0; sp < 32u * SPIN_CAP && rfl(*(volatile LAS unsigned*)(size_t)a) < n; ++sp) __builtin_amdgcn_s_sleep(1);
  asm volatile("" ::: "memory");
}
DI void dma64_sc1(unsigned ldsdst, unsigned voff, __amdgpu_buffer_rsrc_t rs, unsigned soff) {
  asm volatile("s_mov_b32 m0, %0\n\ts_nop 0\n\tbuffer_load_dwordx4 %1, %2, %3 offen sc1 lds\n\tbuffer_load_dwordx4 %1, %2, %3 offen offset:1024 sc1 lds\n\t"
               "buffer_load_dwordx4 %1, %2, %3 offen offset:2048 sc1 lds\n\tbuffer_load_dwordx4 %1, %2, %3 offen offset:3072 sc1 lds" ::"s"(ldsdst), "v"(voff), "s"(rs), "s"(soff) : "memory");
}
DI float swapmax(float m) {
  auto rr = __builtin_amdgcn_permlane32_swap(__float_as_uint(m), __float_as_uint(m), false, false);
  return __builtin_fmaxf(__uint_as_float(rr[0]), __uint_as_float(rr[1]));
}
DI float swapsum(float m) {
  auto rr = __builtin_amdgcn_permlane32_swap(__float_as_uint(m), __float_as_uint(m), false, false);
  return __uint_as_float(rr[0]) + __uint_as_float(rr[1]);
}
#define MX3(a, b, c) __builtin_fmaxf(__builtin_fmaxf((a), (b)), (c))
DI float max16(const f32x16& p) {
  float a = MX3(p[0], p[1], p[2]), b = MX3(p[3], p[4], p[5]);
  a = MX3(a, p[6], p[7]); b = MX3(b, p[8], p[9]); a = MX3(a, p[10], p[11]); b = MX3(b, p[12], p[13]);
  a = MX3(a, p[14], p[15]);
  return __builtin_fmaxf(a, b);
}
DI bool sum_bad(float t) { return __float_as_uint(t) - SUM_LO > SUM_HI - SUM_LO; }
DI int keyidx(int i) { return (i & 3) + 16 * ((i >> 2) & 1) + 8 * (i >> 3); }
DI void redo_tile(unsigned tile, bf16x8 q0, bf16x8 q1, bf16x8 sel, f32x16& o, f32x16& negm, float& l, u32x4& p0, u32x4& p1, f32x4& L) {
  int ln = threadIdx.x & 63; PIN(ln);
  const unsigned ka = (tile & ~3u) + ln * 16, mode = tile & 3u;
  const bf16x8 k0 = lds128(ka), k1 = lds128(ka + 1024);
  const int rr = (ln & 31) - 4 * (ln >> 5);
  f32x16 s;
#pragma unroll
  for (int i = 0; i < 16; ++i) s[i] = (mode != 0 && (keyidx(i) <= rr) == (mode == 2)) ? -__builtin_inff() : negm[0];
  s = MFMA(k0, q0, s); s = MFMA(k1, q1, s);
  const float mx = swapmax(max16(s)), d = mode == 1 ? mx : __builtin_fmaxf(mx, 0.f), f = mode == 1 ? 0.f : __builtin_amdgcn_exp2f(-d), nm = negm[0] - d;
  l *= f;
#pragma unroll
  for (int i = 0; i < 16; ++i) { negm[i] = nm; o[i] *= f; s[i] = __builtin_amdgcn_exp2f(s[i] - d); }
  p0 = pack8(s, 0); p1 = pack8(s, 1);
  f32x4 z = {};
  L = __builtin_amdgcn_mfma_f32_16x16x32_bf16(sel, asb(p0), z, 0, 0, 0);
  L = __builtin_amdgcn_mfma_f32_16x16x32_bf16(sel, asb(p1), L, 0, 0, 0);
  float t = L[0]; PIN(t); L[0] = t;
  PIN(p0); PIN(p1); PIN(o); PIN(negm);
  asm volatile("s_nop 1" ::: "memory");
}

typedef unsigned long long u64x8 __attribute__((ext_vector_type(8)));
__global__ __launch_bounds__(512, 2) void k_fused(const void* xA_, const void* wA_, const void* woA_, const void* xB_, const void* wB_, const void* woB_,
                                                  void* ws_, void* out_) {
  u64x8 ka; asm volatile("s_load_dwordx16 %0, %1, 0x0" : "=&s"(ka) : "s"(__builtin_amdgcn_kernarg_segment_ptr()) : "memory");
  __shared__ __attribute__((aligned(1024))) char smem[LDS_BYTES];
  const unsigned lds0 = (unsigned)(size_t)(LAS char*)smem;
  const unsigned bid = blockIdx.x, xcd = bid & 7, idx = bid >> 3;
  const unsigned br = xcd >> 2, R = RMAP, hp = HPMAP;
  const unsigned L = (br * 8 + R) * 16 + hp;
  const unsigned G = br * 8 + R, ns = hp, loc = hp >> 3;
  const unsigned tid = threadIdx.x, l = tid & 63, w = rfl(tid >> 6), wm = w & 3, wn = w >> 2;
  const unsigned mtw = w, mp = wn ? 3 - wm : wm;
  unsigned xv0 = (R * 256 + mtw * 32 + (l >> 3)) * 2048 + (((l & 7) ^ (l >> 4)) << 4);
  unsigned xv1 = (R * 256 + mtw * 32 + 8 + (l >> 3)) * 2048 + (((l & 7) ^ (4 + (l >> 4))) << 4);
  unsigned wv = (8 * w + (l >> 3)) * 6144 + (((l & 7) ^ ((((l >> 4) & 1) | ((w & 1) << 1)) << 1)) << 4) + hp * 128;
  asm volatile("s_waitcnt lgkmcnt(0)" : "+s"(ka), "+v"(xv0), "+v"(xv1), "+v"(wv) :: "memory");
  const void *xA = (const void*)ka[0], *wA = (const void*)ka[1], *woA = (const void*)ka[2], *xB = (const void*)ka[3], *wB = (const void*)ka[4], *woB = (const void*)ka[5];
  void *ws = (void*)ka[6], *out = (void*)ka[7];
  const __amdgpu_buffer_rsrc_t rx = mkrs(br ? xB : xA, NOMEMX * S_ * D_ * 2), rw = mkrs(br ? wB : wA, D_ * 3 * D_ * 2), rws = mkrs(ws, WS_END);
  STAMP(0);
  const unsigned xdst = lds0 + w * 4096, wdst = lds0 + LDS_W + w * 1024;
#define DMA_X(t, st) do { const unsigned so_ = (t) * 128u, d_ = xdst + (st); \
    dma16x(d_, xv0, rx, so_); dma16x(d_ + 1024, xv1, rx, so_); dma16x(d_ + 2048, xv0, rx, so_ + 32768); dma16x(d_ + 3072, xv1, rx, so_ + 32768); } while (0)
#define DMA_W(t, st) do { const unsigned so_ = (t) * (64u * 6144u), d_ = wdst + (st); \
    dma16w(d_, wv, rw, so_); dma16w(d_ + 8192, wv, rw, so_ + 2048); dma16w(d_ + 16384, wv, rw, so_ + 4096); } while (0)
  DMA_W(0, 0); DMA_X(0, 0);
  const unsigned fl_id = l < 32 ? G * 16 + (l >> 1) : (l == 32 ? L - 16 : L);
  const bool fl_ok = l < 34 && !(l == 32 && R == 0);
  unsigned obase = ld32_issue(rws, fl_ok ? WS_FLAG + fl_id * 128 : OOB);
  DMA_W(1, WST); DMA_X(1, XST);
  const unsigned pli = l < 13 ? l : 0;
  const unsigned pwl = ((idx & 3) * 8 + w) * 6 + (pli - 4);
  const unsigned pq = (idx * 8 + w) * 3 + (pli - 10);
  const char* pfp = pli < 4 ? (const char*)(br ? xB : xA) + (R * 256 + ((idx >> 2) * 8 + w) * 4 + pli) * 2048 + (PFD - 1) * 128
                  : pli < 10 ? (const char*)(br ? wB : wA) + (pwl / 3 + (PFD - 1) * 64) * 6144 + (pwl % 3) * 2048 + hp * 128
                             : (const char*)(br ? woB : woA) + (pq >> 3) * 2048 + loc * 1024 + (pq & 7) * 128;
  unsigned pstep = pli < 4 ? 128u : pli < 10 ? 64u * 6144u : (unsigned)((1023 - 95) / (16 - PFD) < 96 ? (1023 - 95) / (16 - PFD) : 96) * 2048u;
  pf4(lds0 + LDS_CNT + 256, pfp); pfp += pstep;
#define CODEPF(bytes) do { unsigned long long pc; asm volatile("s_getpc_b64 %0" : "=s"(pc));     \
    const unsigned co = (w * 64 + l) * 128; \
    pf4(lds0 + LDS_CNT + 256, (const char*)(pc & ~127ull) + (co < (bytes) ? co : 0)); } while (0)
  if (tid < 8) *(LAS unsigned*)(size_t)(lds0 + LDS_CNT + tid * 4) = 0;
  const unsigned c = l & 15, g = l >> 4, q4 = c >> 2;
  const unsigned xa_ = lds0 + mp * 8192 + c * 128 + ((g ^ (c >> 1)) << 4);
  const unsigned wa_ = lds0 + LDS_W + (8 * g + q4) * 128 + (((2 * wn) ^ ((q4 >> 1) | ((g & 1) << 1))) << 5) + (l & 3) * 8;
  f32x4 acc[6][4] = {};
  u32x4 mk0, mk1; unsigned sel1 = ((c & 3) == 0 && ((c >> 2) & 1) == (g & 1)) ? 0x3F803F80u : 0u;
  { const int rr = (int)(l & 31) - 4 * (int)(l >> 5);
#pragma unroll
    for (int i = 0; i < 4; ++i) {
      mk0[i] = (keyidx(2 * i) <= rr ? 0xFFFFu : 0u) | (keyidx(2 * i + 1) <= rr ? 0xFFFF0000u : 0u);
      mk1[i] = (keyidx(8 + 2 * i) <= rr ? 0xFFFFu : 0u) | (keyidx(9 + 2 * i) <= rr ? 0xFFFF0000u : 0u); }
    PIN(mk0); PIN(mk1); PIN(sel1); }
  f32x16 oa, ob; f32x4 Ls; u32x4 p0, p1; bf16x8 v0, v1, sel;
  Frag A, B;
#define RDX(F, xb, s, u) F.x[u] = lds128(((xb) ^ ((s) << 6)) + (u) * 2048)
#define MM(C, n, u) do { const bf16x8 wf_ = cat(C.wl[n], C.wh[n]); if ((n) < 4) MFA(acc[n][u], wf_, C.x[u]); else MFA(acc[n][u], C.x[u], wf_); } while (0)
#define RDWL(F, wb, s, n) F.wl[n] = ldstr(((wb) ^ (((n) & 1) << 5)) + ((n) >> 1) * 8192 + (s) * 4096)
#define RDWH(F, wb, s, n) F.wh[n] = ldstr(((wb) ^ (((n) & 1) << 5)) + ((n) >> 1) * 8192 + (s) * 4096 + 512)
#define RD1(F, xb, wb, s, r) do { if ((r) == 0) RDX(F, xb, s, 0); else if ((r) == 1) RDWL(F, wb, s, 0); else if ((r) == 2) RDWH(F, wb, s, 0); \
    else if ((r) < 6) RDX(F, xb, s, ((r) - 2) & 3); else if (((r) & 1) == 0) RDWL(F, wb, s, (((r) + 8) >> 1) % 6); else RDWH(F, wb, s, (((r) + 8) >> 1) % 6); } while (0)
#define PB 27
#define PFP 36
#define BP(r) ((r) * 3 / 2)
#define AP(r) (29 + (r) * 6 / 5)
#define XP(j) (2 + (j) * 6)
#define WP(j) (28 + (j) * 3)
#define TILE(LDA, DMA, WAITB) do { \
    const unsigned xb = xa + xs, wb = wa + wsx, wd = wdst + wsx; \
    xp = xs; xs = (xs == 2 * XST) ? 0 : xs + XST; wsx ^= WST; \
    const unsigned xn = xa + xs, wn_ = wa + wsx; \
    sfor<0, 48>([&](auto mi) AI { constexpr int m_ = decltype(mi)::v; \
      if constexpr (m_ < 24) MM(A, m_ >> 2, m_ & 3); else MM(B, (m_ - 24) >> 2, m_ & 3); \
      sfor<0, 16>([&](auto ri) AI { constexpr int r_ = decltype(ri)::v; \
        if constexpr (BP(r_) == m_) RD1(B, xb, wb, 1, r_); if constexpr (LDA && AP(r_) == m_) RD1(A, xn, wn_, 0, r_); }); \
      sfor<0, 4>([&](auto ji) AI { constexpr int j_ = decltype(ji)::v; \
        if constexpr (DMA && XP(j_) == m_) dma16x(xd + j_ * 1024, (j_ & 1) ? xv1 : xv0, rx, xso + (j_ >> 1) * 32768); \
        if constexpr (DMA && j_ < 3 && WP(j_) == m_) dma16w(wd + j_ * 8192, wv, rw, wso + j_ * 2048); }); \
      if constexpr (PFI && DMA && m_ == PFP) { pf4(lds0 + LDS_CNT + 256, pfp); pfp += pinc; } \
      if constexpr (!LDA) {     \
        if constexpr (m_ == 28) { oa = f32x16{}; PIN(oa); } if constexpr (m_ == 34) { ob = f32x16{}; PIN(ob); } \
        if constexpr (m_ == 40) { p0 = u32x4{}; p1 = u32x4{}; v0 = bf16x8{}; v1 = bf16x8{}; PIN(p0); PIN(p1); PIN(v0); PIN(v1); } \
        if constexpr (m_ == 44) { Ls = f32x4{}; sel = asb(u32x4{sel1, sel1, sel1, sel1}); PIN(Ls); PIN(sel); } } \
      SBAR(); \
      if constexpr (m_ == PB) { WAITB; SBAR(); } }); } while (0)
  const bool ack_ok = l < 17 && !(l == 16 && R == 7);
  unsigned ackoff = ack_ok ? WS_ACK + L * 128 + l * 4 : OOB;
  const char* codep;
  { unsigned long long pc; asm volatile("s_getpc_b64 %0" : "=s"(pc)); const unsigned co = (w * 64 + l) * 128; codep = (const char*)(pc & ~127ull) + (co < CODE_BYTES ? co : 0); }
  unsigned ackdst = (fl_ok && l <= 32 && !(l & 1)) ? WS_ACK + fl_id * 128 + (l < 32 ? ns : 16) * 4 : OOB;
  unsigned xa = xa_, wa = wa_;
  PIN(ackoff); PIN(codep); PIN(ackdst); PIN(xa); PIN(wa); PIN(pfp); PIN(pstep);
  _Pragma("unroll") for (int i_ = 0; i_ < 24; ++i_) PIN(acc[i_ >> 2][i_ & 3]);
  WAIT_BAR_V(8, obase);
  STAMP(1);
  unsigned xs = 0, wsx = 0, xp = 2 * XST;
  sfor<0, 16>([&](auto ri) AI { constexpr int r_ = decltype(ri)::v; RD1(A, xa, wa, 0, r_); SBAR(); });
  if (w == 0) st32(obase ^ ACKX, rws, ackdst);
  const unsigned pbase = __builtin_amdgcn_readlane(obase, 32), mybase = __builtin_amdgcn_readlane(obase, 33);
  SBAR();
#pragma unroll 1
  for (int t = 0; t < 15; ++t) {
    const unsigned xso = t < 14 ? (t + 2) * 128u : 0x80000000u, wso = t < 14 ? (t + 2) * (64u * 6144u) : OOB;
    const unsigned xd = t < 14 ? xdst + xp : lds0 + LDS_SPARE;
    if (__builtin_expect((unsigned)(t - (15 - PFD)) < 2u, 0)) {
      if (t == 15 - PFD) {
        dma4_sc1(lds0 + LDS_CTL + w * 256, ackoff, rws, 0);
        pf4(lds0 + LDS_CNT + 256, codep);
      } else {
        pfp = (const char*)(br ? woB : woA) + ((idx * 8 + w) * 4 + ((l >> 3) & 3)) * 2048 + loc * 1024 + (l & 7) * 128; pstep = 0;
      }
    }
    const unsigned pinc = pstep;
    TILE(true, true, WAIT_BAR(5));
  }
  bool acked = __all(!ack_ok || *(volatile LAS unsigned*)(size_t)(lds0 + LDS_CTL + w * 256 + l * 4) == (mybase ^ ACKX));
  __builtin_amdgcn_s_setprio(2);
  { const unsigned xso = 0, wso = 0, xd = 0, pinc = 0; TILE(false, false, (void)0); }
  asm volatile("s_nop 7" ::: "memory");
  _Pragma("unroll") for (int i_ = 0; i_ < 24; ++i_) PIN(acc[i_ >> 2][i_ & 3]);
  STAMP(2);
  bf16x8 qa0, qa1, qb0, qb1;
  f32x16 sa, sb, na, nb;
  {
    const unsigned ko = (g & 1) * 1024 + ((g >> 1) * 32 + (c & 7) + 16 * (c >> 3)) * 16;
    const unsigned vo = IMG + (g >> 1) * 1024 + ((g & 1) * 32 + c) * 16;
    const unsigned t0 = wn * 32768 + mp * 2 * TIL;
    const unsigned gs = R < 7 ? WS_KV + L * 65536 + t0 : OOB;
#pragma unroll
    for (int u = 0; u < 4; ++u) {
      const unsigned tb = (u >> 1) * TIL;
      const u32x4 kk = pack44(acc[2][u], acc[3][u]), vv = pack44(acc[4 + (u & 1)][u & 2], acc[4 + (u & 1)][(u & 2) + 1]);
      __builtin_amdgcn_raw_buffer_store_b128(kk, rws, ko + (u & 1) * 128 + tb, gs, 18);
      __builtin_amdgcn_raw_buffer_store_b128(vv, rws, vo + (u & 1) * 256 + tb, gs, 18);
      sts128(lds0 + LDS_OWN + t0 + tb + ko + (u & 1) * 128, kk);
      sts128(lds0 + LDS_OWN + t0 + tb + vo + (u & 1) * 256, vv);
    }
    lds_inc(lds0 + LDS_CNT + wn * 4);
    if (mp < 2) __builtin_amdgcn_s_setprio(1);
    u32x4 qf[4]; float sd[4];
#pragma unroll
    for (int u = 0; u < 4; ++u) {
      const f32x4 qx = acc[0][u] * QSCALE, qy = acc[1][u] * QSCALE;
      qf[u] = pack44(qx, qy);
      float d = qx[0] * acc[2][u][0];
#pragma unroll
      for (int i = 1; i < 4; ++i) d = __builtin_fmaf(qx[i], acc[2][u][i], d);
#pragma unroll
      for (int i = 0; i < 4; ++i) d = __builtin_fmaf(qy[i], acc[3][u][i], d);
      sd[u] = d;
    }
#pragma unroll
    for (int p = 0; p < 2; ++p) {
#pragma unroll
      for (int i = 0; i < 4; ++i) { auto r = __builtin_amdgcn_permlane16_swap(qf[2 * p][i], qf[2 * p + 1][i], false, false); qf[2 * p][i] = r[0]; qf[2 * p + 1][i] = r[1]; }
      auto r = __builtin_amdgcn_permlane16_swap(__float_as_uint(sd[2 * p]), __float_as_uint(sd[2 * p + 1]), false, false);
      sd[2 * p] = -swapsum(__uint_as_float(r[0]) + __uint_as_float(r[1]));
    }
    qb0 = asb(qf[0]); qb1 = asb(qf[1]); qa0 = asb(qf[2]); qa1 = asb(qf[3]);
    { const float one = l < 32 ? 1.f : 0.f;
      asm volatile("s_nop 1\n\tv_mfma_f32_32x32x2_f32 %0, %2, %3, 0\n\tv_mfma_f32_32x32x2_f32 %1, %2, %4, 0" : "=&v"(na), "=&v"(nb) : "v"(one), "v"(sd[2]), "v"(sd[0])); }
  }
  float la = 0.f, lb = 0.f;
  bf16x8 k0, k1;
  PIN(qa0); PIN(qa1); PIN(qb0); PIN(qb1);
  STAMP(3);
  const __amdgpu_buffer_rsrc_t rwo = mkrs(br ? woB : woA, D_ * D_ * 2);
  const unsigned wov = (l >> 3) * 2048 + (((l & 7) ^ (((l >> 4) & 3) << 1)) << 4) + ns * 128;
#define WOB(gq) ((gq) == 0 ? 131072u : ((gq) & 3) == 3 ? 0u : (gq) * 32768u)
#define DMA_WO(j, ww) dma16(lds0 + WOB((j) >> 2) + ((j) & 3) * 8192 + (ww) * 1024, wov, rwo, ((j) ^ (loc * 8)) * (64u * 2048u) + (ww) * 16384u)
  const unsigned lb16 = l * 16, own = lds0 + LDS_OWN + wn * 32768, prv = R > 0 ? lds0 + wn * 98304 : own;
  const unsigned myflag = WS_FLAG + L * 128, pfoff = WS_FLAG + (L - 16) * 128 + 4 + wn * 4;
#define QK0(S, Q, N) asm volatile("v_mfma_f32_32x32x16_bf16 %0, %1, %2, %3" : "=&v"(S) : "v"(k0), "v"(Q), "v"(N), "v"(k1))
#define QK1(S, Q) asm volatile("v_mfma_f32_32x32x16_bf16 %0, %1, %2, %0" : "+v"(S) : "v"(k1), "v"(Q))
#define PV0(O) asm volatile("v_mfma_f32_32x32x16_bf16 %0, %1, %2, %0" : "+v"(O) : "v"(v0), "v"(p0), "v"(v1))
#define PV0S(O) asm volatile("s_nop 1\n\tv_mfma_f32_32x32x16_bf16 %0, %1, %2, %0" : "+v"(O) : "v"(v0), "v"(p0), "v"(v1))
#define PV1(O) asm volatile("v_mfma_f32_32x32x16_bf16 %0, %1, %2, %0" : "+v"(O) : "v"(v1), "v"(p1))
#define EX(S, i) do { S[i] = __builtin_amdgcn_exp2f(S[i]); } while (0)
#define RDK(a) do { k0 = lds128(a); k1 = lds128((a) + 1024); } while (0)
  unsigned pa = own + (2 * mp + 1) * TIL, pb = pa - TIL, pbp = pb;
  unsigned va = lb16 + pa, vb = lb16 + pb;
  RDK(va);
  asm volatile("s_nop 1" ::: "memory");
  QK0(sa, qa0, na); QK1(sa, qa1);
  RDK(vb);
  asm volatile("s_nop 7\n\ts_nop 1" ::: "memory");
  SBAR();
#define EXP(i) e_[i] = __builtin_amdgcn_exp2f(sx_[i])
#define HALF(SX, SY, NY, LY, OY, QY0, QY1, PY, RD, VX, KNEXT, BAR1, MSK, LSUM, PVF) do { \
    const f32x16 sx_ = SX; float e_[16]; u32x4 pn_; \
    EXP(0); EXP(1); EXP(2); EXP(3); EXP(4); asm volatile("" : "+v"(e_[0]), "+v"(e_[1]), "+v"(e_[2]), "+v"(e_[3]), "+v"(e_[4])); SBAR();     \
    EXP(5); asm volatile("" : "+v"(e_[5]), "+v"(Ls)); SBAR(); \
    float t_ = LY + Ls[0]; unsigned long long bad_ = (BAR1) ? 0ull : __builtin_amdgcn_ballot_w64(sum_bad(t_)); asm volatile("" : "+s"(bad_), "+v"(t_)); SBAR(); \
    pn_[0] = pk2(e_[0], e_[1]); pn_[1] = pk2(e_[2], e_[3]); pn_[2] = pk2(e_[4], e_[5]); EXP(6); \
    asm volatile("" : "+v"(pn_[0]), "+v"(pn_[1]), "+v"(pn_[2]), "+v"(e_[6])); SBAR(); \
    if (!(BAR1) && __builtin_expect(bad_ != 0, 0)) { redo_tile(PY, QY0, QY1, sel, OY, NY, LY, p0, p1, Ls); t_ = LY + Ls[0]; } \
    LY = t_; \
    PVF(OY); SBAR(); \
    EXP(7); EXP(8); pn_[3] = pk2(e_[6], e_[7]); asm volatile("" : "+v"(pn_[3]), "+v"(e_[8])); SBAR(); \
    PV1(OY); SBAR(); if (RD) { v0 = lds128((VX) + IMG); v1 = lds128((VX) + IMG + 1024); VX = KNEXT; } \
    EXP(9); EXP(10); asm volatile("" : "+v"(e_[9]), "+v"(e_[10]), "+v"(VX)); SBAR(); \
    QK0(SY, QY0, NY); SBAR(); EXP(11); EXP(12); EXP(13); asm volatile("" : "+v"(e_[11]), "+v"(e_[12]), "+v"(e_[13])); SBAR(); \
    QK1(SY, QY1); SBAR(); \
    EXP(14); EXP(15); asm volatile("" : "+v"(e_[14]), "+v"(e_[15])); SBAR(); \
    if (BAR1) { if (PFI) WAITV(9); else WAITV(8); lds_inc(lds0 + LDS_CNT + 8); if (mp) lds_wait(lds0 + LDS_CNT + wn * 4, 4); } \
    if (RD) RDK(VX); SBAR(); \
    p0 = pn_; p1[0] = pk2(e_[8], e_[9]); p1[1] = pk2(e_[10], e_[11]); p1[2] = pk2(e_[12], e_[13]); p1[3] = pk2(e_[14], e_[15]); SBAR(); \
    if (MSK) { p0 &= mk0; p1 &= mk1; PIN(p0); PIN(p1); asm volatile("s_nop 0"); } \
    LSUM; SBAR(); } while (0)
#define LMF0 "v_mfma_f32_16x16x32_bf16 %0, %1, %2, 0\n\t"
#define LMF1 "v_mfma_f32_16x16x32_bf16 %0, %1, %3, %0\n\t"
#define LSUM_G() asm volatile(LMF0 "s_nop 0\n\t" LMF1 "s_nop 1" : "=&v"(Ls) : "v"(sel), "v"(p0), "v"(p1))
#define LSUM_A() asm volatile(LMF0 "s_nop 0\n\t" LMF1 "s_nop 0" : "=&v"(Ls) : "v"(sel), "v"(p0), "v"(p1))
#define LSUM_B() asm volatile("v_mfma_f32_16x16x32_bf16 %0, %3, %4, 0\n\ts_mov_b32 %1, %2\n\tv_mfma_f32_16x16x32_bf16 %0, %3, %5, %0\n\ts_add_i32 %2, %2, 0x1000" : "=&v"(Ls), "=&s"(pbp), "+s"(pa) : "v"(sel), "v"(p0), "v"(p1) : "scc")
  unsigned sent = 0;

#define REPF() do {} while (0)
#define SIGNAL() do { WAITV(0); \
    _Pragma("unroll 1") for (unsigned sp = 0; !acked && sp < SPIN_CAP; ++sp) { __builtin_amdgcn_s_sleep(2); acked = __all(!ack_ok || ld32_wait(rws, ackoff) == (mybase ^ ACKX)); } \
    if (lds_inc_rtn(lds0 + LDS_CNT + 24 + wn * 4) == 3 && l == 0) st32(mybase + 1, rws, myflag + 4 + wn * 4); \
    REPF(); \
    sent = 1; if (mp >= 2) __builtin_amdgcn_s_setprio(0); STAMP(13); } while (0)
#define TRYSIG() do { if (__builtin_expect(!sent, 0)) { if ((__builtin_amdgcn_s_getreg(7 | (31 << 11)) & 0x00C0000Fu) == 0) SIGNAL(); } } while (0)
#define PAIR(PAN, PBN, MD, FIRST) do { const unsigned pan = (PAN), pbn = (PBN); \
      HALF(sa, sb, nb, lb, ob, qb0, qb1, pbp, true, va, lb16 + pan, FIRST, (MD) != 0, LSUM_G(), PV0S); \
      HALF(sb, sa, na, la, oa, qa0, qa1, pa | (MD), true, vb, lb16 + pbn, false, (MD) != 0, LSUM_G(), PV0S); \
      pbp = pb | (MD); pa = pan; pb = pbn; } while (0)
#define HOT(N, SIG) do { _Pragma("unroll 1") for (int n = (N); n > 0; --n) {     \
      if (SIG) TRYSIG(); \
      HALF(sa, sb, nb, lb, ob, qb0, qb1, pbp, true, va, va + TIL, false, false, LSUM_A(), PV0); \
      HALF(sb, sa, na, la, oa, qa0, qa1, pa, false, vb, 0, false, false, LSUM_B(), PV0); } } while (0)
  PAIR(own, own, 1u, true);
  HOT(2 * mp, true);
  {
    STAMP(4);
    if (!sent) SIGNAL();
    STAMP(5);
    mk0 = ~mk0; mk1 = ~mk1; PIN(mk0); PIN(mk1);
    if (mp < 2) {
      lds_wait(lds0 + LDS_CNT + 8, 8);
      unsigned h_ = wn; asm volatile("" : "+s"(h_));
      if (mp) {
#pragma unroll
        for (int j = 0; j < 15; ++j) DMA_WO(j >> 2, h_ * 4 + (j & 3));
        if (!h_) DMA_WO(3, 3);
      } else if (R > 0) {
#pragma unroll 1
        for (unsigned sp = 0; sp < SPIN_CAP; ++sp) {
          if (rfl(ld32_wait(rws, pfoff)) == pbase + 1) break;
          __builtin_amdgcn_s_sleep(KVS);
        }
        STAMP(14);
#pragma unroll
        for (int j = 0; j < 8; ++j) dma64_sc1(lds0 + h_ * 98304 + j * TIL, lb16, rws, WS_KV + (L - 16) * 65536 + h_ * 32768 + j * TIL);
        WAITV(12);
        lds_inc(lds0 + LDS_CNT + 12 + h_ * 4);
      }
    }
    STAMP(6);
    if (R > 0) lds_wait(lds0 + LDS_CNT + 12 + wn * 4, mp < 2 ? 1 : 2);
    STAMP(7);
    pb = prv + (2 * mp + 1) * TIL; vb = lb16 + pb;
    RDK(vb);
    SBAR();
  }
  PAIR(prv + (2 * mp + 1) * TIL, prv + 2 * mp * TIL, 0u, false);
  lds_inc(lds0 + LDS_CNT + 20);
  if (R > 0) {
    if (mp == 0) { WAITV(0); lds_inc(lds0 + LDS_CNT + 12 + wn * 4); }
    PAIR(prv + (2 * mp + 2) * TIL, prv + (2 * mp + 2) * TIL, 2u, false);
    if (mp == 1) lds_wait(lds0 + LDS_CNT + 12 + wn * 4, 2);
    HOT(6 - 2 * mp, false);
    asm volatile("s_nop 7" : "+v"(Ls) :: "memory");
    if (__builtin_expect(__any(sum_bad(lb + Ls[0])), 0)) redo_tile(pbp, qb0, qb1, sel, ob, nb, lb, p0, p1, Ls);
    lb += Ls[0];
    PV0S(ob); PV1(ob);
  } else {
    asm volatile("s_nop 7" : "+v"(Ls), "+v"(sa) :: "memory");
  }
  __builtin_amdgcn_s_setprio(0);
  STAMP(8);
  { const float ia = __builtin_amdgcn_rcpf(la), ib = __builtin_amdgcn_rcpf(lb);
    oa *= ia;
    const unsigned hh = hp * 2 + wn, T0 = br * 64 + R * 8 + 2 * mp;
    const unsigned ol = ((l >> 4) & 1) * 1024 + ((l >> 5) * 16 + (l & 15)) * 16;
    __builtin_amdgcn_raw_buffer_store_b128(pack8(oa, 0), rws, ol, WS_O + (T0 * 32 + 32 + hh) * IMG, SC1);
    __builtin_amdgcn_raw_buffer_store_b128(pack8(oa, 1), rws, ol + 512, WS_O + (T0 * 32 + 32 + hh) * IMG, SC1);
    SBAR();
    asm volatile("s_nop 7" : "+v"(ob) :: "memory");
    ob *= ib;
    __builtin_amdgcn_raw_buffer_store_b128(pack8(ob, 0), rws, ol, WS_O + (T0 * 32 + hh) * IMG, SC1);
    __builtin_amdgcn_raw_buffer_store_b128(pack8(ob, 1), rws, ol + 512, WS_O + (T0 * 32 + hh) * IMG, SC1); }
  WAITV(0);
  STAMP(9);
  asm volatile("s_barrier" ::: "memory");
  if (tid == 64) st32(mybase + 2, rws, myflag);
  dma4(lds0 + LDS_WOX + 1024, l < 32 ? (l >> 4) * 65536 + (l & 15) * 128 : OOB, rws, WS_O + ((br * 64 + R * 8 + 2 * mp) * 32 + hp * 2 + wn) * IMG);
  if (w == 7) DMA_WO(3, 7);
  const unsigned fo = l < 32 ? WS_FLAG + (G * 16 + (l >> 1)) * 128 : OOB;
#pragma unroll
  for (int j = 4; j < 16; ++j) DMA_WO(j, w);
  if (w == POLLW) {
    unsigned f;
#pragma unroll 1
    for (unsigned sp = 0; sp < SPIN_CAP; ++sp) {
      f = ld32_wait(rws, fo);
      if (__all((l >> 4) != loc || f == obase + 2)) break;
      __builtin_amdgcn_s_sleep(POLLS);
    }
    *(volatile LAS unsigned*)(size_t)(lds0 + LDS_WOX) = __all(l >= 32 || f == obase + 2);
  }
  WAIT_BAR(0);
  STAMP(10);
  const unsigned T = G * 8 + w;
  const unsigned obs = WS_O + T * 32 * IMG;
  const unsigned wa2 = lds0 + (16 * (g >> 1) + 4 * (g & 1) + q4) * 128 + ((2 * (g & 1) + (q4 >> 1)) << 5) + (l & 3) * 8;
  f32x4 oc[4][2] = {};
  u32x4 of[16];
  s16x4 wl[2][4], wh[2][4];
#pragma unroll
  for (int i = 0; i < 16; ++i) {
    of[i] = __builtin_amdgcn_raw_buffer_load_b128(rws, l * 16 + (i & 3) * 1024, obs + loc * 32768 + (i >> 2) * 4096, SC1);
    if (i == FILL0 - 1 || i == FILL1 - 1) { SBAR(); __builtin_amdgcn_s_sleep(FILLS); SBAR(); }
  }
  const unsigned allrdy = rfl(*(volatile LAS unsigned*)(size_t)(lds0 + LDS_WOX));
#pragma unroll
  for (int nt = 0; nt < 4; ++nt) { wl[0][nt] = ldstr((wa2 ^ (nt << 5)) + WOB(0)); wh[0][nt] = ldstr((wa2 ^ (nt << 5)) + WOB(0) + 1024); }
  _Pragma("unroll") for (int i_ = 0; i_ < 8; ++i_) PIN(oc[i_ >> 1][i_ & 1]);
  asm volatile("s_nop 1" ::: "memory");
  SBAR();
#pragma unroll 1
  for (int gq = 0; gq < 4; ++gq) {
    const unsigned wb = wa2 + WOB(gq), wbn = wa2 + WOB(gq + 1);
    if (__builtin_expect(gq == 1 && !allrdy, 0)) {
      if (w == POLLW) {
#pragma unroll 1
        for (unsigned sp = 0; sp < SPIN_CAP; ++sp) { if (__all(l >= 32 || ld32_wait(rws, fo) == obase + 2)) break; __builtin_amdgcn_s_sleep(POLLS); }
      }
      asm volatile("s_barrier" ::: "memory");
    }
    const unsigned on = gq < 3 ? obs + ((gq + 1) ^ (loc * 2)) * 16384 : OOB;
    if (gq == 0) __builtin_amdgcn_s_setprio(3); else if (gq == 1) __builtin_amdgcn_s_setprio(2); else if (gq == 2) __builtin_amdgcn_s_setprio(1); else __builtin_amdgcn_s_setprio(0);
    sfor<0, 8>([&](auto hi) AI { constexpr int h_ = decltype(hi)::v, cur = h_ & 1, nxt = cur ^ 1;
      sfor<0, 8>([&](auto mi) AI { constexpr int m_ = decltype(mi)::v, nt = m_ >> 1, tt = m_ & 1;
        { const bf16x8 wf = cat(wl[cur][nt], wh[cur][nt]);
          if constexpr (m_ == 0) {
            const bf16x8 w3 = cat(wl[cur][3], wh[cur][3]);
            asm volatile("v_mfma_f32_16x16x32_bf16 %0, %1, %2, %0" : "+v"(oc[0][0]) : "v"(wf), "v"(of[2 * h_]), "v"(w3), "v"(of[2 * h_ + 1]));
          } else MFA(oc[nt][tt], wf, of[2 * h_ + tt]); }
        sfor<0, 8>([&](auto ri) AI { constexpr int r_ = decltype(ri)::v, rn = r_ >> 1;
          if constexpr (G2R(r_) == m_) {
            const unsigned wq_ = ((h_ < 7 ? wb : wbn) ^ (rn << 5)) + ((h_ + 1) & 7) * 4096;
            if constexpr ((r_ & 1) == 0) wl[nxt][rn] = ldstr(wq_); else wh[nxt][rn] = ldstr(wq_ + 1024); } });
        if constexpr (m_ >= 6) of[2 * h_ + tt] = __builtin_amdgcn_raw_buffer_load_b128(rws, l * 16 + ((2 * h_ + tt) & 3) * 1024, on + ((2 * h_ + tt) >> 2) * 4096, SC1);
        SBAR(); }); });
  }
  asm volatile("s_nop 7" ::: "memory");
  _Pragma("unroll") for (int i_ = 0; i_ < 8; ++i_) PIN(oc[i_ >> 1][i_ & 1]);
  STAMP(11);
  const __amdgpu_buffer_rsrc_t rout = mkrs(out, S_ * 2 * D_ * 2);
  const unsigned orow = ((R * 8 + w) * 32 + c) * 4096 + br * 2048 + ns * 128 + (g & 1) * 32 + (g >> 1) * 16;
  const unsigned orow2 = orow - (c & 8) * 4096 + (c & 8) * 8;
#pragma unroll
  for (int tt = 0; tt < 2; ++tt) {
    u32x4 v[2], va_, vb_;
#pragma unroll
    for (int np = 0; np < 2; ++np) {
      const f32x4 &a = oc[2 * np][tt], &b = oc[2 * np + 1][tt];
      auto r0 = __builtin_amdgcn_permlane16_swap(pk2(a[0], a[1]), pk2(b[0], b[1]), false, false);
      auto r1 = __builtin_amdgcn_permlane16_swap(pk2(a[2], a[3]), pk2(b[2], b[3]), false, false);
      v[np] = u32x4{r0[0], r1[0], r0[1], r1[1]};
    }
#pragma unroll
    for (int i = 0; i < 4; ++i) {
      const unsigned y = c < 8 ? v[1][i] : v[0][i];
      const unsigned r = (unsigned)__builtin_amdgcn_update_dpp(0, (int)y, 0x128, 0xf, 0xf, false);
      va_[i] = c < 8 ? v[0][i] : r; vb_[i] = c < 8 ? r : v[1][i];
    }
    __builtin_amdgcn_raw_buffer_store_b128(va_, rout, orow2 + tt * 65536, 0, OUTPOL);
    __builtin_amdgcn_raw_buffer_store_b128(vb_, rout, orow2 + tt * 65536 + 32768, 0, OUTPOL);
  }
}

extern "C" void kernel_launch(void* const* d_in, const int* in_sizes, int n_in, void* d_out, int out_size, void* d_ws, size_t ws_size,
                              hipStream_t stream) {
  const size_t wsoff = (ws_size - WS_END) & ~(size_t)0xfffff;
  k_fused<<<256, 512, 0, stream>>>(d_in[0], d_in[1], d_in[2], d_in[3], d_in[4], d_in[5], (char*)d_ws + wsoff, d_out);
}
```

```cpp
#define DIAG 2
#define PFMODE 2
#define POLLS 8
#include <hip/hip_runtime.h>
#include <hip/hip_bf16.h>

typedef short bf16x8 __attribute__((ext_vector_type(8)));
typedef short s16x4 __attribute__((ext_vector_type(4)));
typedef float f32x16 __attribute__((ext_vector_type(16)));
typedef float f32x4 __attribute__((ext_vector_type(4)));
typedef unsigned u32x4 __attribute__((ext_vector_type(4)));
typedef __bf16 bf16x2_t __attribute__((ext_vector_type(2)));
typedef float f32x2 __attribute__((ext_vector_type(2)));
#define LAS __attribute__((address_space(3)))
#define DI __device__ __forceinline__
#define AI __attribute__((always_inline))
#define MFMA(a, b, c) __builtin_amdgcn_mfma_f32_32x32x16_bf16((a), (b), (c), 0, 0, 0)
#define SBAR() __builtin_amdgcn_sched_barrier(0)
#define PIN(x) asm volatile("" : "+v"(x))
#define MFA(acc_, a_, b_) asm volatile("v_mfma_f32_16x16x32_bf16 %0, %1, %2, %0" : "+v"(acc_) : "v"(a_), "v"(b_))

constexpr int S_ = 2048, D_ = 1024;
constexpr float QSCALE = 0.17677669529663687f * 1.4426950408889634f;
constexpr unsigned SUM_LO = 0x2B800000u, SUM_HI = 0x53800000u;
constexpr unsigned IMG = 2048;
constexpr unsigned WS_FLAG = 0, WS_ACK = 32768, WS_KV = 65536, WS_O = WS_KV + 256u * 65536u, WS_END = WS_O + 2u * 64u * 32u * IMG;
constexpr unsigned ACKX = 0x5bd1e995u;
constexpr int SC1 = 16;
constexpr unsigned CODE_BYTES = 25600;
constexpr unsigned SPIN_CAP = 1u << 18;

#define HPMAP ((xcd & 1) * 8 + (idx >> 2))
#define RMAP (((xcd >> 1) & 1) * 4 + (idx & 3))
#define NOMEMX 1
template <int I> struct IC { static constexpr int v = I; };
template <int B, int E, class F> DI void sfor(F&& f) { if constexpr (B < E) { f(IC<B>{}); sfor<B + 1, E>(f); } }
DI unsigned rfl(unsigned x) { return (unsigned)__builtin_amdgcn_readfirstlane((int)x); }
DI unsigned pk2(float lo, float hi) { f32x2 v = {lo, hi}; return __builtin_bit_cast(unsigned, __builtin_convertvector(v, bf16x2_t)); }
DI u32x4 pack8(const f32x16& x, int s) {
  u32x4 p;
  p[0] = pk2(x[8 * s + 0], x[8 * s + 1]); p[1] = pk2(x[8 * s + 2], x[8 * s + 3]);
  p[2] = pk2(x[8 * s + 4], x[8 * s + 5]); p[3] = pk2(x[8 * s + 6], x[8 * s + 7]);
  return p;
}
DI u32x4 pack44(const f32x4& a, const f32x4& b) { u32x4 p; p[0] = pk2(a[0], a[1]); p[1] = pk2(a[2], a[3]); p[2] = pk2(b[0], b[1]); p[3] = pk2(b[2], b[3]); return p; }
DI bf16x8 asb(u32x4 v) { return __builtin_bit_cast(bf16x8, v); }
DI bf16x8 lds128(unsigned a) { return *(const LAS bf16x8*)(size_t)a; }
DI void sts128(unsigned a, u32x4 v) { *(LAS u32x4*)(size_t)a = v; }
DI s16x4 ldstr(unsigned a) { return __builtin_amdgcn_ds_read_tr16_b64_v4i16((LAS s16x4*)(size_t)a); }
DI bf16x8 cat(s16x4 lo, s16x4 hi) { return __builtin_shufflevector(lo, hi, 0, 1, 2, 3, 4, 5, 6, 7); }
DI void dma16(unsigned ldsdst, unsigned voff, __amdgpu_buffer_rsrc_t rs, unsigned soff) {
  asm volatile("s_mov_b32 m0, %0\n\ts_nop 0\n\tbuffer_load_dwordx4 %1, %2, %3 offen lds" ::"s"(ldsdst), "v"(voff), "s"(rs), "s"(soff) : "memory");
}
#define XPOL ""
#define WPOL ""
DI void dma16x(unsigned ldsdst, unsigned voff, __amdgpu_buffer_rsrc_t rs, unsigned soff) {
  asm volatile("s_mov_b32 m0, %0\n\ts_nop 0\n\tbuffer_load_dwordx4 %1, %2, %3 offen " XPOL " lds" ::"s"(ldsdst), "v"(voff), "s"(rs), "s"(soff) : "memory");
}
DI void dma16w(unsigned ldsdst, unsigned voff, __amdgpu_buffer_rsrc_t rs, unsigned soff) {
  asm volatile("s_mov_b32 m0, %0\n\ts_nop 0\n\tbuffer_load_dwordx4 %1, %2, %3 offen " WPOL " lds" ::"s"(ldsdst), "v"(voff), "s"(rs), "s"(soff) : "memory");
}
#define PPOL ""
#define OUTPOL 16
#define KVS 1
#define FILL0 4
#define FILL1 8
#define FILLS 4
#define G2R(r) ((r) * 3 / 4)
#define POLLW 0
#define PFI true
#define PFD 3
#define PFH 1
DI void pf4(unsigned ldsdst, const char* p) {
  asm volatile("s_mov_b32 m0, %0\n\ts_nop 0\n\tglobal_load_lds_dword %1, off " PPOL ::"s"(ldsdst), "v"(p) : "memory");
}
DI void dma16_sc1(unsigned ldsdst, unsigned voff, __amdgpu_buffer_rsrc_t rs, unsigned soff) {
  asm volatile("s_mov_b32 m0, %0\n\ts_nop 0\n\tbuffer_load_dwordx4 %1, %2, %3 offen sc1 lds" ::"s"(ldsdst), "v"(voff), "s"(rs), "s"(soff) : "memory");
}
#define WAITV(n) asm volatile("s_waitcnt vmcnt(" #n ")" ::: "memory")
#define WAIT_BAR(n) asm volatile("s_waitcnt vmcnt(" #n ") lgkmcnt(0)\n\ts_barrier" ::: "memory")
DI __amdgpu_buffer_rsrc_t mkrs(const void* p, unsigned bytes) { return __builtin_amdgcn_make_buffer_rsrc((void*)p, (short)0, (int)bytes, 0x00020000); }
DI unsigned ld32_issue(__amdgpu_buffer_rsrc_t rs, unsigned off) { unsigned r; asm volatile("buffer_load_dword %0, %1, %2, 0 offen sc1" : "=v"(r) : "v"(off), "s"(rs) : "memory"); return r; }
DI unsigned ld32_wait(__amdgpu_buffer_rsrc_t rs, unsigned off) { unsigned r; asm volatile("buffer_load_dword %0, %1, %2, 0 offen sc1\n\ts_waitcnt vmcnt(0)" : "=&v"(r) : "v"(off), "s"(rs) : "memory"); return r; }
DI u32x4 ld128_issue(__amdgpu_buffer_rsrc_t rs, unsigned off) { u32x4 r; asm volatile("buffer_load_dwordx4 %0, %1, %2, 0 offen sc1" : "=v"(r) : "v"(off), "s"(rs) : "memory"); return r; }
DI u32x4 ld128_wait(__amdgpu_buffer_rsrc_t rs, unsigned off) { u32x4 r; asm volatile("buffer_load_dwordx4 %0, %1, %2, 0 offen sc1\n\ts_waitcnt vmcnt(0)" : "=&v"(r) : "v"(off), "s"(rs) : "memory"); return r; }
DI void dma4(unsigned ldsdst, unsigned voff, __amdgpu_buffer_rsrc_t rs, unsigned soff) {
  asm volatile("s_mov_b32 m0, %0\n\ts_nop 0\n\tbuffer_load_dword %1, %2, %3 offen lds" ::"s"(ldsdst), "v"(voff), "s"(rs), "s"(soff) : "memory");
}
DI void dma4_sc1(unsigned ldsdst, unsigned voff, __amdgpu_buffer_rsrc_t rs, unsigned soff) {
  asm volatile("s_mov_b32 m0, %0\n\ts_nop 0\n\tbuffer_load_dword %1, %2, %3 offen sc1 lds" ::"s"(ldsdst), "v"(voff), "s"(rs), "s"(soff) : "memory");
}
#define WAIT_BAR_V(n, x) asm volatile("s_waitcnt vmcnt(" #n ") lgkmcnt(0)\n\ts_barrier" : "+v"(x) :: "memory")
constexpr unsigned OOB = 0x80000000u;
DI void st32(unsigned v, __amdgpu_buffer_rsrc_t rs, unsigned off) { __builtin_amdgcn_raw_buffer_store_b32(v, rs, off, 0, SC1); }

#define STAMP(i) do { if ((i) == DIAG) { dg1 = __builtin_amdgcn_s_memrealtime(); __builtin_amdgcn_s_waitcnt(0xC07F); } } while (0)

constexpr unsigned XST = 32768, WST = 24576, LDS_W = 3 * XST, LDS_SPARE = LDS_W + 2 * WST, LDS_CTL = 159744;

constexpr unsigned LDS_OWN = 32768, LDS_CNT = 162816, LDS_WOX = 98304, LDS_BYTES = 163840, TIL = 2 * IMG;

struct Frag { bf16x8 x[4]; s16x4 wl[6], wh[6]; };

DI void lds_inc(unsigned a) { asm volatile("s_mov_b64 exec, 1\n\tds_add_u32 %0, %1\n\ts_mov_b64 exec, -1" ::"v"(a), "v"(1u) : "memory"); }
DI unsigned lds_inc_rtn(unsigned a) { unsigned r; asm volatile("s_mov_b64 exec, 1\n\tds_add_rtn_u32 %0, %1, %2\n\ts_mov_b64 exec, -1\n\ts_waitcnt lgkmcnt(0)" : "=&v"(r) : "v"(a), "v"(1u) : "memory"); return rfl(r); }
DI void lds_wait(unsigned a, unsigned n) {
#pragma unroll 1
  for (unsigned sp = 0; sp < 32u * SPIN_CAP && rfl(*(volatile LAS unsigned*)(size_t)a) < n; ++sp) __builtin_amdgcn_s_sleep(1);
  asm volatile("" ::: "memory");
}
DI void dma64_sc1(unsigned ldsdst, unsigned voff, __amdgpu_buffer_rsrc_t rs, unsigned soff) {
  asm volatile("s_mov_b32 m0, %0\n\ts_nop 0\n\tbuffer_load_dwordx4 %1, %2, %3 offen sc1 lds\n\tbuffer_load_dwordx4 %1, %2, %3 offen offset:1024 sc1 lds\n\t"
               "buffer_load_dwordx4 %1, %2, %3 offen offset:2048 sc1 lds\n\tbuffer_load_dwordx4 %1, %2, %3 offen offset:3072 sc1 lds" ::"s"(ldsdst), "v"(voff), "s"(rs), "s"(soff) : "memory");
}
DI float swapmax(float m) {
  auto rr = __builtin_amdgcn_permlane32_swap(__float_as_uint(m), __float_as_uint(m), false, false);
  return __builtin_fmaxf(__uint_as_float(rr[0]), __uint_as_float(rr[1]));
}
DI float swapsum(float m) {
  auto rr = __builtin_amdgcn_permlane32_swap(__float_as_uint(m), __float_as_uint(m), false, false);
  return __uint_as_float(rr[0]) + __uint_as_float(rr[1]);
}
#define MX3(a, b, c) __builtin_fmaxf(__builtin_fmaxf((a), (b)), (c))
DI float max16(const f32x16& p) {
  float a = MX3(p[0], p[1], p[2]), b = MX3(p[3], p[4], p[5]);
  a = MX3(a, p[6], p[7]); b = MX3(b, p[8], p[9]); a = MX3(a, p[10], p[11]); b = MX3(b, p[12], p[13]);
  a = MX3(a, p[14], p[15]);
  return __builtin_fmaxf(a, b);
}
DI bool sum_bad(float t) { return __float_as_uint(t) - SUM_LO > SUM_HI - SUM_LO; }
DI int keyidx(int i) { return (i & 3) + 16 * ((i >> 2) & 1) + 8 * (i >> 3); }
DI void redo_tile(unsigned tile, bf16x8 q0, bf16x8 q1, bf16x8 sel, f32x16& o, f32x16& negm, float& l, u32x4& p0, u32x4& p1, f32x4& L) {
  int ln = threadIdx.x & 63; PIN(ln);
  const unsigned ka = (tile & ~3u) + ln * 16, mode = tile & 3u;
  const bf16x8 k0 = lds128(ka), k1 = lds128(ka + 1024);
  const int rr = (ln & 31) - 4 * (ln >> 5);
  f32x16 s;
#pragma unroll
  for (int i = 0; i < 16; ++i) s[i] = (mode != 0 && (keyidx(i) <= rr) == (mode == 2)) ? -__builtin_inff() : negm[0];
  s = MFMA(k0, q0, s); s = MFMA(k1, q1, s);
  const float mx = swapmax(max16(s)), d = mode == 1 ? mx : __builtin_fmaxf(mx, 0.f), f = mode == 1 ? 0.f : __builtin_amdgcn_exp2f(-d), nm = negm[0] - d;
  l *= f;
#pragma unroll
  for (int i = 0; i < 16; ++i) { negm[i] = nm; o[i] *= f; s[i] = __builtin_amdgcn_exp2f(s[i] - d); }
  p0 = pack8(s, 0); p1 = pack8(s, 1);
  f32x4 z = {};
  L = __builtin_amdgcn_mfma_f32_16x16x32_bf16(sel, asb(p0), z, 0, 0, 0);
  L = __builtin_amdgcn_mfma_f32_16x16x32_bf16(sel, asb(p1), L, 0, 0, 0);
  float t = L[0]; PIN(t); L[0] = t;
  PIN(p0); PIN(p1); PIN(o); PIN(negm);
  asm volatile("s_nop 1" ::: "memory");
}

typedef unsigned long long u64x8 __attribute__((ext_vector_type(8)));
__global__ __launch_bounds__(512, 2) void k_fused(const void* xA_, const void* wA_, const void* woA_, const void* xB_, const void* wB_, const void* woB_,
                                                  void* ws_, void* out_) {
  unsigned long long dg0, dg1 = 0; asm volatile("s_memrealtime %0\n\ts_waitcnt lgkmcnt(0)" : "=s"(dg0) :: "memory"); if (DIAG == 99) dg1 = dg0;
  u64x8 ka; asm volatile("s_load_dwordx16 %0, %1, 0x0" : "=&s"(ka) : "s"(__builtin_amdgcn_kernarg_segment_ptr()) : "memory");
  __shared__ __attribute__((aligned(1024))) char smem[LDS_BYTES];
  const unsigned lds0 = (unsigned)(size_t)(LAS char*)smem;
  const unsigned bid = blockIdx.x, xcd = bid & 7, idx = bid >> 3;
  const unsigned br = xcd >> 2, R = RMAP, hp = HPMAP;
  const unsigned L = (br * 8 + R) * 16 + hp;
  const unsigned G = br * 8 + R, ns = hp, loc = hp >> 3;
  const unsigned tid = threadIdx.x, l = tid & 63, w = rfl(tid >> 6), wm = w & 3, wn = w >> 2;
  const unsigned mtw = w, mp = wn ? 3 - wm : wm;
  unsigned xv0 = (R * 256 + mtw * 32 + (l >> 3)) * 2048 + (((l & 7) ^ (l >> 4)) << 4);
  unsigned xv1 = (R * 256 + mtw * 32 + 8 + (l >> 3)) * 2048 + (((l & 7) ^ (4 + (l >> 4))) << 4);
  unsigned wv = (8 * w + (l >> 3)) * 6144 + (((l & 7) ^ ((((l >> 4) & 1) | ((w & 1) << 1)) << 1)) << 4) + hp * 128;
  asm volatile("s_waitcnt lgkmcnt(0)" : "+s"(ka), "+v"(xv0), "+v"(xv1), "+v"(wv) :: "memory");
  const void *xA = (const void*)ka[0], *wA = (const void*)ka[1], *woA = (const void*)ka[2], *xB = (const void*)ka[3], *wB = (const void*)ka[4], *woB = (const void*)ka[5];
  void *ws = (void*)ka[6], *out = (void*)ka[7];
  const __amdgpu_buffer_rsrc_t rx = mkrs(br ? xB : xA, NOMEMX * S_ * D_ * 2), rw = mkrs(br ? wB : wA, D_ * 3 * D_ * 2), rws = mkrs(ws, WS_END);
  STAMP(0);
  const unsigned xdst = lds0 + w * 4096, wdst = lds0 + LDS_W + w * 1024;
#define DMA_X(t, st) do { const unsigned so_ = (t) * 128u, d_ = xdst + (st); \
    dma16x(d_, xv0, rx, so_); dma16x(d_ + 1024, xv1, rx, so_); dma16x(d_ + 2048, xv0, rx, so_ + 32768); dma16x(d_ + 3072, xv1, rx, so_ + 32768); } while (0)
#define DMA_W(t, st) do { const unsigned so_ = (t) * (64u * 6144u), d_ = wdst + (st); \
    dma16w(d_, wv, rw, so_); dma16w(d_ + 8192, wv, rw, so_ + 2048); dma16w(d_ + 16384, wv, rw, so_ + 4096); } while (0)
  DMA_W(0, 0); DMA_X(0, 0);
  const unsigned fl_id = l < 32 ? G * 16 + (l >> 1) : (l == 32 ? L - 16 : L);
  const bool fl_ok = l < 34 && !(l == 32 && R == 0);
  unsigned obase = ld32_issue(rws, fl_ok ? WS_FLAG + fl_id * 128 : OOB);
  DMA_W(1, WST); DMA_X(1, XST);
  const unsigned pli = l < 13 ? l : 0;
  const unsigned pwl = ((idx & 3) * 8 + w) * 6 + (pli - 4);
  const unsigned pq = (idx * 8 + w) * 3 + (pli - 10);
  const char* pfp = pli < 4 ? (const char*)(br ? xB : xA) + (R * 256 + ((idx >> 2) * 8 + w) * 4 + pli) * 2048 + (PFD - 1) * 128
                  : pli < 10 ? (const char*)(br ? wB : wA) + (pwl / 3 + (PFD - 1) * 64) * 6144 + (pwl % 3) * 2048 + hp * 128
                             : (const char*)(br ? woB : woA) + (pq >> 3) * 2048 + loc * 1024 + (pq & 7) * 128;
  unsigned pstep = pli < 4 ? 128u : pli < 10 ? 64u * 6144u : (unsigned)((1023 - 95) / (16 - PFD) < 96 ? (1023 - 95) / (16 - PFD) : 96) * 2048u;
  if (pli >= 10) { pfp = (const char*)xA + 1280 * 128; pstep = 0; }
  pf4(lds0 + LDS_CNT + 256, pfp); pfp += pstep;
#define CODEPF(bytes) do { unsigned long long pc; asm volatile("s_getpc_b64 %0" : "=s"(pc));     \
    const unsigned co = (w * 64 + l) * 128; \
    pf4(lds0 + LDS_CNT + 256, (const char*)(pc & ~127ull) + (co < (bytes) ? co : 0)); } while (0)
  if (tid < 8) *(LAS unsigned*)(size_t)(lds0 + LDS_CNT + tid * 4) = 0;
  const unsigned c = l & 15, g = l >> 4, q4 = c >> 2;
  const unsigned xa_ = lds0 + mp * 8192 + c * 128 + ((g ^ (c >> 1)) << 4);
  const unsigned wa_ = lds0 + LDS_W + (8 * g + q4) * 128 + (((2 * wn) ^ ((q4 >> 1) | ((g & 1) << 1))) << 5) + (l & 3) * 8;
  f32x4 acc[6][4] = {};
  u32x4 mk0, mk1; unsigned sel1 = ((c & 3) == 0 && ((c >> 2) & 1) == (g & 1)) ? 0x3F803F80u : 0u;
  { const int rr = (int)(l & 31) - 4 * (int)(l >> 5);
#pragma unroll
    for (int i = 0; i < 4; ++i) {
      mk0[i] = (keyidx(2 * i) <= rr ? 0xFFFFu : 0u) | (keyidx(2 * i + 1) <= rr ? 0xFFFF0000u : 0u);
      mk1[i] = (keyidx(8 + 2 * i) <= rr ? 0xFFFFu : 0u) | (keyidx(9 + 2 * i) <= rr ? 0xFFFF0000u : 0u); }
    PIN(mk0); PIN(mk1); PIN(sel1); }
  f32x16 oa, ob; f32x4 Ls; u32x4 p0, p1; bf16x8 v0, v1, sel;
  Frag A, B;
#define RDX(F, xb, s, u) F.x[u] = lds128(((xb) ^ ((s) << 6)) + (u) * 2048)
#define MM(C, n, u) do { const bf16x8 wf_ = cat(C.wl[n], C.wh[n]); if ((n) < 4) MFA(acc[n][u], wf_, C.x[u]); else MFA(acc[n][u], C.x[u], wf_); } while (0)
#define RDWL(F, wb, s, n) F.wl[n] = ldstr(((wb) ^ (((n) & 1) << 5)) + ((n) >> 1) * 8192 + (s) * 4096)
#define RDWH(F, wb, s, n) F.wh[n] = ldstr(((wb) ^ (((n) & 1) << 5)) + ((n) >> 1) * 8192 + (s) * 4096 + 512)
#define RD1(F, xb, wb, s, r) do { if ((r) == 0) RDX(F, xb, s, 0); else if ((r) == 1) RDWL(F, wb, s, 0); else if ((r) == 2) RDWH(F, wb, s, 0); \
    else if ((r) < 6) RDX(F, xb, s, ((r) - 2) & 3); else if (((r) & 1) == 0) RDWL(F, wb, s, (((r) + 8) >> 1) % 6); else RDWH(F, wb, s, (((r) + 8) >> 1) % 6); } while (0)
#define PB 27
#define PFP 36
#define BP(r) ((r) * 3 / 2)
#define AP(r) (29 + (r) * 6 / 5)
#define XP(j) (2 + (j) * 6)
#define WP(j) (28 + (j) * 3)
#define TILE(LDA, DMA, WAITB) do { \
    const unsigned xb = xa + xs, wb = wa + wsx, wd = wdst + wsx; \
    xp = xs; xs = (xs == 2 * XST) ? 0 : xs + XST; wsx ^= WST; \
    const unsigned xn = xa + xs, wn_ = wa + wsx; \
    sfor<0, 48>([&](auto mi) AI { constexpr int m_ = decltype(mi)::v; \
      if constexpr (m_ < 24) MM(A, m_ >> 2, m_ & 3); else MM(B, (m_ - 24) >> 2, m_ & 3); \
      sfor<0, 16>([&](auto ri) AI { constexpr int r_ = decltype(ri)::v; \
        if constexpr (BP(r_) == m_) RD1(B, xb, wb, 1, r_); if constexpr (LDA && AP(r_) == m_) RD1(A, xn, wn_, 0, r_); }); \
      sfor<0, 4>([&](auto ji) AI { constexpr int j_ = decltype(ji)::v; \
        if constexpr (DMA && XP(j_) == m_) dma16x(xd + j_ * 1024, (j_ & 1) ? xv1 : xv0, rx, xso + (j_ >> 1) * 32768); \
        if constexpr (DMA && j_ < 3 && WP(j_) == m_) dma16w(wd + j_ * 8192, wv, rw, wso + j_ * 2048); }); \
      if constexpr (PFI && DMA && m_ == PFP) { pf4(lds0 + LDS_CNT + 256, pfp); pfp += pinc; } \
      if constexpr (!LDA) {     \
        if constexpr (m_ == 28) { oa = f32x16{}; PIN(oa); } if constexpr (m_ == 34) { ob = f32x16{}; PIN(ob); } \
        if constexpr (m_ == 40) { p0 = u32x4{}; p1 = u32x4{}; v0 = bf16x8{}; v1 = bf16x8{}; PIN(p0); PIN(p1); PIN(v0); PIN(v1); } \
        if constexpr (m_ == 44) { Ls = f32x4{}; sel = asb(u32x4{sel1, sel1, sel1, sel1}); PIN(Ls); PIN(sel); } } \
      SBAR(); \
      if constexpr (m_ == PB) { WAITB; SBAR(); } }); } while (0)
  const bool ack_ok = l < 17 && !(l == 16 && R == 7);
  unsigned ackoff = ack_ok ? WS_ACK + L * 128 + l * 4 : OOB;
  const char* codep;
  { unsigned long long pc; asm volatile("s_getpc_b64 %0" : "=s"(pc)); const unsigned co = (w * 64 + l) * 128; codep = (const char*)(pc & ~127ull) + (co < CODE_BYTES ? co : 0); }
  unsigned ackdst = (fl_ok && l <= 32 && !(l & 1)) ? WS_ACK + fl_id * 128 + (l < 32 ? ns : 16) * 4 : OOB;
  unsigned xa = xa_, wa = wa_;
  PIN(ackoff); PIN(codep); PIN(ackdst); PIN(xa); PIN(wa); PIN(pfp); PIN(pstep);
  _Pragma("unroll") for (int i_ = 0; i_ < 24; ++i_) PIN(acc[i_ >> 2][i_ & 3]);
  WAIT_BAR_V(8, obase);
  STAMP(1);
  unsigned xs = 0, wsx = 0, xp = 2 * XST;
  sfor<0, 16>([&](auto ri) AI { constexpr int r_ = decltype(ri)::v; RD1(A, xa, wa, 0, r_); SBAR(); });
  if (w == 0) st32(obase ^ ACKX, rws, ackdst);
  const unsigned pbase = __builtin_amdgcn_readlane(obase, 32), mybase = __builtin_amdgcn_readlane(obase, 33);
  SBAR();
#pragma unroll 1
  for (int t = 0; t < 15; ++t) {
    const unsigned xso = t < 14 ? (t + 2) * 128u : 0x80000000u, wso = t < 14 ? (t + 2) * (64u * 6144u) : OOB;
    const unsigned xd = t < 14 ? xdst + xp : lds0 + LDS_SPARE;
    if (__builtin_expect((unsigned)(t - (15 - PFD)) < 2u, 0)) {
      if (t == 15 - PFD) {
        dma4_sc1(lds0 + LDS_CTL + w * 256, ackoff, rws, 0);
        pf4(lds0 + LDS_CNT + 256, codep);
      } else {
        pfp = (const char*)(br ? woB : woA) + ((idx * 8 + w) * 4 + ((l >> 3) & 3)) * 2048 + loc * 1024 + (l & 7) * 128; pstep = 0;
      }
    }
    const unsigned pinc = pstep;
    TILE(true, true, WAIT_BAR(5));
  }
  bool acked = __all(!ack_ok || *(volatile LAS unsigned*)(size_t)(lds0 + LDS_CTL + w * 256 + l * 4) == (mybase ^ ACKX));
  __builtin_amdgcn_s_setprio(2);
  { const unsigned xso = 0, wso = 0, xd = 0, pinc = 0; TILE(false, false, (void)0); }
  asm volatile("s_nop 7" ::: "memory");
  _Pragma("unroll") for (int i_ = 0; i_ < 24; ++i_) PIN(acc[i_ >> 2][i_ & 3]);
  STAMP(2);
  bf16x8 qa0, qa1, qb0, qb1;
  f32x16 sa, sb, na, nb;
  {
    const unsigned ko = (g & 1) * 1024 + ((g >> 1) * 32 + (c & 7) + 16 * (c >> 3)) * 16;
    const unsigned vo = IMG + (g >> 1) * 1024 + ((g & 1) * 32 + c) * 16;
    const unsigned t0 = wn * 32768 + mp * 2 * TIL;
    const unsigned gs = R < 7 ? WS_KV + L * 65536 + t0 : OOB;
#pragma unroll
    for (int u = 0; u < 4; ++u) {
      const unsigned tb = (u >> 1) * TIL;
      const u32x4 kk = pack44(acc[2][u], acc[3][u]), vv = pack44(acc[4 + (u & 1)][u & 2], acc[4 + (u & 1)][(u & 2) + 1]);
      __builtin_amdgcn_raw_buffer_store_b128(kk, rws, ko + (u & 1) * 128 + tb, gs, SC1);
      __builtin_amdgcn_raw_buffer_store_b128(vv, rws, vo + (u & 1) * 256 + tb, gs, SC1);
      sts128(lds0 + LDS_OWN + t0 + tb + ko + (u & 1) * 128, kk);
      sts128(lds0 + LDS_OWN + t0 + tb + vo + (u & 1) * 256, vv);
    }
    lds_inc(lds0 + LDS_CNT + wn * 4);
    if (mp < 2) __builtin_amdgcn_s_setprio(1);
    u32x4 qf[4]; float sd[4];
#pragma unroll
    for (int u = 0; u < 4; ++u) {
      const f32x4 qx = acc[0][u] * QSCALE, qy = acc[1][u] * QSCALE;
      qf[u] = pack44(qx, qy);
      float d = qx[0] * acc[2][u][0];
#pragma unroll
      for (int i = 1; i < 4; ++i) d = __builtin_fmaf(qx[i], acc[2][u][i], d);
#pragma unroll
      for (int i = 0; i < 4; ++i) d = __builtin_fmaf(qy[i], acc[3][u][i], d);
      sd[u] = d;
    }
#pragma unroll
    for (int p = 0; p < 2; ++p) {
#pragma unroll
      for (int i = 0; i < 4; ++i) { auto r = __builtin_amdgcn_permlane16_swap(qf[2 * p][i], qf[2 * p + 1][i], false, false); qf[2 * p][i] = r[0]; qf[2 * p + 1][i] = r[1]; }
      auto r = __builtin_amdgcn_permlane16_swap(__float_as_uint(sd[2 * p]), __float_as_uint(sd[2 * p + 1]), false, false);
      sd[2 * p] = -swapsum(__uint_as_float(r[0]) + __uint_as_float(r[1]));
    }
    qb0 = asb(qf[0]); qb1 = asb(qf[1]); qa0 = asb(qf[2]); qa1 = asb(qf[3]);
    { const float one = l < 32 ? 1.f : 0.f;
      asm volatile("s_nop 1\n\tv_mfma_f32_32x32x2_f32 %0, %2, %3, 0\n\tv_mfma_f32_32x32x2_f32 %1, %2, %4, 0" : "=&v"(na), "=&v"(nb) : "v"(one), "v"(sd[2]), "v"(sd[0])); }
  }
  float la = 0.f, lb = 0.f;
  bf16x8 k0, k1;
  PIN(qa0); PIN(qa1); PIN(qb0); PIN(qb1);
  STAMP(3);
  const __amdgpu_buffer_rsrc_t rwo = mkrs(br ? woB : woA, D_ * D_ * 2);
  const unsigned wov = (l >> 3) * 2048 + (((l & 7) ^ (((l >> 4) & 3) << 1)) << 4) + ns * 128;
#define WOB(gq) ((gq) == 0 ? 131072u : ((gq) & 3) == 3 ? 0u : (gq) * 32768u)
#define DMA_WO(j, ww) dma16(lds0 + WOB((j) >> 2) + ((j) & 3) * 8192 + (ww) * 1024, wov, rwo, ((j) ^ (loc * 8)) * (64u * 2048u) + (ww) * 16384u)
  const unsigned lb16 = l * 16, own = lds0 + LDS_OWN + wn * 32768, prv = R > 0 ? lds0 + wn * 98304 : own;
  const unsigned myflag = WS_FLAG + L * 128, pfoff = WS_FLAG + (L - 16) * 128 + 4 + wn * 4;
#define QK0(S, Q, N) asm volatile("v_mfma_f32_32x32x16_bf16 %0, %1, %2, %3" : "=&v"(S) : "v"(k0), "v"(Q), "v"(N), "v"(k1))
#define QK1(S, Q) asm volatile("v_mfma_f32_32x32x16_bf16 %0, %1, %2, %0" : "+v"(S) : "v"(k1), "v"(Q))
#define PV0(O) asm volatile("v_mfma_f32_32x32x16_bf16 %0, %1, %2, %0" : "+v"(O) : "v"(v0), "v"(p0), "v"(v1))
#define PV0S(O) asm volatile("s_nop 1\n\tv_mfma_f32_32x32x16_bf16 %0, %1, %2, %0" : "+v"(O) : "v"(v0), "v"(p0), "v"(v1))
#define PV1(O) asm volatile("v_mfma_f32_32x32x16_bf16 %0, %1, %2, %0" : "+v"(O) : "v"(v1), "v"(p1))
#define EX(S, i) do { S[i] = __builtin_amdgcn_exp2f(S[i]); } while (0)
#define RDK(a) do { k0 = lds128(a); k1 = lds128((a) + 1024); } while (0)
  unsigned pa = own + (2 * mp + 1) * TIL, pb = pa - TIL, pbp = pb;
  unsigned va = lb16 + pa, vb = lb16 + pb;
  RDK(va);
  asm volatile("s_nop 1" ::: "memory");
  QK0(sa, qa0, na); QK1(sa, qa1);
  RDK(vb);
  asm volatile("s_nop 7\n\ts_nop 1" ::: "memory");
  SBAR();
#define EXP(i) e_[i] = __builtin_amdgcn_exp2f(sx_[i])
#define HALF(SX, SY, NY, LY, OY, QY0, QY1, PY, RD, VX, KNEXT, BAR1, MSK, LSUM, PVF) do { \
    const f32x16 sx_ = SX; float e_[16]; u32x4 pn_; \
    EXP(0); EXP(1); EXP(2); EXP(3); EXP(4); asm volatile("" : "+v"(e_[0]), "+v"(e_[1]), "+v"(e_[2]), "+v"(e_[3]), "+v"(e_[4])); SBAR();     \
    EXP(5); asm volatile("" : "+v"(e_[5]), "+v"(Ls)); SBAR(); \
    float t_ = LY + Ls[0]; unsigned long long bad_ = (BAR1) ? 0ull : __builtin_amdgcn_ballot_w64(sum_bad(t_)); asm volatile("" : "+s"(bad_), "+v"(t_)); SBAR(); \
    pn_[0] = pk2(e_[0], e_[1]); pn_[1] = pk2(e_[2], e_[3]); pn_[2] = pk2(e_[4], e_[5]); EXP(6); \
    asm volatile("" : "+v"(pn_[0]), "+v"(pn_[1]), "+v"(pn_[2]), "+v"(e_[6])); SBAR(); \
    if (!(BAR1) && __builtin_expect(bad_ != 0, 0)) { redo_tile(PY, QY0, QY1, sel, OY, NY, LY, p0, p1, Ls); t_ = LY + Ls[0]; } \
    LY = t_; \
    PVF(OY); SBAR(); \
    EXP(7); EXP(8); pn_[3] = pk2(e_[6], e_[7]); asm volatile("" : "+v"(pn_[3]), "+v"(e_[8])); SBAR(); \
    PV1(OY); SBAR(); if (RD) { v0 = lds128((VX) + IMG); v1 = lds128((VX) + IMG + 1024); VX = KNEXT; } \
    EXP(9); EXP(10); asm volatile("" : "+v"(e_[9]), "+v"(e_[10]), "+v"(VX)); SBAR(); \
    QK0(SY, QY0, NY); SBAR(); EXP(11); EXP(12); EXP(13); asm volatile("" : "+v"(e_[11]), "+v"(e_[12]), "+v"(e_[13])); SBAR(); \
    QK1(SY, QY1); SBAR(); \
    EXP(14); EXP(15); asm volatile("" : "+v"(e_[14]), "+v"(e_[15])); SBAR(); \
    if (BAR1) { if (PFI) WAITV(9); else WAITV(8); lds_inc(lds0 + LDS_CNT + 8); if (mp) lds_wait(lds0 + LDS_CNT + wn * 4, 4); } \
    if (RD) RDK(VX); SBAR(); \
    p0 = pn_; p1[0] = pk2(e_[8], e_[9]); p1[1] = pk2(e_[10], e_[11]); p1[2] = pk2(e_[12], e_[13]); p1[3] = pk2(e_[14], e_[15]); SBAR(); \
    if (MSK) { p0 &= mk0; p1 &= mk1; PIN(p0); PIN(p1); asm volatile("s_nop 0"); } \
    LSUM; SBAR(); } while (0)
#define LMF0 "v_mfma_f32_16x16x32_bf16 %0, %1, %2, 0\n\t"
#define LMF1 "v_mfma_f32_16x16x32_bf16 %0, %1, %3, %0\n\t"
#define LSUM_G() asm volatile(LMF0 "s_nop 0\n\t" LMF1 "s_nop 1" : "=&v"(Ls) : "v"(sel), "v"(p0), "v"(p1))
#define LSUM_A() asm volatile(LMF0 "s_nop 0\n\t" LMF1 "s_nop 0" : "=&v"(Ls) : "v"(sel), "v"(p0), "v"(p1))
#define LSUM_B() asm volatile("v_mfma_f32_16x16x32_bf16 %0, %3, %4, 0\n\ts_mov_b32 %1, %2\n\tv_mfma_f32_16x16x32_bf16 %0, %3, %5, %0\n\ts_add_i32 %2, %2, 0x1000" : "=&v"(Ls), "=&s"(pbp), "+s"(pa) : "v"(sel), "v"(p0), "v"(p1) : "scc")
  unsigned sent = 0;

#define REPF() do {} while (0)
#define SIGNAL() do { WAITV(0); \
    _Pragma("unroll 1") for (unsigned sp = 0; !acked && sp < SPIN_CAP; ++sp) { __builtin_amdgcn_s_sleep(2); acked = __all(!ack_ok || ld32_wait(rws, ackoff) == (mybase ^ ACKX)); } \
    if (lds_inc_rtn(lds0 + LDS_CNT + 24 + wn * 4) == 3 && l == 0) st32(mybase + 1, rws, myflag + 4 + wn * 4); \
    REPF(); \
    sent = 1; if (mp >= 2) __builtin_amdgcn_s_setprio(0); STAMP(13); } while (0)
#define TRYSIG() do { if (__builtin_expect(!sent, 0)) { if ((__builtin_amdgcn_s_getreg(7 | (31 << 11)) & 0x00C0000Fu) == 0) SIGNAL(); } } while (0)
#define PAIR(PAN, PBN, MD, FIRST) do { const unsigned pan = (PAN), pbn = (PBN); \
      HALF(sa, sb, nb, lb, ob, qb0, qb1, pbp, true, va, lb16 + pan, FIRST, (MD) != 0, LSUM_G(), PV0S); \
      HALF(sb, sa, na, la, oa, qa0, qa1, pa | (MD), true, vb, lb16 + pbn, false, (MD) != 0, LSUM_G(), PV0S); \
      pbp = pb | (MD); pa = pan; pb = pbn; } while (0)
#define HOT(N, SIG) do { _Pragma("unroll 1") for (int n = (N); n > 0; --n) {     \
      if (SIG) TRYSIG(); \
      HALF(sa, sb, nb, lb, ob, qb0, qb1, pbp, true, va, va + TIL, false, false, LSUM_A(), PV0); \
      HALF(sb, sa, na, la, oa, qa0, qa1, pa, false, vb, 0, false, false, LSUM_B(), PV0); } } while (0)
  PAIR(own, own, 1u, true);
  HOT(2 * mp, true);
  {
    STAMP(4);
    if (!sent) SIGNAL();
    STAMP(5);
    mk0 = ~mk0; mk1 = ~mk1; PIN(mk0); PIN(mk1);
    if (mp < 2) {
      lds_wait(lds0 + LDS_CNT + 8, 8);
      unsigned h_ = wn; asm volatile("" : "+s"(h_));
      if (mp) {
#pragma unroll
        for (int j = 0; j < 15; ++j) DMA_WO(j >> 2, h_ * 4 + (j & 3));
        if (!h_) DMA_WO(3, 3);
      } else if (R > 0) {
#pragma unroll 1
        for (unsigned sp = 0; sp < SPIN_CAP; ++sp) {
          if (rfl(ld32_wait(rws, pfoff)) == pbase + 1) break;
          __builtin_amdgcn_s_sleep(KVS);
        }
        STAMP(14);
#pragma unroll
        for (int j = 0; j < 8; ++j) dma64_sc1(lds0 + h_ * 98304 + j * TIL, lb16, rws, WS_KV + (L - 16) * 65536 + h_ * 32768 + j * TIL);
        WAITV(12);
        lds_inc(lds0 + LDS_CNT + 12 + h_ * 4);
      }
    }
    STAMP(6);
    if (R > 0) lds_wait(lds0 + LDS_CNT + 12 + wn * 4, mp < 2 ? 1 : 2);
    STAMP(7);
    pb = prv + (2 * mp + 1) * TIL; vb = lb16 + pb;
    RDK(vb);
    SBAR();
  }
  PAIR(prv + (2 * mp + 1) * TIL, prv + 2 * mp * TIL, 0u, false);
  lds_inc(lds0 + LDS_CNT + 20);
  if (R > 0) {
    if (mp == 0) { WAITV(0); lds_inc(lds0 + LDS_CNT + 12 + wn * 4); }
    PAIR(prv + (2 * mp + 2) * TIL, prv + (2 * mp + 2) * TIL, 2u, false);
    if (mp == 1) lds_wait(lds0 + LDS_CNT + 12 + wn * 4, 2);
    HOT(6 - 2 * mp, false);
    asm volatile("s_nop 7" : "+v"(Ls) :: "memory");
    if (__builtin_expect(__any(sum_bad(lb + Ls[0])), 0)) redo_tile(pbp, qb0, qb1, sel, ob, nb, lb, p0, p1, Ls);
    lb += Ls[0];
    PV0S(ob); PV1(ob);
  } else {
    asm volatile("s_nop 7" : "+v"(Ls), "+v"(sa) :: "memory");
  }
  __builtin_amdgcn_s_setprio(0);
  STAMP(8);
  { const float ia = __builtin_amdgcn_rcpf(la), ib = __builtin_amdgcn_rcpf(lb);
    oa *= ia;
    const unsigned hh = hp * 2 + wn, T0 = br * 64 + R * 8 + 2 * mp;
    const unsigned ol = ((l >> 4) & 1) * 1024 + ((l >> 5) * 16 + (l & 15)) * 16;
    __builtin_amdgcn_raw_buffer_store_b128(pack8(oa, 0), rws, ol, WS_O + (T0 * 32 + 32 + hh) * IMG, SC1);
    __builtin_amdgcn_raw_buffer_store_b128(pack8(oa, 1), rws, ol + 512, WS_O + (T0 * 32 + 32 + hh) * IMG, SC1);
    SBAR();
    asm volatile("s_nop 7" : "+v"(ob) :: "memory");
    ob *= ib;
    __builtin_amdgcn_raw_buffer_store_b128(pack8(ob, 0), rws, ol, WS_O + (T0 * 32 + hh) * IMG, SC1);
    __builtin_amdgcn_raw_buffer_store_b128(pack8(ob, 1), rws, ol + 512, WS_O + (T0 * 32 + hh) * IMG, SC1); }
  WAITV(0);
  STAMP(9);
  asm volatile("s_barrier" ::: "memory");
  if (tid == 64) st32(mybase + 2, rws, myflag);
  dma4(lds0 + LDS_WOX + 1024, l < 32 ? (l >> 4) * 65536 + (l & 15) * 128 : OOB, rws, WS_O + ((br * 64 + R * 8 + 2 * mp) * 32 + hp * 2 + wn) * IMG);
  if (w == 7) DMA_WO(3, 7);
  const unsigned fo = l < 32 ? WS_FLAG + (G * 16 + (l >> 1)) * 128 : OOB;
#pragma unroll
  for (int j = 4; j < 16; ++j) DMA_WO(j, w);
  if (w == POLLW) {
    unsigned f;
#pragma unroll 1
    for (unsigned sp = 0; sp < SPIN_CAP; ++sp) {
      f = ld32_wait(rws, fo);
      if (__all((l >> 4) != loc || f == obase + 2)) break;
      __builtin_amdgcn_s_sleep(POLLS);
    }
    *(volatile LAS unsigned*)(size_t)(lds0 + LDS_WOX) = __all(l >= 32 || f == obase + 2);
  }
  WAIT_BAR(0);
  STAMP(10);
  const unsigned T = G * 8 + w;
  const unsigned obs = WS_O + T * 32 * IMG;
  const unsigned wa2 = lds0 + (16 * (g >> 1) + 4 * (g & 1) + q4) * 128 + ((2 * (g & 1) + (q4 >> 1)) << 5) + (l & 3) * 8;
  f32x4 oc[4][2] = {};
  u32x4 of[16];
  s16x4 wl[2][4], wh[2][4];
#pragma unroll
  for (int i = 0; i < 16; ++i) {
    of[i] = __builtin_amdgcn_raw_buffer_load_b128(rws, l * 16 + (i & 3) * 1024, obs + loc * 32768 + (i >> 2) * 4096, SC1);
    if (i == FILL0 - 1 || i == FILL1 - 1) { SBAR(); __builtin_amdgcn_s_sleep(FILLS); SBAR(); }
  }
  const unsigned allrdy = rfl(*(volatile LAS unsigned*)(size_t)(lds0 + LDS_WOX));
#pragma unroll
  for (int nt = 0; nt < 4; ++nt) { wl[0][nt] = ldstr((wa2 ^ (nt << 5)) + WOB(0)); wh[0][nt] = ldstr((wa2 ^ (nt << 5)) + WOB(0) + 1024); }
  _Pragma("unroll") for (int i_ = 0; i_ < 8; ++i_) PIN(oc[i_ >> 1][i_ & 1]);
  asm volatile("s_nop 1" ::: "memory");
  SBAR();
#pragma unroll 1
  for (int gq = 0; gq < 4; ++gq) {
    const unsigned wb = wa2 + WOB(gq), wbn = wa2 + WOB(gq + 1);
    if (__builtin_expect(gq == 1 && !allrdy, 0)) {
      if (w == POLLW) {
#pragma unroll 1
        for (unsigned sp = 0; sp < SPIN_CAP; ++sp) { if (__all(l >= 32 || ld32_wait(rws, fo) == obase + 2)) break; __builtin_amdgcn_s_sleep(POLLS); }
      }
      asm volatile("s_barrier" ::: "memory");
    }
    const unsigned on = gq < 3 ? obs + ((gq + 1) ^ (loc * 2)) * 16384 : OOB;
    if (gq == 0) __builtin_amdgcn_s_setprio(3); else if (gq == 1) __builtin_amdgcn_s_setprio(2); else if (gq == 2) __builtin_amdgcn_s_setprio(1); else __builtin_amdgcn_s_setprio(0);
    sfor<0, 8>([&](auto hi) AI { constexpr int h_ = decltype(hi)::v, cur = h_ & 1, nxt = cur ^ 1;
      sfor<0, 8>([&](auto mi) AI { constexpr int m_ = decltype(mi)::v, nt = m_ >> 1, tt = m_ & 1;
        { const bf16x8 wf = cat(wl[cur][nt], wh[cur][nt]);
          if constexpr (m_ == 0) {
            const bf16x8 w3 = cat(wl[cur][3], wh[cur][3]);
            asm volatile("v_mfma_f32_16x16x32_bf16 %0, %1, %2, %0" : "+v"(oc[0][0]) : "v"(wf), "v"(of[2 * h_]), "v"(w3), "v"(of[2 * h_ + 1]));
          } else MFA(oc[nt][tt], wf, of[2 * h_ + tt]); }
        sfor<0, 8>([&](auto ri) AI { constexpr int r_ = decltype(ri)::v, rn = r_ >> 1;
          if constexpr (G2R(r_) == m_) {
            const unsigned wq_ = ((h_ < 7 ? wb : wbn) ^ (rn << 5)) + ((h_ + 1) & 7) * 4096;
            if constexpr ((r_ & 1) == 0) wl[nxt][rn] = ldstr(wq_); else wh[nxt][rn] = ldstr(wq_ + 1024); } });
        if constexpr (m_ >= 6) of[2 * h_ + tt] = __builtin_amdgcn_raw_buffer_load_b128(rws, l * 16 + ((2 * h_ + tt) & 3) * 1024, on + ((2 * h_ + tt) >> 2) * 4096, SC1);
        SBAR(); }); });
  }
  asm volatile("s_nop 7" ::: "memory");
  _Pragma("unroll") for (int i_ = 0; i_ < 8; ++i_) PIN(oc[i_ >> 1][i_ & 1]);
  STAMP(11);
  const __amdgpu_buffer_rsrc_t rout = mkrs(out, S_ * 2 * D_ * 2);
  const unsigned orow = ((R * 8 + w) * 32 + c) * 4096 + br * 2048 + ns * 128 + (g & 1) * 32 + (g >> 1) * 16;
  const unsigned orow2 = orow - (c & 8) * 4096 + (c & 8) * 8;
#pragma unroll
  for (int tt = 0; tt < 2; ++tt) {
    u32x4 v[2], va_, vb_;
#pragma unroll
    for (int np = 0; np < 2; ++np) {
      const f32x4 &a = oc[2 * np][tt], &b = oc[2 * np + 1][tt];
      auto r0 = __builtin_amdgcn_permlane16_swap(pk2(a[0], a[1]), pk2(b[0], b[1]), false, false);
      auto r1 = __builtin_amdgcn_permlane16_swap(pk2(a[2], a[3]), pk2(b[2], b[3]), false, false);
      v[np] = u32x4{r0[0], r1[0], r0[1], r1[1]};
    }
#pragma unroll
    for (int i = 0; i < 4; ++i) {
      const unsigned y = c < 8 ? v[1][i] : v[0][i];
      const unsigned r = (unsigned)__builtin_amdgcn_update_dpp(0, (int)y, 0x128, 0xf, 0xf, false);
      va_[i] = c < 8 ? v[0][i] : r; vb_[i] = c < 8 ? r : v[1][i];
    }
    __builtin_amdgcn_raw_buffer_store_b128(va_, rout, orow2 + tt * 65536, 0, OUTPOL);
    __builtin_amdgcn_raw_buffer_store_b128(vb_, rout, orow2 + tt * 65536 + 32768, 0, OUTPOL);
  }
  { WAITV(0); STAMP(12); const unsigned long long te = dg0 + 6000 + 2 * (dg1 - dg0); while (__builtin_amdgcn_s_memrealtime() < te) __builtin_amdgcn_s_sleep(1); }
}

extern "C" void kernel_launch(void* const* d_in, const int* in_sizes, int n_in, void* d_out, int out_size, void* d_ws, size_t ws_size,
                              hipStream_t stream) {
  const size_t wsoff = (ws_size - WS_END) & ~(size_t)0xfffff;
  k_fused<<<256, 512, 0, stream>>>(d_in[0], d_in[1], d_in[2], d_in[3], d_in[4], d_in[5], (char*)d_ws + wsoff, d_out);
}
```

```cpp
#define POLLS 8
#include <hip/hip_runtime.h>
#include <hip/hip_bf16.h>

typedef short bf16x8 __attribute__((ext_vector_type(8)));
typedef short s16x4 __attribute__((ext_vector_type(4)));
typedef float f32x16 __attribute__((ext_vector_type(16)));
typedef float f32x4 __attribute__((ext_vector_type(4)));
typedef unsigned u32x4 __attribute__((ext_vector_type(4)));
typedef __bf16 bf16x2_t __attribute__((ext_vector_type(2)));
typedef float f32x2 __attribute__((ext_vector_type(2)));
#define LAS __attribute__((address_space(3)))
#define DI __device__ __forceinline__
#define AI __attribute__((always_inline))
#define MFMA(a, b, c) __builtin_amdgcn_mfma_f32_32x32x16_bf16((a), (b), (c), 0, 0, 0)
#define SBAR() __builtin_amdgcn_sched_barrier(0)
#define PIN(x) asm volatile("" : "+v"(x))
#define MFA(acc_, a_, b_) asm volatile("v_mfma_f32_16x16x32_bf16 %0, %1, %2, %0" : "+v"(acc_) : "v"(a_), "v"(b_))

constexpr int S_ = 2048, D_ = 1024;
constexpr float QSCALE = 0.17677669529663687f * 1.4426950408889634f;
constexpr unsigned SUM_LO = 0x2B800000u, SUM_HI = 0x53800000u;
constexpr unsigned IMG = 2048;
constexpr unsigned WS_FLAG = 0, WS_ACK = 32768, WS_KV = 65536, WS_O = WS_KV + 256u * 65536u, WS_END = WS_O + 2u * 64u * 32u * IMG;
constexpr unsigned ACKX = 0x5bd1e995u;
constexpr int SC1 = 16;
constexpr unsigned CODE_BYTES = 25600;
constexpr unsigned SPIN_CAP = 1u << 18;

#define HPMAP ((xcd & 1) * 8 + (idx >> 2))
#define RMAP (((xcd >> 1) & 1) * 4 + (idx & 3))
#define NOMEMX 1
template <int I> struct IC { static constexpr int v = I; };
template <int B, int E, class F> DI void sfor(F&& f) { if constexpr (B < E) { f(IC<B>{}); sfor<B + 1, E>(f); } }
DI unsigned rfl(unsigned x) { return (unsigned)__builtin_amdgcn_readfirstlane((int)x); }
DI unsigned pk2(float lo, float hi) { f32x2 v = {lo, hi}; return __builtin_bit_cast(unsigned, __builtin_convertvector(v, bf16x2_t)); }
DI u32x4 pack8(const f32x16& x, int s) {
  u32x4 p;
  p[0] = pk2(x[8 * s + 0], x[8 * s + 1]); p[1] = pk2(x[8 * s + 2], x[8 * s + 3]);
  p[2] = pk2(x[8 * s + 4], x[8 * s + 5]); p[3] = pk2(x[8 * s + 6], x[8 * s + 7]);
  return p;
}
DI u32x4 pack44(const f32x4& a, const f32x4& b) { u32x4 p; p[0] = pk2(a[0], a[1]); p[1] = pk2(a[2], a[3]); p[2] = pk2(b[0], b[1]); p[3] = pk2(b[2], b[3]); return p; }
DI bf16x8 asb(u32x4 v) { return __builtin_bit_cast(bf16x8, v); }
DI bf16x8 lds128(unsigned a) { return *(const LAS bf16x8*)(size_t)a; }
DI void sts128(unsigned a, u32x4 v) { *(LAS u32x4*)(size_t)a = v; }
DI s16x4 ldstr(unsigned a) { return __builtin_amdgcn_ds_read_tr16_b64_v4i16((LAS s16x4*)(size_t)a); }
DI bf16x8 cat(s16x4 lo, s16x4 hi) { return __builtin_shufflevector(lo, hi, 0, 1, 2, 3, 4, 5, 6, 7); }
DI void dma16(unsigned ldsdst, unsigned voff, __amdgpu_buffer_rsrc_t rs, unsigned soff) {
  asm volatile("s_mov_b32 m0, %0\n\ts_nop 0\n\tbuffer_load_dwordx4 %1, %2, %3 offen lds" ::"s"(ldsdst), "v"(voff), "s"(rs), "s"(soff) : "memory");
}
#define XPOL ""
#define WPOL ""
DI void dma16x(unsigned ldsdst, unsigned voff, __amdgpu_buffer_rsrc_t rs, unsigned soff) {
  asm volatile("s_mov_b32 m0, %0\n\ts_nop 0\n\tbuffer_load_dwordx4 %1, %2, %3 offen " XPOL " lds" ::"s"(ldsdst), "v"(voff), "s"(rs), "s"(soff) : "memory");
}
DI void dma16w(unsigned ldsdst, unsigned voff, __amdgpu_buffer_rsrc_t rs, unsigned soff) {
  asm volatile("s_mov_b32 m0, %0\n\ts_nop 0\n\tbuffer_load_dwordx4 %1, %2, %3 offen " WPOL " lds" ::"s"(ldsdst), "v"(voff), "s"(rs), "s"(soff) : "memory");
}
#define PPOL ""
#define OUTPOL 16
#define KVS 1
#define FILL0 4
#define FILL1 8
#define FILLS 4
#define G2R(r) ((r) * 3 / 4)
#define POLLW 0
#define PFMODE 0
#define PFI true
#define PFD 3
#define PFH 1
DI void pf4(unsigned ldsdst, const char* p) {
  asm volatile("s_mov_b32 m0, %0\n\ts_nop 0\n\tglobal_load_lds_dword %1, off " PPOL ::"s"(ldsdst), "v"(p) : "memory");
}
DI void dma16_sc1(unsigned ldsdst, unsigned voff, __amdgpu_buffer_rsrc_t rs, unsigned soff) {
  asm volatile("s_mov_b32 m0, %0\n\ts_nop 0\n\tbuffer_load_dwordx4 %1, %2, %3 offen sc1 lds" ::"s"(ldsdst), "v"(voff), "s"(rs), "s"(soff) : "memory");
}
#define WAITV(n) asm volatile("s_waitcnt vmcnt(" #n ")" ::: "memory")
#define WAIT_BAR(n) asm volatile("s_waitcnt vmcnt(" #n ") lgkmcnt(0)\n\ts_barrier" ::: "memory")
DI __amdgpu_buffer_rsrc_t mkrs(const void* p, unsigned bytes) { return __builtin_amdgcn_make_buffer_rsrc((void*)p, (short)0, (int)bytes, 0x00020000); }
DI unsigned ld32_issue(__amdgpu_buffer_rsrc_t rs, unsigned off) { unsigned r; asm volatile("buffer_load_dword %0, %1, %2, 0 offen sc1" : "=v"(r) : "v"(off), "s"(rs) : "memory"); return r; }
DI unsigned ld32_wait(__amdgpu_buffer_rsrc_t rs, unsigned off) { unsigned r; asm volatile("buffer_load_dword %0, %1, %2, 0 offen sc1\n\ts_waitcnt vmcnt(0)" : "=&v"(r) : "v"(off), "s"(rs) : "memory"); return r; }
DI u32x4 ld128_issue(__amdgpu_buffer_rsrc_t rs, unsigned off) { u32x4 r; asm volatile("buffer_load_dwordx4 %0, %1, %2, 0 offen sc1" : "=v"(r) : "v"(off), "s"(rs) : "memory"); return r; }
DI u32x4 ld128_wait(__amdgpu_buffer_rsrc_t rs, unsigned off) { u32x4 r; asm volatile("buffer_load_dwordx4 %0, %1, %2, 0 offen sc1\n\ts_waitcnt vmcnt(0)" : "=&v"(r) : "v"(off), "s"(rs) : "memory"); return r; }
DI void dma4(unsigned ldsdst, unsigned voff, __amdgpu_buffer_rsrc_t rs, unsigned soff) {
  asm volatile("s_mov_b32 m0, %0\n\ts_nop 0\n\tbuffer_load_dword %1, %2, %3 offen lds" ::"s"(ldsdst), "v"(voff), "s"(rs), "s"(soff) : "memory");
}
DI void dma4_sc1(unsigned ldsdst, unsigned voff, __amdgpu_buffer_rsrc_t rs, unsigned soff) {
  asm volatile("s_mov_b32 m0, %0\n\ts_nop 0\n\tbuffer_load_dword %1, %2, %3 offen sc1 lds" ::"s"(ldsdst), "v"(voff), "s"(rs), "s"(soff) : "memory");
}
#define WAIT_BAR_V(n, x) asm volatile("s_waitcnt vmcnt(" #n ") lgkmcnt(0)\n\ts_barrier" : "+v"(x) :: "memory")
constexpr unsigned OOB = 0x80000000u;
DI void st32(unsigned v, __amdgpu_buffer_rsrc_t rs, unsigned off) { __builtin_amdgcn_raw_buffer_store_b32(v, rs, off, 0, SC1); }

#define STAMP(i) do {} while (0)

constexpr unsigned XST = 32768, WST = 24576, LDS_W = 3 * XST, LDS_SPARE = LDS_W + 2 * WST, LDS_CTL = 159744;

constexpr unsigned LDS_OWN = 32768, LDS_CNT = 162816, LDS_WOX = 98304, LDS_BYTES = 163840, TIL = 2 * IMG;

struct Frag { bf16x8 x[4]; s16x4 wl[6], wh[6]; };

DI void lds_inc(unsigned a) { asm volatile("s_mov_b64 exec, 1\n\tds_add_u32 %0, %1\n\ts_mov_b64 exec, -1" ::"v"(a), "v"(1u) : "memory"); }
DI unsigned lds_inc_rtn(unsigned a) { unsigned r; asm volatile("s_mov_b64 exec, 1\n\tds_add_rtn_u32 %0, %1, %2\n\ts_mov_b64 exec, -1\n\ts_waitcnt lgkmcnt(0)" : "=&v"(r) : "v"(a), "v"(1u) : "memory"); return rfl(r); }
DI void lds_wait(unsigned a, unsigned n) {
#pragma unroll 1
  for (unsigned sp = 0; sp < 32u * SPIN_CAP && rfl(*(volatile LAS unsigned*)(size_t)a) < n; ++sp) __builtin_amdgcn_s_sleep(1);
  asm volatile("" ::: "memory");
}
DI void dma64_sc1(unsigned ldsdst, unsigned voff, __amdgpu_buffer_rsrc_t rs, unsigned soff) {
  asm volatile("s_mov_b32 m0, %0\n\ts_nop 0\n\tbuffer_load_dwordx4 %1, %2, %3 offen sc1 lds\n\tbuffer_load_dwordx4 %1, %2, %3 offen offset:1024 sc1 lds\n\t"
               "buffer_load_dwordx4 %1, %2, %3 offen offset:2048 sc1 lds\n\tbuffer_load_dwordx4 %1, %2, %3 offen offset:3072 sc1 lds" ::"s"(ldsdst), "v"(voff), "s"(rs), "s"(soff) : "memory");
}
DI float swapmax(float m) {
  auto rr = __builtin_amdgcn_permlane32_swap(__float_as_uint(m), __float_as_uint(m), false, false);
  return __builtin_fmaxf(__uint_as_float(rr[0]), __uint_as_float(rr[1]));
}
DI float swapsum(float m) {
  auto rr = __builtin_amdgcn_permlane32_swap(__float_as_uint(m), __float_as_uint(m), false, false);
  return __uint_as_float(rr[0]) + __uint_as_float(rr[1]);
}
#define MX3(a, b, c) __builtin_fmaxf(__builtin_fmaxf((a), (b)), (c))
DI float max16(const f32x16& p) {
  float a = MX3(p[0], p[1], p[2]), b = MX3(p[3], p[4], p[5]);
  a = MX3(a, p[6], p[7]); b = MX3(b, p[8], p[9]); a = MX3(a, p[10], p[11]); b = MX3(b, p[12], p[13]);
  a = MX3(a, p[14], p[15]);
  return __builtin_fmaxf(a, b);
}
DI bool sum_bad(float t) { return __float_as_uint(t) - SUM_LO > SUM_HI - SUM_LO; }
DI int keyidx(int i) { return (i & 3) + 16 * ((i >> 2) & 1) + 8 * (i >> 3); }
DI void redo_tile(unsigned tile, bf16x8 q0, bf16x8 q1, bf16x8 sel, f32x16& o, f32x16& negm, float& l, u32x4& p0, u32x4& p1, f32x4& L) {
  int ln = threadIdx.x & 63; PIN(ln);
  const unsigned ka = (tile & ~3u) + ln * 16, mode = tile & 3u;
  const bf16x8 k0 = lds128(ka), k1 = lds128(ka + 1024);
  const int rr = (ln & 31) - 4 * (ln >> 5);
  f32x16 s;
#pragma unroll
  for (int i = 0; i < 16; ++i) s[i] = (mode != 0 && (keyidx(i) <= rr) == (mode == 2)) ? -__builtin_inff() : negm[0];
  s = MFMA(k0, q0, s); s = MFMA(k1, q1, s);
  const float mx = swapmax(max16(s)), d = mode == 1 ? mx : __builtin_fmaxf(mx, 0.f), f = mode == 1 ? 0.f : __builtin_amdgcn_exp2f(-d), nm = negm[0] - d;
  l *= f;
#pragma unroll
  for (int i = 0; i < 16; ++i) { negm[i] = nm; o[i] *= f; s[i] = __builtin_amdgcn_exp2f(s[i] - d); }
  p0 = pack8(s, 0); p1 = pack8(s, 1);
  f32x4 z = {};
  L = __builtin_amdgcn_mfma_f32_16x16x32_bf16(sel, asb(p0), z, 0, 0, 0);
  L = __builtin_amdgcn_mfma_f32_16x16x32_bf16(sel, asb(p1), L, 0, 0, 0);
  float t = L[0]; PIN(t); L[0] = t;
  PIN(p0); PIN(p1); PIN(o); PIN(negm);
  asm volatile("s_nop 1" ::: "memory");
}

typedef unsigned long long u64x8 __attribute__((ext_vector_type(8)));
__global__ __launch_bounds__(512, 2) void k_fused(const void* xA_, const void* wA_, const void* woA_, const void* xB_, const void* wB_, const void* woB_,
                                                  void* ws_, void* out_) {
  u64x8 ka; asm volatile("s_load_dwordx16 %0, %1, 0x0" : "=&s"(ka) : "s"(__builtin_amdgcn_kernarg_segment_ptr()) : "memory");
  __shared__ __attribute__((aligned(1024))) char smem[LDS_BYTES];
  const unsigned lds0 = (unsigned)(size_t)(LAS char*)smem;
  const unsigned bid = blockIdx.x, xcd = bid & 7, idx = bid >> 3;
  const unsigned br = xcd >> 2, R = RMAP, hp = HPMAP;
  const unsigned L = (br * 8 + R) * 16 + hp;
  const unsigned G = br * 8 + R, ns = hp, loc = hp >> 3;
  const unsigned tid = threadIdx.x, l = tid & 63, w = rfl(tid >> 6), wm = w & 3, wn = w >> 2;
  const unsigned mtw = w, mp = wn ? 3 - wm : wm;
  unsigned xv0 = (R * 256 + mtw * 32 + (l >> 3)) * 2048 + (((l & 7) ^ (l >> 4)) << 4);
  unsigned xv1 = (R * 256 + mtw * 32 + 8 + (l >> 3)) * 2048 + (((l & 7) ^ (4 + (l >> 4))) << 4);
  unsigned wv = (8 * w + (l >> 3)) * 6144 + (((l & 7) ^ ((((l >> 4) & 1) | ((w & 1) << 1)) << 1)) << 4) + hp * 128;
  asm volatile("s_waitcnt lgkmcnt(0)" : "+s"(ka), "+v"(xv0), "+v"(xv1), "+v"(wv) :: "memory");
  const void *xA = (const void*)ka[0], *wA = (const void*)ka[1], *woA = (const void*)ka[2], *xB = (const void*)ka[3], *wB = (const void*)ka[4], *woB = (const void*)ka[5];
  void *ws = (void*)ka[6], *out = (void*)ka[7];
  const __amdgpu_buffer_rsrc_t rx = mkrs(br ? xB : xA, NOMEMX * S_ * D_ * 2), rw = mkrs(br ? wB : wA, D_ * 3 * D_ * 2), rws = mkrs(ws, WS_END);
  STAMP(0);
  const unsigned xdst = lds0 + w * 4096, wdst = lds0 + LDS_W + w * 1024;
#define DMA_X(t, st) do { const unsigned so_ = (t) * 128u, d_ = xdst + (st); \
    dma16x(d_, xv0, rx, so_); dma16x(d_ + 1024, xv1, rx, so_); dma16x(d_ + 2048, xv0, rx, so_ + 32768); dma16x(d_ + 3072, xv1, rx, so_ + 32768); } while (0)
#define DMA_W(t, st) do { const unsigned so_ = (t) * (64u * 6144u), d_ = wdst + (st); \
    dma16w(d_, wv, rw, so_); dma16w(d_ + 8192, wv, rw, so_ + 2048); dma16w(d_ + 16384, wv, rw, so_ + 4096); } while (0)
  DMA_W(0, 0); DMA_X(0, 0);
  const unsigned fl_id = l < 32 ? G * 16 + (l >> 1) : (l == 32 ? L - 16 : L);
  const bool fl_ok = l < 34 && !(l == 32 && R == 0);
  unsigned obase = ld32_issue(rws, fl_ok ? WS_FLAG + fl_id * 128 : OOB);
  DMA_W(1, WST); DMA_X(1, XST);
  const unsigned pli = l < 13 ? l : 0;
  const unsigned pwl = ((idx & 3) * 8 + w) * 6 + (pli - 4);
  const unsigned pq = (idx * 8 + w) * 3 + (pli - 10);
  const char* pfp = pli < 4 ? (const char*)(br ? xB : xA) + (R * 256 + ((idx >> 2) * 8 + w) * 4 + pli) * 2048 + (PFD - 1) * 128
                  : pli < 10 ? (const char*)(br ? wB : wA) + (pwl / 3 + (PFD - 1) * 64) * 6144 + (pwl % 3) * 2048 + hp * 128
                             : (const char*)(br ? woB : woA) + (pq >> 3) * 2048 + loc * 1024 + (pq & 7) * 128;
  unsigned pstep = pli < 4 ? 128u : pli < 10 ? 64u * 6144u : (unsigned)((1023 - 95) / (16 - PFD) < 96 ? (1023 - 95) / (16 - PFD) : 96) * 2048u;
  pf4(lds0 + LDS_CNT + 256, pfp); pfp += pstep;
#define CODEPF(bytes) do { unsigned long long pc; asm volatile("s_getpc_b64 %0" : "=s"(pc));     \
    const unsigned co = (w * 64 + l) * 128; \
    pf4(lds0 + LDS_CNT + 256, (const char*)(pc & ~127ull) + (co < (bytes) ? co : 0)); } while (0)
  if (tid < 8) *(LAS unsigned*)(size_t)(lds0 + LDS_CNT + tid * 4) = 0;
  const unsigned c = l & 15, g = l >> 4, q4 = c >> 2;
  const unsigned xa_ = lds0 + mp * 8192 + c * 128 + ((g ^ (c >> 1)) << 4);
  const unsigned wa_ = lds0 + LDS_W + (8 * g + q4) * 128 + (((2 * wn) ^ ((q4 >> 1) | ((g & 1) << 1))) << 5) + (l & 3) * 8;
  f32x4 acc[6][4] = {};
  u32x4 mk0, mk1; unsigned sel1 = ((c & 3) == 0 && ((c >> 2) & 1) == (g & 1)) ? 0x3F803F80u : 0u;
  { const int rr = (int)(l & 31) - 4 * (int)(l >> 5);
#pragma unroll
    for (int i = 0; i < 4; ++i) {
      mk0[i] = (keyidx(2 * i) <= rr ? 0xFFFFu : 0u) | (keyidx(2 * i + 1) <= rr ? 0xFFFF0000u : 0u);
      mk1[i] = (keyidx(8 + 2 * i) <= rr ? 0xFFFFu : 0u) | (keyidx(9 + 2 * i) <= rr ? 0xFFFF0000u : 0u); }
    PIN(mk0); PIN(mk1); PIN(sel1); }
  f32x16 oa, ob; f32x4 Ls; u32x4 p0, p1; bf16x8 v0, v1, sel;
  Frag A, B;
#define RDX(F, xb, s, u) F.x[u] = lds128(((xb) ^ ((s) << 6)) + (u) * 2048)
#define MM(C, n, u) do { const bf16x8 wf_ = cat(C.wl[n], C.wh[n]); if ((n) < 4) MFA(acc[n][u], wf_, C.x[u]); else MFA(acc[n][u], C.x[u], wf_); } while (0)
#define RDWL(F, wb, s, n) F.wl[n] = ldstr(((wb) ^ (((n) & 1) << 5)) + ((n) >> 1) * 8192 + (s) * 4096)
#define RDWH(F, wb, s, n) F.wh[n] = ldstr(((wb) ^ (((n) & 1) << 5)) + ((n) >> 1) * 8192 + (s) * 4096 + 512)
#define RD1(F, xb, wb, s, r) do { if ((r) == 0) RDX(F, xb, s, 0); else if ((r) == 1) RDWL(F, wb, s, 0); else if ((r) == 2) RDWH(F, wb, s, 0); \
    else if ((r) < 6) RDX(F, xb, s, ((r) - 2) & 3); else if (((r) & 1) == 0) RDWL(F, wb, s, (((r) + 8) >> 1) % 6); else RDWH(F, wb, s, (((r) + 8) >> 1) % 6); } while (0)
#define PB 27
#define PFP 36
#define BP(r) ((r) * 3 / 2)
#define AP(r) (29 + (r) * 6 / 5)
#define XP(j) (2 + (j) * 6)
#define WP(j) (28 + (j) * 3)
#define TILE(LDA, DMA, WAITB) do { \
    const unsigned xb = xa + xs, wb = wa + wsx, wd = wdst + wsx; \
    xp = xs; xs = (xs == 2 * XST) ? 0 : xs + XST; wsx ^= WST; \
    const unsigned xn = xa + xs, wn_ = wa + wsx; \
    sfor<0, 48>([&](auto mi) AI { constexpr int m_ = decltype(mi)::v; \
      if constexpr (m_ < 24) MM(A, m_ >> 2, m_ & 3); else MM(B, (m_ - 24) >> 2, m_ & 3); \
      sfor<0, 16>([&](auto ri) AI { constexpr int r_ = decltype(ri)::v; \
        if constexpr (BP(r_) == m_) RD1(B, xb, wb, 1, r_); if constexpr (LDA && AP(r_) == m_) RD1(A, xn, wn_, 0, r_); }); \
      sfor<0, 4>([&](auto ji) AI { constexpr int j_ = decltype(ji)::v; \
        if constexpr (DMA && XP(j_) == m_) dma16x(xd + j_ * 1024, (j_ & 1) ? xv1 : xv0, rx, xso + (j_ >> 1) * 32768); \
        if constexpr (DMA && j_ < 3 && WP(j_) == m_) dma16w(wd + j_ * 8192, wv, rw, wso + j_ * 2048); }); \
      if constexpr (PFI && DMA && m_ == PFP) { pf4(lds0 + LDS_CNT + 256, pfp); pfp += pinc; } \
      if constexpr (!LDA) {     \
        if constexpr (m_ == 28) { oa = f32x16{}; PIN(oa); } if constexpr (m_ == 34) { ob = f32x16{}; PIN(ob); } \
        if constexpr (m_ == 40) { p0 = u32x4{}; p1 = u32x4{}; v0 = bf16x8{}; v1 = bf16x8{}; PIN(p0); PIN(p1); PIN(v0); PIN(v1); } \
        if constexpr (m_ == 44) { Ls = f32x4{}; sel = asb(u32x4{sel1, sel1, sel1, sel1}); PIN(Ls); PIN(sel); } } \
      SBAR(); \
      if constexpr (m_ == PB) { WAITB; SBAR(); } }); } while (0)
  const bool ack_ok = l < 17 && !(l == 16 && R == 7);
  unsigned ackoff = ack_ok ? WS_ACK + L * 128 + l * 4 : OOB;
  const char* codep;
  { unsigned long long pc; asm volatile("s_getpc_b64 %0" : "=s"(pc)); const unsigned co = (w * 64 + l) * 128; codep = (const char*)(pc & ~127ull) + (co < CODE_BYTES ? co : 0); }
  unsigned ackdst = (fl_ok && l <= 32 && !(l & 1)) ? WS_ACK + fl_id * 128 + (l < 32 ? ns : 16) * 4 : OOB;
  unsigned xa = xa_, wa = wa_;
  PIN(ackoff); PIN(codep); PIN(ackdst); PIN(xa); PIN(wa); PIN(pfp); PIN(pstep);
  _Pragma("unroll") for (int i_ = 0; i_ < 24; ++i_) PIN(acc[i_ >> 2][i_ & 3]);
  WAIT_BAR_V(8, obase);
  STAMP(1);
  unsigned xs = 0, wsx = 0, xp = 2 * XST;
  sfor<0, 16>([&](auto ri) AI { constexpr int r_ = decltype(ri)::v; RD1(A, xa, wa, 0, r_); SBAR(); });
  if (w == 0) st32(obase ^ ACKX, rws, ackdst);
  const unsigned pbase = __builtin_amdgcn_readlane(obase, 32), mybase = __builtin_amdgcn_readlane(obase, 33);
  SBAR();
#pragma unroll 1
  for (int t = 0; t < 15; ++t) {
    const unsigned xso = t < 14 ? (t + 2) * 128u : 0x80000000u, wso = t < 14 ? (t + 2) * (64u * 6144u) : OOB;
    const unsigned xd = t < 14 ? xdst + xp : lds0 + LDS_SPARE;
    if (__builtin_expect((unsigned)(t - (15 - PFD)) < 2u, 0)) {
      if (t == 15 - PFD) {
        dma4_sc1(lds0 + LDS_CTL + w * 256, ackoff, rws, 0);
        pf4(lds0 + LDS_CNT + 256, codep);
      } else {
        pfp = (const char*)(br ? woB : woA) + ((idx * 8 + w) * 4 + ((l >> 3) & 3)) * 2048 + loc * 1024 + (l & 7) * 128; pstep = 0;
      }
    }
    const unsigned pinc = pstep;
    TILE(true, true, WAIT_BAR(5));
  }
  bool acked = __all(!ack_ok || *(volatile LAS unsigned*)(size_t)(lds0 + LDS_CTL + w * 256 + l * 4) == (mybase ^ ACKX));
  __builtin_amdgcn_s_setprio(2);
  { const unsigned xso = 0, wso = 0, xd = 0, pinc = 0; TILE(false, false, (void)0); }
  asm volatile("s_nop 7" ::: "memory");
  _Pragma("unroll") for (int i_ = 0; i_ < 24; ++i_) PIN(acc[i_ >> 2][i_ & 3]);
  STAMP(2);
  bf16x8 qa0, qa1, qb0, qb1;
  f32x16 sa, sb, na, nb;
  {
    const unsigned ko = (g & 1) * 1024 + ((g >> 1) * 32 + (c & 7) + 16 * (c >> 3)) * 16;
    const unsigned vo = IMG + (g >> 1) * 1024 + ((g & 1) * 32 + c) * 16;
    const unsigned t0 = wn * 32768 + mp * 2 * TIL;
    const unsigned gs = R < 7 ? WS_KV + L * 65536 + t0 : OOB;
#pragma unroll
    for (int u = 0; u < 4; ++u) {
      const unsigned tb = (u >> 1) * TIL;
      const u32x4 kk = pack44(acc[2][u], acc[3][u]), vv = pack44(acc[4 + (u & 1)][u & 2], acc[4 + (u & 1)][(u & 2) + 1]);
      __builtin_amdgcn_raw_buffer_store_b128(kk, rws, ko + (u & 1) * 128 + tb, gs, SC1);
      __builtin_amdgcn_raw_buffer_store_b128(vv, rws, vo + (u & 1) * 256 + tb, gs, SC1);
      sts128(lds0 + LDS_OWN + t0 + tb + ko + (u & 1) * 128, kk);
      sts128(lds0 + LDS_OWN + t0 + tb + vo + (u & 1) * 256, vv);
    }
    lds_inc(lds0 + LDS_CNT + wn * 4);
    if (mp < 2) __builtin_amdgcn_s_setprio(1);
    u32x4 qf[4]; float sd[4];
#pragma unroll
    for (int u = 0; u < 4; ++u) {
      const f32x4 qx = acc[0][u] * QSCALE, qy = acc[1][u] * QSCALE;
      qf[u] = pack44(qx, qy);
      float d = qx[0] * acc[2][u][0];
#pragma unroll
      for (int i = 1; i < 4; ++i) d = __builtin_fmaf(qx[i], acc[2][u][i], d);
#pragma unroll
      for (int i = 0; i < 4; ++i) d = __builtin_fmaf(qy[i], acc[3][u][i], d);
      sd[u] = d;
    }
#pragma unroll
    for (int p = 0; p < 2; ++p) {
#pragma unroll
      for (int i = 0; i < 4; ++i) { auto r = __builtin_amdgcn_permlane16_swap(qf[2 * p][i], qf[2 * p + 1][i], false, false); qf[2 * p][i] = r[0]; qf[2 * p + 1][i] = r[1]; }
      auto r = __builtin_amdgcn_permlane16_swap(__float_as_uint(sd[2 * p]), __float_as_uint(sd[2 * p + 1]), false, false);
      sd[2 * p] = -swapsum(__uint_as_float(r[0]) + __uint_as_float(r[1]));
    }
    qb0 = asb(qf[0]); qb1 = asb(qf[1]); qa0 = asb(qf[2]); qa1 = asb(qf[3]);
    { const float one = l < 32 ? 1.f : 0.f;
      asm volatile("s_nop 1\n\tv_mfma_f32_32x32x2_f32 %0, %2, %3, 0\n\tv_mfma_f32_32x32x2_f32 %1, %2, %4, 0" : "=&v"(na), "=&v"(nb) : "v"(one), "v"(sd[2]), "v"(sd[0])); }
  }
  float la = 0.f, lb = 0.f;
  bf16x8 k0, k1;
  PIN(qa0); PIN(qa1); PIN(qb0); PIN(qb1);
  STAMP(3);
  const __amdgpu_buffer_rsrc_t rwo = mkrs(br ? woB : woA, D_ * D_ * 2);
  const unsigned wov = (l >> 3) * 2048 + (((l & 7) ^ (((l >> 4) & 3) << 1)) << 4) + ns * 128;
#define WOB(gq) ((gq) == 0 ? 131072u : ((gq) & 3) == 3 ? 0u : (gq) * 32768u)
#define DMA_WO(j, ww) dma16(lds0 + WOB((j) >> 2) + ((j) & 3) * 8192 + (ww) * 1024, wov, rwo, ((j) ^ (loc * 8)) * (64u * 2048u) + (ww) * 16384u)
  const unsigned lb16 = l * 16, own = lds0 + LDS_OWN + wn * 32768, prv = R > 0 ? lds0 + wn * 98304 : own;
  const unsigned myflag = WS_FLAG + L * 128, pfoff = WS_FLAG + (L - 16) * 128 + 4 + wn * 4;
#define QK0(S, Q, N) asm volatile("v_mfma_f32_32x32x16_bf16 %0, %1, %2, %3" : "=&v"(S) : "v"(k0), "v"(Q), "v"(N), "v"(k1))
#define QK1(S, Q) asm volatile("v_mfma_f32_32x32x16_bf16 %0, %1, %2, %0" : "+v"(S) : "v"(k1), "v"(Q))
#define PV0(O) asm volatile("v_mfma_f32_32x32x16_bf16 %0, %1, %2, %0" : "+v"(O) : "v"(v0), "v"(p0), "v"(v1))
#define PV0S(O) asm volatile("s_nop 1\n\tv_mfma_f32_32x32x16_bf16 %0, %1, %2, %0" : "+v"(O) : "v"(v0), "v"(p0), "v"(v1))
#define PV1(O) asm volatile("v_mfma_f32_32x32x16_bf16 %0, %1, %2, %0" : "+v"(O) : "v"(v1), "v"(p1))
#define EX(S, i) do { S[i] = __builtin_amdgcn_exp2f(S[i]); } while (0)
#define RDK(a) do { k0 = lds128(a); k1 = lds128((a) + 1024); } while (0)
  unsigned pa = own + (2 * mp + 1) * TIL, pb = pa - TIL, pbp = pb;
  unsigned va = lb16 + pa, vb = lb16 + pb;
  RDK(va);
  asm volatile("s_nop 1" ::: "memory");
  QK0(sa, qa0, na); QK1(sa, qa1);
  RDK(vb);
  asm volatile("s_nop 7\n\ts_nop 1" ::: "memory");
  SBAR();
#define EXP(i) e_[i] = __builtin_amdgcn_exp2f(sx_[i])
#define HALF(SX, SY, NY, LY, OY, QY0, QY1, PY, RD, VX, KNEXT, BAR1, MSK, LSUM, PVF) do { \
    const f32x16 sx_ = SX; float e_[16]; u32x4 pn_; \
    EXP(0); EXP(1); EXP(2); EXP(3); EXP(4); asm volatile("" : "+v"(e_[0]), "+v"(e_[1]), "+v"(e_[2]), "+v"(e_[3]), "+v"(e_[4])); SBAR();     \
    EXP(5); asm volatile("" : "+v"(e_[5]), "+v"(Ls)); SBAR(); \
    float t_ = LY + Ls[0]; unsigned long long bad_ = (BAR1) ? 0ull : __builtin_amdgcn_ballot_w64(sum_bad(t_)); asm volatile("" : "+s"(bad_), "+v"(t_)); SBAR(); \
    pn_[0] = pk2(e_[0], e_[1]); pn_[1] = pk2(e_[2], e_[3]); pn_[2] = pk2(e_[4], e_[5]); EXP(6); \
    asm volatile("" : "+v"(pn_[0]), "+v"(pn_[1]), "+v"(pn_[2]), "+v"(e_[6])); SBAR(); \
    if (!(BAR1) && __builtin_expect(bad_ != 0, 0)) { redo_tile(PY, QY0, QY1, sel, OY, NY, LY, p0, p1, Ls); t_ = LY + Ls[0]; } \
    LY = t_; \
    PVF(OY); SBAR(); \
    EXP(7); EXP(8); pn_[3] = pk2(e_[6], e_[7]); asm volatile("" : "+v"(pn_[3]), "+v"(e_[8])); SBAR(); \
    PV1(OY); SBAR(); if (RD) { v0 = lds128((VX) + IMG); v1 = lds128((VX) + IMG + 1024); VX = KNEXT; } \
    EXP(9); EXP(10); asm volatile("" : "+v"(e_[9]), "+v"(e_[10]), "+v"(VX)); SBAR(); \
    QK0(SY, QY0, NY); SBAR(); EXP(11); EXP(12); EXP(13); asm volatile("" : "+v"(e_[11]), "+v"(e_[12]), "+v"(e_[13])); SBAR(); \
    QK1(SY, QY1); SBAR(); \
    EXP(14); EXP(15); asm volatile("" : "+v"(e_[14]), "+v"(e_[15])); SBAR(); \
    if (BAR1) { if (PFI) WAITV(9); else WAITV(8); lds_inc(lds0 + LDS_CNT + 8); if (mp) lds_wait(lds0 + LDS_CNT + wn * 4, 4); } \
    if (RD) RDK(VX); SBAR(); \
    p0 = pn_; p1[0] = pk2(e_[8], e_[9]); p1[1] = pk2(e_[10], e_[11]); p1[2] = pk2(e_[12], e_[13]); p1[3] = pk2(e_[14], e_[15]); SBAR(); \
    if (MSK) { p0 &= mk0; p1 &= mk1; PIN(p0); PIN(p1); asm volatile("s_nop 0"); } \
    LSUM; SBAR(); } while (0)
#define LMF0 "v_mfma_f32_16x16x32_bf16 %0, %1, %2, 0\n\t"
#define LMF1 "v_mfma_f32_16x16x32_bf16 %0, %1, %3, %0\n\t"
#define LSUM_G() asm volatile(LMF0 "s_nop 0\n\t" LMF1 "s_nop 1" : "=&v"(Ls) : "v"(sel), "v"(p0), "v"(p1))
#define LSUM_A() asm volatile(LMF0 "s_nop 0\n\t" LMF1 "s_nop 0" : "=&v"(Ls) : "v"(sel), "v"(p0), "v"(p1))
#define LSUM_B() asm volatile("v_mfma_f32_16x16x32_bf16 %0, %3, %4, 0\n\ts_mov_b32 %1, %2\n\tv_mfma_f32_16x16x32_bf16 %0, %3, %5, %0\n\ts_add_i32 %2, %2, 0x1000" : "=&v"(Ls), "=&s"(pbp), "+s"(pa) : "v"(sel), "v"(p0), "v"(p1) : "scc")
  unsigned sent = 0;

#define REPF() do {} while (0)
#define SIGNAL() do { WAITV(0); \
    _Pragma("unroll 1") for (unsigned sp = 0; !acked && sp < SPIN_CAP; ++sp) { __builtin_amdgcn_s_sleep(2); acked = __all(!ack_ok || ld32_wait(rws, ackoff) == (mybase ^ ACKX)); } \
    if (lds_inc_rtn(lds0 + LDS_CNT + 24 + wn * 4) == 3 && l == 0) st32(mybase + 1, rws, myflag + 4 + wn * 4); \
    REPF(); \
    sent = 1; if (mp >= 2) __builtin_amdgcn_s_setprio(0); STAMP(13); } while (0)
#define TRYSIG() do { if (__builtin_expect(!sent, 0)) { if ((__builtin_amdgcn_s_getreg(7 | (31 << 11)) & 0x00C0000Fu) == 0) SIGNAL(); } } while (0)
#define PAIR(PAN, PBN, MD, FIRST) do { const unsigned pan = (PAN), pbn = (PBN); \
      HALF(sa, sb, nb, lb, ob, qb0, qb1, pbp, true, va, lb16 + pan, FIRST, (MD) != 0, LSUM_G(), PV0S); \
      HALF(sb, sa, na, la, oa, qa0, qa1, pa | (MD), true, vb, lb16 + pbn, false, (MD) != 0, LSUM_G(), PV0S); \
      pbp = pb | (MD); pa = pan; pb = pbn; } while (0)
#define HOT(N, SIG) do { _Pragma("unroll 1") for (int n = (N); n > 0; --n) {     \
      if (SIG) TRYSIG(); \
      HALF(sa, sb, nb, lb, ob, qb0, qb1, pbp, true, va, va + TIL, false, false, LSUM_A(), PV0); \
      HALF(sb, sa, na, la, oa, qa0, qa1, pa, false, vb, 0, false, false, LSUM_B(), PV0); } } while (0)
  PAIR(own, own, 1u, true);
  HOT(2 * mp, true);
  {
    STAMP(4);
    if (!sent) SIGNAL();
    STAMP(5);
    mk0 = ~mk0; mk1 = ~mk1; PIN(mk0); PIN(mk1);
    if (mp < 2) {
      lds_wait(lds0 + LDS_CNT + 8, 8);
      unsigned h_ = wn; asm volatile("" : "+s"(h_));
      if (mp) {
#pragma unroll
        for (int j = 0; j < 15; ++j) DMA_WO(j >> 2, h_ * 4 + (j & 3));
        if (!h_) DMA_WO(3, 3);
      } else if (R > 0) {
#pragma unroll 1
        for (unsigned sp = 0; sp < SPIN_CAP; ++sp) {
          if (rfl(ld32_wait(rws, pfoff)) == pbase + 1) break;
          __builtin_amdgcn_s_sleep(KVS);
        }
        STAMP(14);
#pragma unroll
        for (int j = 0; j < 8; ++j) dma64_sc1(lds0 + h_ * 98304 + j * TIL, lb16, rws, WS_KV + (L - 16) * 65536 + h_ * 32768 + j * TIL);
        WAITV(12);
        lds_inc(lds0 + LDS_CNT + 12 + h_ * 4);
      }
    }
    STAMP(6);
    if (R > 0) lds_wait(lds0 + LDS_CNT + 12 + wn * 4, mp < 2 ? 1 : 2);
    STAMP(7);
    pb = prv + (2 * mp + 1) * TIL; vb = lb16 + pb;
    RDK(vb);
    SBAR();
  }
  PAIR(prv + (2 * mp + 1) * TIL, prv + 2 * mp * TIL, 0u, false);
  lds_inc(lds0 + LDS_CNT + 20);
  if (R > 0) {
    if (mp == 0) { WAITV(0); lds_inc(lds0 + LDS_CNT + 12 + wn * 4); }
    PAIR(prv + (2 * mp + 2) * TIL, prv + (2 * mp + 2) * TIL, 2u, false);
    if (mp == 1) lds_wait(lds0 + LDS_CNT + 12 + wn * 4, 2);
    HOT(6 - 2 * mp, false);
    asm volatile("s_nop 7" : "+v"(Ls) :: "memory");
    if (__builtin_expect(__any(sum_bad(lb + Ls[0])), 0)) redo_tile(pbp, qb0, qb1, sel, ob, nb, lb, p0, p1, Ls);
    lb += Ls[0];
    PV0S(ob); PV1(ob);
  } else {
    asm volatile("s_nop 7" : "+v"(Ls), "+v"(sa) :: "memory");
  }
  __builtin_amdgcn_s_setprio(0);
  STAMP(8);
  { const float ia = __builtin_amdgcn_rcpf(la), ib = __builtin_amdgcn_rcpf(lb);
    oa *= ia;
    const unsigned hh = hp * 2 + wn, T0 = br * 64 + R * 8 + 2 * mp;
    const unsigned ol = ((l >> 4) & 1) * 1024 + ((l >> 5) * 16 + (l & 15)) * 16;
    __builtin_amdgcn_raw_buffer_store_b128(pack8(oa, 0), rws, ol, WS_O + (T0 * 32 + 32 + hh) * IMG, SC1);
    __builtin_amdgcn_raw_buffer_store_b128(pack8(oa, 1), rws, ol + 512, WS_O + (T0 * 32 + 32 + hh) * IMG, SC1);
    SBAR();
    asm volatile("s_nop 7" : "+v"(ob) :: "memory");
    ob *= ib;
    __builtin_amdgcn_raw_buffer_store_b128(pack8(ob, 0), rws, ol, WS_O + (T0 * 32 + hh) * IMG, SC1);
    __builtin_amdgcn_raw_buffer_store_b128(pack8(ob, 1), rws, ol + 512, WS_O + (T0 * 32 + hh) * IMG, SC1); }
  WAITV(0);
  STAMP(9);
  asm volatile("s_barrier" ::: "memory");
  if (tid == 64) st32(mybase + 2, rws, myflag);
  dma4(lds0 + LDS_WOX + 1024, l < 32 ? (l >> 4) * 65536 + (l & 15) * 128 : OOB, rws, WS_O + ((br * 64 + R * 8 + 2 * mp) * 32 + hp * 2 + wn) * IMG);
  if (w == 7) DMA_WO(3, 7);
  const unsigned fo = l < 32 ? WS_FLAG + (G * 16 + (l >> 1)) * 128 : OOB;
#pragma unroll
  for (int j = 4; j < 16; ++j) DMA_WO(j, w);
  if (w == POLLW) {
    unsigned f;
#pragma unroll 1
    for (unsigned sp = 0; sp < SPIN_CAP; ++sp) {
      f = ld32_wait(rws, fo);
      if (__all((l >> 4) != loc || f == obase + 2)) break;
      __builtin_amdgcn_s_sleep(POLLS);
    }
    *(volatile LAS unsigned*)(size_t)(lds0 + LDS_WOX) = __all(l >= 32 || f == obase + 2);
  }
  WAIT_BAR(0);
  STAMP(10);
  const unsigned T = G * 8 + w;
  const unsigned obs = WS_O + T * 32 * IMG;
  const unsigned wa2 = lds0 + (16 * (g >> 1) + 4 * (g & 1) + q4) * 128 + ((2 * (g & 1) + (q4 >> 1)) << 5) + (l & 3) * 8;
  f32x4 oc[4][2] = {};
  u32x4 of[16];
  s16x4 wl[2][4], wh[2][4];
#pragma unroll
  for (int i = 0; i < 16; ++i) {
    of[i] = __builtin_amdgcn_raw_buffer_load_b128(rws, l * 16 + (i & 3) * 1024, obs + loc * 32768 + (i >> 2) * 4096, SC1);
    if (i == FILL0 - 1 || i == FILL1 - 1) { SBAR(); __builtin_amdgcn_s_sleep(FILLS); SBAR(); }
  }
  const unsigned allrdy = rfl(*(volatile LAS unsigned*)(size_t)(lds0 + LDS_WOX));
#pragma unroll
  for (int nt = 0; nt < 4; ++nt) { wl[0][nt] = ldstr((wa2 ^ (nt << 5)) + WOB(0)); wh[0][nt] = ldstr((wa2 ^ (nt << 5)) + WOB(0) + 1024); }
  _Pragma("unroll") for (int i_ = 0; i_ < 8; ++i_) PIN(oc[i_ >> 1][i_ & 1]);
  asm volatile("s_nop 1" ::: "memory");
  SBAR();
#pragma unroll 1
  for (int gq = 0; gq < 4; ++gq) {
    const unsigned wb = wa2 + WOB(gq), wbn = wa2 + WOB(gq + 1);
    if (__builtin_expect(gq == 1 && !allrdy, 0)) {
      if (w == POLLW) {
#pragma unroll 1
        for (unsigned sp = 0; sp < SPIN_CAP; ++sp) { if (__all(l >= 32 || ld32_wait(rws, fo) == obase + 2)) break; __builtin_amdgcn_s_sleep(POLLS); }
      }
      asm volatile("s_barrier" ::: "memory");
    }
    const unsigned on = gq < 3 ? obs + ((gq + 1) ^ (loc * 2)) * 16384 : OOB;
    if (gq == 0) __builtin_amdgcn_s_setprio(3); else if (gq == 1) __builtin_amdgcn_s_setprio(2); else if (gq == 2) __builtin_amdgcn_s_setprio(1); else __builtin_amdgcn_s_setprio(0);
    sfor<0, 8>([&](auto hi) AI { constexpr int h_ = decltype(hi)::v, cur = h_ & 1, nxt = cur ^ 1;
      sfor<0, 8>([&](auto mi) AI { constexpr int m_ = decltype(mi)::v, nt = m_ >> 1, tt = m_ & 1;
        { const bf16x8 wf = cat(wl[cur][nt], wh[cur][nt]);
          if constexpr (m_ == 0) {
            const bf16x8 w3 = cat(wl[cur][3], wh[cur][3]);
            asm volatile("v_mfma_f32_16x16x32_bf16 %0, %1, %2, %0" : "+v"(oc[0][0]) : "v"(wf), "v"(of[2 * h_]), "v"(w3), "v"(of[2 * h_ + 1]));
          } else MFA(oc[nt][tt], wf, of[2 * h_ + tt]); }
        sfor<0, 8>([&](auto ri) AI { constexpr int r_ = decltype(ri)::v, rn = r_ >> 1;
          if constexpr (G2R(r_) == m_) {
            const unsigned wq_ = ((h_ < 7 ? wb : wbn) ^ (rn << 5)) + ((h_ + 1) & 7) * 4096;
            if constexpr ((r_ & 1) == 0) wl[nxt][rn] = ldstr(wq_); else wh[nxt][rn] = ldstr(wq_ + 1024); } });
        if constexpr (m_ >= 6) of[2 * h_ + tt] = __builtin_amdgcn_raw_buffer_load_b128(rws, l * 16 + ((2 * h_ + tt) & 3) * 1024, on + ((2 * h_ + tt) >> 2) * 4096, SC1);
        SBAR(); }); });
  }
  asm volatile("s_nop 7" ::: "memory");
  _Pragma("unroll") for (int i_ = 0; i_ < 8; ++i_) PIN(oc[i_ >> 1][i_ & 1]);
  STAMP(11);
  const __amdgpu_buffer_rsrc_t rout = mkrs(out, S_ * 2 * D_ * 2);
  const unsigned orow = ((R * 8 + w) * 32 + c) * 4096 + br * 2048 + ns * 128 + (g & 1) * 32 + (g >> 1) * 16;
  const unsigned orow2 = orow - (c & 8) * 4096 + (c & 8) * 8;
#pragma unroll
  for (int tt = 0; tt < 2; ++tt) {
    u32x4 v[2], va_, vb_;
#pragma unroll
    for (int np = 0; np < 2; ++np) {
      const f32x4 &a = oc[2 * np][tt], &b = oc[2 * np + 1][tt];
      auto r0 = __builtin_amdgcn_permlane16_swap(pk2(a[0], a[1]), pk2(b[0], b[1]), false, false);
      auto r1 = __builtin_amdgcn_permlane16_swap(pk2(a[2], a[3]), pk2(b[2], b[3]), false, false);
      v[np] = u32x4{r0[0], r1[0], r0[1], r1[1]};
    }
#pragma unroll
    for (int i = 0; i < 4; ++i) {
      const unsigned y = c < 8 ? v[1][i] : v[0][i];
      const unsigned r = (unsigned)__builtin_amdgcn_update_dpp(0, (int)y, 0x128, 0xf, 0xf, false);
      va_[i] = c < 8 ? v[0][i] : r; vb_[i] = c < 8 ? r : v[1][i];
    }
    __builtin_amdgcn_raw_buffer_store_b128(va_, rout, orow2 + tt * 65536, 0, OUTPOL);
    __builtin_amdgcn_raw_buffer_store_b128(vb_, rout, orow2 + tt * 65536 + 32768, 0, OUTPOL);
  }
}

extern "C" void kernel_launch(void* const* d_in, const int* in_sizes, int n_in, void* d_out, int out_size, void* d_ws, size_t ws_size,
                              hipStream_t stream) {
  const size_t wsoff = (ws_size - WS_END) & ~(size_t)0xfffff;
  k_fused<<<256, 512, 0, stream>>>(d_in[0], d_in[1], d_in[2], d_in[3], d_in[4], d_in[5], (char*)d_ws + wsoff, d_out);
}
```
